```python
import torch

def _softmax(x, axis=-1):
    return torch.softmax(x, dim=axis)
def _gelu(x):
    return torch.nn.functional.gelu(x, approximate="tanh")
def _silu(x):
    return torch.nn.functional.silu(x)
def _sigmoid(x):
    return torch.sigmoid(x)
def _layernorm(x, axis=-1, eps=1e-5):
    m = x.mean(dim=axis, keepdim=True)
    v = x.var(dim=axis, keepdim=True, unbiased=False)
    return (x - m) / torch.sqrt(v + eps)
def _scatter_add(tgt, idx, src, axis=0):
    return tgt.clone().index_add_(axis, idx, src)
def _segment_sum(src, seg, n):
    out = torch.zeros((n,) + src.shape[1:], dtype=src.dtype)
    return out.index_add_(0, seg, src)
def _pad(x, pw):
    flat = tuple(v for pair in reversed(pw) for v in pair)
    return torch.nn.functional.pad(x, flat)

kernel_name = 'parallel_sum__sliding_block_cached__dense_activation_relu__decode'

def setup_inputs():

    _w = (lambda a: (lambda t: t.to(torch.bfloat16) if t.is_floating_point() else t)(torch.as_tensor(a)))
    def _A_setup():
        B, Sq, D = 1, 1, 640
        Hq, Hkv, Sk, Dh = 32, 32, 32768, 192
        assert Hq % Hkv == 0, (Hq, Hkv)
        return {
            "x": _w((lambda *s: torch.randn(*s, dtype=torch.bfloat16) * 0.05)(B, Sq, D)),
            "k_cache": _w((lambda *s: torch.randn(*s, dtype=torch.bfloat16) * 3.2)(B, Hkv, Sk, Dh)),
            "v_cache": _w((lambda *s: torch.randn(*s, dtype=torch.bfloat16) * 1.6)(B, Hkv, Sk, Dh)),
            "wq": _w((lambda *s: torch.randn(*s, dtype=torch.bfloat16) * 3.2)(D, Hq * Dh) * (D ** -0.5)),
            "wo": _w((lambda *s: torch.randn(*s, dtype=torch.bfloat16) * 1.6)(Hq * Dh, D) * ((Hq * Dh) ** -0.5)),
        }
    _A_in = _A_setup()

    def _B_setup():
        B, S, D, Dff = 1, 1, 640, 1536
        return {
            "x": _w((lambda *s: torch.randn(*s, dtype=torch.bfloat16) * 0.05)(B, S, D)),
            "w1": _w((lambda *s: torch.randn(*s, dtype=torch.bfloat16) * 0.05)(D, Dff)),
            "w2": _w((lambda *s: torch.randn(*s, dtype=torch.bfloat16) * 0.05)(Dff, D)),
        }
    _B_in = _B_setup()

    return {
        "A_x": _A_in["x"],
        "A_k_cache": _A_in["k_cache"],
        "A_v_cache": _A_in["v_cache"],
        "A_wq": _A_in["wq"],
        "A_wo": _A_in["wo"],
        "B_x": _B_in["x"],
        "B_w1": _B_in["w1"],
        "B_w2": _B_in["w2"],
    }

def reference(A_x, A_k_cache, A_v_cache, A_wq, A_wo, B_x, B_w1, B_w2):
    def _A_ref(x, k_cache, v_cache, wq, wo):
        B, Sq, D = x.shape
        Hq, Dh = 32, 192
        Hkv, Sk = k_cache.shape[1], k_cache.shape[2]
        W = min(4096, max(256, Sk // 4))
        rep = Hq // Hkv
        q = (lambda x, *d: x.permute(*d))((x @ wq).reshape(B, Sq, Hq, Dh), 0, 2, 1, 3)
        k = (lambda x, n, axis: torch.repeat_interleave(x, n, dim=axis))(k_cache[:, :, Sk - W:, :], rep, axis=1)
        v = (lambda x, n, axis: torch.repeat_interleave(x, n, dim=axis))(v_cache[:, :, Sk - W:, :], rep, axis=1)
        scores = torch.einsum("bhqd,bhkd->bhqk", q, k) * (Dh ** -0.5)
        p = _softmax(scores, axis=-1)
        o = torch.einsum("bhqk,bhkd->bhqd", p, v)
        return (lambda x, *d: x.permute(*d))(o, 0, 2, 1, 3).reshape(B, Sq, Hq * Dh) @ wo

    def _B_ref(x, w1, w2):
        h = torch.einsum("bsd,df->bsf", x, w1)
        zero = torch.zeros_like(h)
        h = torch.maximum(h, zero)
        return torch.einsum("bsf,fd->bsd", h, w2)

    x = A_x
    xn = _layernorm(x, axis=-1)
    return x + _A_ref(xn, A_k_cache, A_v_cache, A_wq, A_wo) + _B_ref(xn, B_w1, B_w2)
```

```cpp
#include <hip/hip_runtime.h>
#include <hip/hip_bf16.h>
#include <cstdio>


namespace {

typedef unsigned v4u __attribute__((ext_vector_type(4)));
typedef unsigned v2u __attribute__((ext_vector_type(2)));
typedef float f32x4 __attribute__((ext_vector_type(4)));
typedef float f32x2 __attribute__((ext_vector_type(2)));
typedef __bf16 bfx8 __attribute__((ext_vector_type(8)));
typedef __bf16 bfx2 __attribute__((ext_vector_type(2)));
typedef __amdgpu_buffer_rsrc_t rsrc_t;

constexpr int AUX_NT = 2;
constexpr int AUX_SC1 = 16;
constexpr unsigned OOB = 0x80000000u;
constexpr unsigned SPIN_CAP = 1u << 16;

constexpr unsigned WS_P1 = 4096;
constexpr unsigned WS_P2 = WS_P1 + 256u * 256u * 8u;
constexpr unsigned WS_BYTES = WS_P2 + 8u * 32u * 160u * 8u;

constexpr int SLOT_M = 226, SLOT_L = 227;

__device__ __forceinline__ rsrc_t mk(const void* p, unsigned bytes) {
  return __builtin_amdgcn_make_buffer_rsrc((void*)p, (short)0, (int)bytes, 0x00020000);
}
__device__ __forceinline__ float rbf(float f) { return (float)(__bf16)f; }
__device__ __forceinline__ float bflo(unsigned u) { return __builtin_bit_cast(float, u << 16); }
__device__ __forceinline__ float bfhi(unsigned u) { return __builtin_bit_cast(float, u & 0xFFFF0000u); }
__device__ __forceinline__ unsigned pk2(float a, float b) {
  f32x2 v = {a, b};
  return __builtin_bit_cast(unsigned, __builtin_convertvector(v, bfx2));
}
__device__ __forceinline__ f32x4 mfma(v4u a, v4u b, f32x4 c) {
  return __builtin_amdgcn_mfma_f32_16x16x32_bf16(__builtin_bit_cast(bfx8, a), __builtin_bit_cast(bfx8, b), c, 0, 0, 0);
}
__device__ __forceinline__ float dot2(unsigned a, unsigned b, float c) {
  return __builtin_amdgcn_fdot2_f32_bf16(__builtin_bit_cast(bfx2, a), __builtin_bit_cast(bfx2, b), c, false);
}
template <int CTRL> __device__ __forceinline__ float dpp(float v) {
  return __builtin_bit_cast(float, __builtin_amdgcn_mov_dpp(__builtin_bit_cast(int, v), CTRL, 0xF, 0xF, true));
}
__device__ __forceinline__ float sum8(float v) {
  v += dpp<0xB1>(v);
  v += dpp<0x4E>(v);
  v += dpp<0x141>(v);
  return v;
}
__device__ __forceinline__ float sum_hi(float v) {
  v += dpp<0x128>(v);
  v += __shfl_xor(v, 16);
  v += __shfl_xor(v, 32);
  return v;
}
__device__ __forceinline__ float max_hi(float v) {
  v = fmaxf(v, dpp<0x128>(v));
  v = fmaxf(v, __shfl_xor(v, 16));
  v = fmaxf(v, __shfl_xor(v, 32));
  return v;
}
__device__ __forceinline__ v4u bfrag(unsigned dup, const unsigned (&mask)[4]) {
  v4u b;
  b.x = dup & mask[0];
  b.y = dup & mask[1];
  b.z = dup & mask[2];
  b.w = dup & mask[3];
  return b;
}
#define SCHED_FENCE() __builtin_amdgcn_sched_barrier(0)

__global__ void __launch_bounds__(512, 2)
fused_decode_kernel(const unsigned short* __restrict__ x, const unsigned short* __restrict__ kc,
                    const unsigned short* __restrict__ vc, const unsigned short* __restrict__ wq,
                    const unsigned short* __restrict__ wo, const unsigned short* __restrict__ w1,
                    const unsigned short* __restrict__ w2, unsigned short* __restrict__ out,
                    unsigned char* __restrict__ ws) {
  __shared__ __attribute__((aligned(16))) float red[16 * 256];
  __shared__ __attribute__((aligned(16))) float red3[8 * 256];
  __shared__ __attribute__((aligned(16))) unsigned short qs[192];
  __shared__ __attribute__((aligned(16))) unsigned short ob[256];
  __shared__ __attribute__((aligned(16))) float mls[16];

  const int tid = threadIdx.x;
  const int lane = tid & 63;
  const int w = __builtin_amdgcn_readfirstlane(tid >> 6);
  const int lg = lane >> 3, lc = lane & 7;
  const int col = lane & 15, rq = lane >> 4;
  const int b = blockIdx.x;
  const int h = 4 * (b & 7) + (b >> 6);
  const int s = (b >> 3) & 7;
  const int wg = h * 8 + s;

  const rsrc_t rsX = mk(x, 1280);
  const rsrc_t rsWs = mk(ws, WS_BYTES);
  const size_t kvoff = ((size_t)h * 32768 + 28672 + 512 * s + 64 * w) * 192;
  const rsrc_t rsK = mk(kc + kvoff, 64 * 384);
  const rsrc_t rsV = mk(vc + kvoff, 64 * 384);
  const rsrc_t rsWq = mk(wq + (size_t)(80 * w) * 6144 + 192 * h, 79 * 12288 + 384);
  const rsrc_t rsW1 = mk(w1 + (size_t)(80 * s + 10 * w) * 1536 + 48 * h, 9 * 3072 + 96);
  const rsrc_t rsWo = mk(wo + (size_t)(192 * h + 24 * w) * 640 + 80 * s, 23 * 1280 + 160);
  const rsrc_t rsW2 = mk(w2 + (size_t)(48 * h + 6 * w) * 640 + 80 * s, 5 * 1280 + 160);

  const unsigned epoch = __builtin_amdgcn_raw_buffer_load_b32(rsWs, 0, 0, AUX_SC1);
  const v4u xs0 = __builtin_amdgcn_raw_buffer_load_b128(rsX, 20 * lane, 0, 0);
  const unsigned xs1 = __builtin_amdgcn_raw_buffer_load_b32(rsX, 20 * lane + 16, 0, 0);
  const v4u xo0 = __builtin_amdgcn_raw_buffer_load_b128(rsX, 160 * w + 20 * lg, 0, 0);
  const unsigned xo1 = __builtin_amdgcn_raw_buffer_load_b32(rsX, 160 * w + 20 * lg + 16, 0, 0);
  const unsigned xw = __builtin_amdgcn_raw_buffer_load_b32(rsX, lg < 5 ? (unsigned)(160 * s + 20 * w + 4 * lg) : OOB, 0, 0);

  const unsigned p1_off = tid < 256 ? WS_P1 + (unsigned)(wg * 256 + tid) * 8u : OOB;
  const int r3 = 4 * (tid >> 6) + (tid & 3), c3 = (tid >> 2) & 15;
  const unsigned p2_off =
      (tid < 256 && r3 < 10) ? WS_P2 + (unsigned)((s * 32 + h) * 160 + (8 * r3 + (c3 & 7)) * 2 + (c3 >> 3)) * 8u : OOB;
  {
    const v2u z = {0u, 0u};
    __builtin_amdgcn_raw_buffer_store_b64(z, rsWs, p1_off, 0, AUX_SC1);
    __builtin_amdgcn_raw_buffer_store_b64(z, rsWs, p2_off, 0, AUX_SC1);
  }

  v4u w1v[2];
  {
    const unsigned vo = (lg < 5 && lc < 6) ? (unsigned)(lg * 6144 + lc * 16) : OOB;
    w1v[0] = __builtin_amdgcn_raw_buffer_load_b128(rsW1, vo, 0, 0);
    w1v[1] = __builtin_amdgcn_raw_buffer_load_b128(rsW1, vo + 3072, 0, 0);
  }
  v4u wqv[10][3];
  {
    const unsigned vo = (unsigned)(lg * 122880 + lc * 16);
#pragma unroll
    for (int g = 0; g < 10; ++g)
#pragma unroll
      for (int j = 0; j < 3; ++j) wqv[g][j] = __builtin_amdgcn_raw_buffer_load_b128(rsWq, vo + j * 128, g * 12288, 0);
  }
  const unsigned vkv = (unsigned)(lg * 384 + lc * 16);
  v4u kv[8][3], vv[8][3];
#pragma unroll
  for (int g = 0; g < 6; ++g)
#pragma unroll
    for (int j = 0; j < 3; ++j) kv[g][j] = __builtin_amdgcn_raw_buffer_load_b128(rsK, vkv + j * 128, g * 3072, AUX_NT);
  SCHED_FENCE();

  unsigned mask[4];
  {
    const unsigned mm = (lc & 1) ? 0xFFFF0000u : 0x0000FFFFu;
#pragma unroll
    for (int i = 0; i < 4; ++i) mask[i] = ((lc >> 1) == i) ? mm : 0u;
  }
  float mb, sd;
  {
    float e[10];
    e[0] = bflo(xs0.x); e[1] = bfhi(xs0.x); e[2] = bflo(xs0.y); e[3] = bfhi(xs0.y);
    e[4] = bflo(xs0.z); e[5] = bfhi(xs0.z); e[6] = bflo(xs0.w); e[7] = bfhi(xs0.w);
    e[8] = bflo(xs1);   e[9] = bfhi(xs1);
    float t = 0.f;
#pragma unroll
    for (int i = 0; i < 10; ++i) t += e[i];
    t = sum_hi(sum8(t));
    const float mean = t * (1.0f / 640.0f);
    float q2 = 0.f;
#pragma unroll
    for (int i = 0; i < 10; ++i) { const float d = e[i] - mean; q2 += d * d; }
    q2 = sum_hi(sum8(q2));
    const float var = rbf(q2 * (1.0f / 640.0f));
    mb = rbf(mean);
    sd = rbf(sqrtf(rbf(var + 1e-5f)));
  }
  auto xnorm = [&](float v) { return rbf(v - mb) / sd; };

  f32x4 hacc = {0.f, 0.f, 0.f, 0.f};
  {
    const float a0 = xnorm(bflo(xw)), a1 = xnorm(bfhi(xw));
    hacc = mfma(w1v[0], bfrag(pk2(a0, a0), mask), hacc);
    hacc = mfma(w1v[1], bfrag(pk2(a1, a1), mask), hacc);
  }
  f32x4 qacc[3] = {{0.f, 0.f, 0.f, 0.f}, {0.f, 0.f, 0.f, 0.f}, {0.f, 0.f, 0.f, 0.f}};
  {
    float e[10];
    e[0] = bflo(xo0.x); e[1] = bfhi(xo0.x); e[2] = bflo(xo0.y); e[3] = bfhi(xo0.y);
    e[4] = bflo(xo0.z); e[5] = bfhi(xo0.z); e[6] = bflo(xo0.w); e[7] = bfhi(xo0.w);
    e[8] = bflo(xo1);   e[9] = bfhi(xo1);
    unsigned dup[10];
#pragma unroll
    for (int g = 0; g < 10; ++g) { const float a = xnorm(e[g]); dup[g] = pk2(a, a); }
    SCHED_FENCE();
#pragma unroll
    for (int g = 0; g < 10; ++g) {
      const v4u bf = bfrag(dup[g], mask);
#pragma unroll
      for (int j = 0; j < 3; ++j) qacc[j] = mfma(wqv[g][j], bf, qacc[j]);
#pragma unroll
      for (int j = 0; j < 3; ++j) {
        if (g < 2) kv[6 + g][j] = __builtin_amdgcn_raw_buffer_load_b128(rsK, vkv + j * 128, (6 + g) * 3072, AUX_NT);
        else vv[g - 2][j] = __builtin_amdgcn_raw_buffer_load_b128(rsV, vkv + j * 128, (g - 2) * 3072, AUX_NT);
      }
      SCHED_FENCE();
    }
  }
  const bool dvalid = (rq >> 1) == (col >> 3);
  float* const myred = red + (w * 2 + (rq >> 1)) * 256 + ((rq & 1) * 8 + (col & 7)) * 4;
  if (dvalid) {
#pragma unroll
    for (int j = 0; j < 3; ++j) *(f32x4*)(myred + j * 64) = qacc[j];
    *(f32x4*)(myred + 192) = hacc;
  }
  __syncthreads();
  const int dmap = (tid & 0xE0) + 8 * (tid & 3) + ((tid >> 2) & 7);
  float hidp = 0.f;
  if (tid < 256) {
    float t = 0.f;
#pragma unroll
    for (int i = 0; i < 16; ++i) t += red[i * 256 + tid];
    if (tid < 192) qs[dmap] = __builtin_bit_cast(unsigned short, (__bf16)t);
    hidp = t;
  }
  __syncthreads();
  v4u qf[3];
#pragma unroll
  for (int j = 0; j < 3; ++j) qf[j] = *(const v4u*)(qs + 64 * j + 8 * lc);

  v4u gv[8];
  const unsigned vo3 = col < 10 ? (unsigned)(rq * 7680 + col * 16) : OOB;
  const unsigned vo4 = (col < 10 && rq < 3) ? (unsigned)(rq * 2560 + col * 16) : OOB;
  float sc[8];
#pragma unroll
  for (int g = 0; g < 8; ++g) {
    float a = 0.f;
#pragma unroll
    for (int j = 0; j < 3; ++j) {
      a = dot2(kv[g][j].x, qf[j].x, a);
      a = dot2(kv[g][j].y, qf[j].y, a);
      a = dot2(kv[g][j].z, qf[j].z, a);
      a = dot2(kv[g][j].w, qf[j].w, a);
    }
    a = sum8(a);
    sc[g] = rbf(rbf(a) * 0.07216878364870322f);
    if (g < 6) gv[g] = __builtin_amdgcn_raw_buffer_load_b128(rsWo, vo3, g * 1280, 0);
    else gv[g] = __builtin_amdgcn_raw_buffer_load_b128(rsW2, vo4, (g - 6) * 1280, 0);
    SCHED_FENCE();
  }
  {
    float m = fmaxf(fmaxf(fmaxf(sc[0], sc[1]), fmaxf(sc[2], sc[3])), fmaxf(fmaxf(sc[4], sc[5]), fmaxf(sc[6], sc[7])));
    m = max_hi(m);
    if (lane == 0) mls[w] = m;
  }
  __syncthreads();
  float M;
  {
    const f32x4 m0 = *(const f32x4*)mls, m1 = *(const f32x4*)(mls + 4);
    M = fmaxf(fmaxf(fmaxf(m0.x, m0.y), fmaxf(m0.z, m0.w)), fmaxf(fmaxf(m1.x, m1.y), fmaxf(m1.z, m1.w)));
  }
  unsigned pd[8];
  {
    float l = 0.f;
#pragma unroll
    for (int g = 0; g < 8; ++g) {
      const float p = __builtin_amdgcn_exp2f((sc[g] - M) * 1.4426950408889634f);
      l += p;
      pd[g] = pk2(p, p);
    }
    l = sum_hi(l);
    if (lane == 0) mls[8 + w] = l;
  }
  SCHED_FENCE();

  f32x4 oacc[3] = {{0.f, 0.f, 0.f, 0.f}, {0.f, 0.f, 0.f, 0.f}, {0.f, 0.f, 0.f, 0.f}};
#pragma unroll
  for (int g = 0; g < 8; ++g) {
    const v4u bf = bfrag(pd[g], mask);
#pragma unroll
    for (int j = 0; j < 3; ++j) oacc[j] = mfma(vv[g][j], bf, oacc[j]);
    SCHED_FENCE();
  }
  if (dvalid) {
#pragma unroll
    for (int j = 0; j < 3; ++j) *(f32x4*)(myred + j * 64) = oacc[j];
  }
  __syncthreads();

  const unsigned tag = (epoch + 1u) | 0x80000000u;
  if (w < 4) {
    float val;
    if (w < 3) {
      float t = 0.f;
#pragma unroll
      for (int i = 0; i < 16; ++i) t += red[i * 256 + tid];
      val = t;
    } else {
      const f32x4 l0 = *(const f32x4*)(mls + 8), l1 = *(const f32x4*)(mls + 12);
      const float L = ((l0.x + l0.y) + (l0.z + l0.w)) + ((l1.x + l1.y) + (l1.z + l1.w));
      val = tid == SLOT_M ? M : (tid == SLOT_L ? L : hidp);
    }
    {
      const v2u g = {__builtin_bit_cast(unsigned, val), tag};
      __builtin_amdgcn_raw_buffer_store_b64(g, rsWs, p1_off, 0, AUX_SC1);
    }
    const unsigned gbase = WS_P1 + (unsigned)(h * 8 * 256 + tid) * 8u;
    const unsigned mlo = WS_P1 + (unsigned)((h * 8 + ((lane >> 1) & 7)) * 256 + SLOT_M + (lane & 1)) * 8u;
    v2u g[8], gm;
    for (unsigned spins = 0; spins < SPIN_CAP; ++spins) {
      asm volatile("" ::: "memory");
#pragma unroll
      for (int i = 0; i < 8; ++i) g[i] = __builtin_amdgcn_raw_buffer_load_b64(rsWs, gbase, i * 2048, AUX_SC1);
      gm = __builtin_amdgcn_raw_buffer_load_b64(rsWs, mlo, 0, AUX_SC1);
      bool ok = gm.y == tag;
#pragma unroll
      for (int i = 0; i < 8; ++i) ok &= g[i].y == tag;
      if (__all(ok)) break;
      __builtin_amdgcn_s_sleep(1);
    }
    if (w < 3) {
      float Ms[8], Ls[8];
#pragma unroll
      for (int i = 0; i < 8; ++i) {
        Ms[i] = __builtin_bit_cast(float, __builtin_amdgcn_readlane((int)gm.x, 2 * i));
        Ls[i] = __builtin_bit_cast(float, __builtin_amdgcn_readlane((int)gm.x, 2 * i + 1));
      }
      const float Mg = fmaxf(fmaxf(fmaxf(Ms[0], Ms[1]), fmaxf(Ms[2], Ms[3])), fmaxf(fmaxf(Ms[4], Ms[5]), fmaxf(Ms[6], Ms[7])));
      float num = 0.f, den = 0.f;
#pragma unroll
      for (int i = 0; i < 8; ++i) {
        const float f = __builtin_amdgcn_exp2f((Ms[i] - Mg) * 1.4426950408889634f);
        num += f * __builtin_bit_cast(float, g[i].x);
        den += f * Ls[i];
      }
      ob[dmap] = __builtin_bit_cast(unsigned short, (__bf16)(num / den));
    } else {
      float t = 0.f;
#pragma unroll
      for (int i = 0; i < 8; ++i) t += __builtin_bit_cast(float, g[i].x);
      t = fmaxf(rbf(t), 0.f);
      if (dmap < 240) ob[dmap] = __builtin_bit_cast(unsigned short, (__bf16)t);
    }
  }
  __syncthreads();

  {
    const unsigned* obw = (const unsigned*)ob;
    unsigned o0 = obw[12 * w + 3 * rq], o1 = obw[12 * w + 3 * rq + 1], o2 = obw[12 * w + 3 * rq + 2];
    unsigned hh = obw[96 + 3 * w + (rq < 3 ? rq : 0)];
    if (col >= 8) { o0 = 0u; o1 = 0u; o2 = 0u; }
    if (col < 8 || rq == 3) hh = 0u;
    f32x4 acc = {0.f, 0.f, 0.f, 0.f};
    acc = mfma(gv[0], bfrag(__builtin_amdgcn_perm(o0, o0, 0x01000100u), mask), acc);
    acc = mfma(gv[1], bfrag(__builtin_amdgcn_perm(o0, o0, 0x03020302u), mask), acc);
    acc = mfma(gv[2], bfrag(__builtin_amdgcn_perm(o1, o1, 0x01000100u), mask), acc);
    acc = mfma(gv[3], bfrag(__builtin_amdgcn_perm(o1, o1, 0x03020302u), mask), acc);
    acc = mfma(gv[4], bfrag(__builtin_amdgcn_perm(o2, o2, 0x01000100u), mask), acc);
    acc = mfma(gv[5], bfrag(__builtin_amdgcn_perm(o2, o2, 0x03020302u), mask), acc);
    acc = mfma(gv[6], bfrag(__builtin_amdgcn_perm(hh, hh, 0x01000100u), mask), acc);
    acc = mfma(gv[7], bfrag(__builtin_amdgcn_perm(hh, hh, 0x03020302u), mask), acc);
    *(f32x4*)(red3 + w * 256 + lane * 4) = acc;
  }
  __syncthreads();

  if (w < 3) {
    float t = 0.f;
#pragma unroll
    for (int i = 0; i < 8; ++i) t += red3[i * 256 + tid];
    const v2u g = {__builtin_bit_cast(unsigned, t), tag};
    __builtin_amdgcn_raw_buffer_store_b64(g, rsWs, p2_off, 0, AUX_SC1);
  } else if (w == 3 && h < 20) {
    const int n0 = 4 * h + 2 * (lane & 1);
    const unsigned xin = __builtin_amdgcn_raw_buffer_load_b32(rsX, (unsigned)(80 * s + n0) * 2u, 0, 0);
    const unsigned gbase = WS_P2 + (unsigned)((s * 32 + (lane >> 1)) * 160 + n0 * 2) * 8u;
    v2u g[4];
    for (unsigned spins = 0; spins < SPIN_CAP; ++spins) {
      asm volatile("" ::: "memory");
#pragma unroll
      for (int i = 0; i < 4; ++i) g[i] = __builtin_amdgcn_raw_buffer_load_b64(rsWs, gbase + i * 8, 0, AUX_SC1);
      bool ok = true;
#pragma unroll
      for (int i = 0; i < 4; ++i) ok &= g[i].y == tag;
      if (__all(ok)) break;
      __builtin_amdgcn_s_sleep(1);
    }
    float v[4];
#pragma unroll
    for (int i = 0; i < 4; ++i) {
      float t = __builtin_bit_cast(float, g[i].x);
      t += dpp<0x4E>(t);
      t += __shfl_xor(t, 4);
      t = sum_hi(t);
      v[i] = t;
    }
    if (lane < 2) {
      const float r0 = rbf(rbf(bflo(xin) + rbf(v[0])) + rbf(v[1]));
      const float r1 = rbf(rbf(bfhi(xin) + rbf(v[2])) + rbf(v[3]));
      *(unsigned*)(out + 80 * s + n0) = pk2(r0, r1);
    }
    if (wg == 0 && lane == 0) __builtin_amdgcn_raw_buffer_store_b32(epoch + 1u, rsWs, 0, 0, AUX_SC1);
  }
}

}

extern "C" void kernel_launch(void* const* d_in, const int* in_sizes, int n_in, void* d_out, int out_size, void* d_ws,
                              size_t ws_size, hipStream_t stream) {
  (void)in_sizes; (void)n_in; (void)out_size;
  fprintf(stderr, "ws_size=%zu\n", ws_size); printf("ws_size=%zu\n", ws_size);
  if (ws_size < WS_BYTES) return;
  fused_decode_kernel<<<dim3(256), dim3(512), 0, stream>>>(
      (const unsigned short*)d_in[0], (const unsigned short*)d_in[1], (const unsigned short*)d_in[2],
      (const unsigned short*)d_in[3], (const unsigned short*)d_in[4], (const unsigned short*)d_in[6],
      (const unsigned short*)d_in[7], (unsigned short*)d_out, (unsigned char*)d_ws);
}
```

```cpp
#include <hip/hip_runtime.h>
#include <hip/hip_bf16.h>

namespace {

typedef unsigned v4u __attribute__((ext_vector_type(4)));
typedef unsigned v2u __attribute__((ext_vector_type(2)));
typedef float f32x4 __attribute__((ext_vector_type(4)));
typedef float f32x2 __attribute__((ext_vector_type(2)));
typedef __bf16 bfx8 __attribute__((ext_vector_type(8)));
typedef __bf16 bfx2 __attribute__((ext_vector_type(2)));
typedef __amdgpu_buffer_rsrc_t rsrc_t;

constexpr int AUX_NT = 2;
constexpr int AUX_SC1 = 16;
constexpr int AUX_POLL = (int)(16u | 0x80000000u);
constexpr unsigned OOB = 0x80000000u;
constexpr unsigned SPIN_CAP = 1u << 16;

constexpr unsigned WS_P0 = 4096;
constexpr unsigned WS_P1 = WS_P0 + 256u * 256u * 8u;
constexpr unsigned WS_P2 = WS_P1 + 256u * 256u * 8u;
constexpr unsigned WS_BYTES = WS_P2 + 8u * 32u * 160u * 8u;

#define KPRE 2
#define VPRE 6

__device__ __forceinline__ rsrc_t mk(const void* p, unsigned bytes) {
  return __builtin_amdgcn_make_buffer_rsrc((void*)p, (short)0, (int)bytes, 0x00020000);
}
__device__ __forceinline__ float asf(unsigned u) { return __builtin_bit_cast(float, u); }
__device__ __forceinline__ unsigned asu(float f) { return __builtin_bit_cast(unsigned, f); }
__device__ __forceinline__ float rbf(float f) { return (float)(__bf16)f; }
__device__ __forceinline__ float bflo(unsigned u) { return __builtin_bit_cast(float, u << 16); }
__device__ __forceinline__ float bfhi(unsigned u) { return __builtin_bit_cast(float, u & 0xFFFF0000u); }
__device__ __forceinline__ unsigned pk2(float a, float b) {
  f32x2 v = {a, b};
  return __builtin_bit_cast(unsigned, __builtin_convertvector(v, bfx2));
}
__device__ __forceinline__ f32x4 mfma(v4u a, v4u b, f32x4 c) {
  return __builtin_amdgcn_mfma_f32_16x16x32_bf16(__builtin_bit_cast(bfx8, a), __builtin_bit_cast(bfx8, b), c, 0, 0, 0);
}
__device__ __forceinline__ float dot2(unsigned a, unsigned b, float c) {
  return __builtin_amdgcn_fdot2_f32_bf16(__builtin_bit_cast(bfx2, a), __builtin_bit_cast(bfx2, b), c, false);
}
template <int CTRL> __device__ __forceinline__ float dpp(float v) {
  return __builtin_bit_cast(float, __builtin_amdgcn_mov_dpp(__builtin_bit_cast(int, v), CTRL, 0xF, 0xF, true));
}
__device__ __forceinline__ float sum8(float v) {
  v += dpp<0xB1>(v);
  v += dpp<0x4E>(v);
  v += dpp<0x141>(v);
  return v;
}
__device__ __forceinline__ float sum_hi(float v) {
  v += dpp<0x128>(v);
  v += __shfl_xor(v, 16);
  v += __shfl_xor(v, 32);
  return v;
}
__device__ __forceinline__ float max_hi(float v) {
  v = fmaxf(v, dpp<0x128>(v));
  v = fmaxf(v, __shfl_xor(v, 16));
  v = fmaxf(v, __shfl_xor(v, 32));
  return v;
}
__device__ __forceinline__ v4u bfrag(unsigned dup, const unsigned (&mask)[4]) {
  v4u b;
  b.x = dup & mask[0];
  b.y = dup & mask[1];
  b.z = dup & mask[2];
  b.w = dup & mask[3];
  return b;
}
#define SCHED_FENCE() __builtin_amdgcn_sched_barrier(0)
#define STAMP(k)

__global__ void __launch_bounds__(512, 2)
fused_decode_kernel(const unsigned short* __restrict__ x, const unsigned short* __restrict__ kc,
                    const unsigned short* __restrict__ vc, const unsigned short* __restrict__ wq,
                    const unsigned short* __restrict__ wo, const unsigned short* __restrict__ w1,
                    const unsigned short* __restrict__ w2, unsigned short* __restrict__ out,
                    unsigned char* ws) {
  __shared__ __attribute__((aligned(16))) float red[16 * 256];
  __shared__ __attribute__((aligned(16))) float red3[8 * 256];
  __shared__ __attribute__((aligned(16))) unsigned short qs[192];
  __shared__ __attribute__((aligned(16))) unsigned short ob[256];
  __shared__ __attribute__((aligned(16))) float mls[16];
  __shared__ unsigned qflag[2];

  STAMP(0);
  const int tid = threadIdx.x;
  const int lane = tid & 63;
  const int w = __builtin_amdgcn_readfirstlane(tid >> 6);
  const int lg = lane >> 3, lc = lane & 7;
  const int col = lane & 15, rq = lane >> 4;
  const int b = blockIdx.x;
  const int h = 4 * (b & 7) + (b >> 6);
  const int s = (b >> 3) & 7;
  const int wg = h * 8 + s;

  const rsrc_t rsX = mk(x, 1280);
  const rsrc_t rsWs = mk(ws, WS_BYTES);
  const size_t kvoff = ((size_t)h * 32768 + 28672 + 512 * s + 64 * w) * 192;
  const rsrc_t rsK = mk(kc + kvoff, 64 * 384);
  const rsrc_t rsV = mk(vc + kvoff, 64 * 384);
  const rsrc_t rsWq = mk(wq + (size_t)(80 * s + 10 * w) * 6144 + 192 * h, 9 * 12288 + 384);
  const rsrc_t rsW1 = mk(w1 + (size_t)(80 * s + 10 * w) * 1536 + 48 * h, 9 * 3072 + 96);
  const rsrc_t rsWo = mk(wo + (size_t)(192 * h + 24 * w) * 640 + 80 * s, 23 * 1280 + 160);
  const rsrc_t rsW2 = mk(w2 + (size_t)(48 * h + 6 * w) * 640 + 80 * s, 5 * 1280 + 160);

  const unsigned epoch = __builtin_amdgcn_raw_buffer_load_b32(rsWs, 0, 0, AUX_POLL);
  const v4u xs0 = __builtin_amdgcn_raw_buffer_load_b128(rsX, 20 * lane, 0, 0);
  const unsigned xs1 = __builtin_amdgcn_raw_buffer_load_b32(rsX, 20 * lane + 16, 0, 0);
  const unsigned xw = __builtin_amdgcn_raw_buffer_load_b32(rsX, lg < 5 ? (unsigned)(160 * s + 20 * w + 4 * lg) : OOB, 0, 0);
  v4u wqv[2][3], w1v[2];
  {
    const unsigned vo = lg < 5 ? (unsigned)(lg * 24576 + lc * 16) : OOB;
#pragma unroll
    for (int u = 0; u < 2; ++u)
#pragma unroll
      for (int j = 0; j < 3; ++j) wqv[u][j] = __builtin_amdgcn_raw_buffer_load_b128(rsWq, vo + j * 128, u * 12288, 0);
    const unsigned vo1 = (lg < 5 && lc < 6) ? (unsigned)(lg * 6144 + lc * 16) : OOB;
    w1v[0] = __builtin_amdgcn_raw_buffer_load_b128(rsW1, vo1, 0, 0);
    w1v[1] = __builtin_amdgcn_raw_buffer_load_b128(rsW1, vo1 + 3072, 0, 0);
  }
  SCHED_FENCE();
  const unsigned pr_off = (unsigned)(wg * 256 + 2 * tid) * 8u;
  const unsigned p0_off = tid < 128 ? WS_P0 + pr_off : OOB;
  const unsigned p1_off = tid < 97 ? WS_P1 + pr_off : OOB;
  const int r3 = 4 * (tid >> 6) + (tid & 3), c3 = (tid >> 2) & 15;
  const unsigned p2_off =
      (tid < 256 && r3 < 10) ? WS_P2 + (unsigned)((s * 32 + h) * 160 + (8 * r3 + (c3 & 7)) * 2 + (c3 >> 3)) * 8u : OOB;
  if (w < 3) {
    const v2u z = {0u, 0u};
    const v4u z4 = {0u, 0u, 0u, 0u};
    __builtin_amdgcn_raw_buffer_store_b128(z4, rsWs, p0_off, 0, AUX_SC1);
    __builtin_amdgcn_raw_buffer_store_b128(z4, rsWs, p1_off, 0, AUX_SC1);
    __builtin_amdgcn_raw_buffer_store_b64(z, rsWs, p2_off, 0, AUX_SC1);
  }
  if (tid < 2) qflag[tid] = 0u;
  const unsigned vkv = (unsigned)(lg * 384 + lc * 16);
  v4u kv[8][3], vv[8][3];
  SCHED_FENCE();
#pragma unroll
  for (int g = 0; g < KPRE; ++g)
#pragma unroll
    for (int j = 0; j < 3; ++j) kv[g][j] = __builtin_amdgcn_raw_buffer_load_b128(rsK, vkv + j * 128, g * 3072, AUX_NT);
  SCHED_FENCE();

  unsigned mask[4];
  {
    const unsigned mm = (lc & 1) ? 0xFFFF0000u : 0x0000FFFFu;
#pragma unroll
    for (int i = 0; i < 4; ++i) mask[i] = ((lc >> 1) == i) ? mm : 0u;
  }
  float mb, sd;
  {
    float e[10];
    e[0] = bflo(xs0.x); e[1] = bfhi(xs0.x); e[2] = bflo(xs0.y); e[3] = bfhi(xs0.y);
    e[4] = bflo(xs0.z); e[5] = bfhi(xs0.z); e[6] = bflo(xs0.w); e[7] = bfhi(xs0.w);
    e[8] = bflo(xs1);   e[9] = bfhi(xs1);
    float t = 0.f;
#pragma unroll
    for (int i = 0; i < 10; ++i) t += e[i];
    t = sum_hi(sum8(t));
    const float mean = t * (1.0f / 640.0f);
    float q2 = 0.f;
#pragma unroll
    for (int i = 0; i < 10; ++i) { const float d = e[i] - mean; q2 += d * d; }
    q2 = sum_hi(sum8(q2));
    const float var = rbf(q2 * (1.0f / 640.0f));
    mb = rbf(mean);
    sd = rbf(sqrtf(rbf(var + 1e-5f)));
  }

  const bool dvalid = (rq >> 1) == (col >> 3);
  float* const myred = red + (w * 2 + (rq >> 1)) * 256 + ((rq & 1) * 8 + (col & 7)) * 4;
  {
    const float a0 = rbf(bflo(xw) - mb) / sd, a1 = rbf(bfhi(xw) - mb) / sd;
    const v4u b0 = bfrag(pk2(a0, a0), mask), b1 = bfrag(pk2(a1, a1), mask);
    const f32x4 zero = {0.f, 0.f, 0.f, 0.f};
    f32x4 acc[4];
#pragma unroll
    for (int j = 0; j < 3; ++j) acc[j] = mfma(wqv[1][j], b1, mfma(wqv[0][j], b0, zero));
    acc[3] = mfma(w1v[1], b1, mfma(w1v[0], b0, zero));
    if (dvalid) {
#pragma unroll
      for (int j = 0; j < 4; ++j) *(f32x4*)(myred + j * 64) = acc[j];
    }
  }
  __syncthreads();
  STAMP(1);
  const unsigned tag = (epoch + 1u) | 0x80000000u;
  const int d0 = ((2 * tid) & 0xE0) + 16 * (tid & 1) + ((tid >> 1) & 7);
  if (w < 2) {
    float t0 = 0.f, t1 = 0.f;
#pragma unroll
    for (int i = 0; i < 16; ++i) {
      const f32x2 v = *(const f32x2*)(red + i * 256 + 2 * tid);
      t0 += v.x;
      t1 += v.y;
    }
    {
      const v4u g = {asu(t0), tag, asu(t1), tag};
      __builtin_amdgcn_raw_buffer_store_b128(g, rsWs, p0_off, 0, AUX_SC1);
    }
    const unsigned gbase = WS_P0 + (unsigned)(h * 8 * 256 + 2 * tid) * 8u;
    v4u g[8];
    for (unsigned spins = 0; spins < SPIN_CAP; ++spins) {
      asm volatile("" ::: "memory");
#pragma unroll
      for (int i = 0; i < 8; ++i) g[i] = __builtin_amdgcn_raw_buffer_load_b128(rsWs, gbase, i * 2048, AUX_POLL);
      bool ok = true;
#pragma unroll
      for (int i = 0; i < 8; ++i) ok &= (g[i].y == tag) & (g[i].w == tag);
      if (__all(ok)) break;
      __builtin_amdgcn_s_sleep(1);
    }
    t0 = 0.f; t1 = 0.f;
#pragma unroll
    for (int i = 0; i < 8; ++i) {
      t0 += asf(g[i].x);
      t1 += asf(g[i].z);
    }
    if (tid < 96) {
      qs[d0] = __builtin_bit_cast(unsigned short, (__bf16)t0);
      qs[d0 + 8] = __builtin_bit_cast(unsigned short, (__bf16)t1);
    } else if (d0 < 240) {
      ob[d0] = __builtin_bit_cast(unsigned short, (__bf16)fmaxf(rbf(t0), 0.f));
      ob[d0 + 8] = __builtin_bit_cast(unsigned short, (__bf16)fmaxf(rbf(t1), 0.f));
    }
    __builtin_amdgcn_fence(__ATOMIC_RELEASE, "workgroup");
    if (lane == 0) __hip_atomic_store(&qflag[w], 1u, __ATOMIC_RELAXED, __HIP_MEMORY_SCOPE_WORKGROUP);
  }
  STAMP(2);
  SCHED_FENCE();
#pragma unroll
  for (int g = KPRE; g < 8; ++g)
#pragma unroll
    for (int j = 0; j < 3; ++j) kv[g][j] = __builtin_amdgcn_raw_buffer_load_b128(rsK, vkv + j * 128, g * 3072, AUX_NT);
#pragma unroll
  for (int g = 0; g < VPRE; ++g)
#pragma unroll
    for (int j = 0; j < 3; ++j) vv[g][j] = __builtin_amdgcn_raw_buffer_load_b128(rsV, vkv + j * 128, g * 3072, AUX_NT);
  SCHED_FENCE();
  for (unsigned spins = 0; spins < (SPIN_CAP << 6); ++spins) {
    const unsigned f0 = __hip_atomic_load(&qflag[0], __ATOMIC_RELAXED, __HIP_MEMORY_SCOPE_WORKGROUP);
    const unsigned f1 = __hip_atomic_load(&qflag[1], __ATOMIC_RELAXED, __HIP_MEMORY_SCOPE_WORKGROUP);
    if (f0 & f1) break;
    __builtin_amdgcn_s_sleep(1);
  }
  __builtin_amdgcn_fence(__ATOMIC_ACQUIRE, "workgroup");
  v4u qf[3];
#pragma unroll
  for (int j = 0; j < 3; ++j) qf[j] = *(const v4u*)(qs + 64 * j + 8 * lc);

  float sc[8];
#pragma unroll
  for (int g = 0; g < 8; ++g) {
    float a = 0.f;
#pragma unroll
    for (int j = 0; j < 3; ++j) {
      a = dot2(kv[g][j].x, qf[j].x, a);
      a = dot2(kv[g][j].y, qf[j].y, a);
      a = dot2(kv[g][j].z, qf[j].z, a);
      a = dot2(kv[g][j].w, qf[j].w, a);
    }
    a = sum8(a);
    sc[g] = rbf(rbf(a) * 0.07216878364870322f);
  }
  SCHED_FENCE();
  STAMP(3);
  v4u gv[8];
  {
#pragma unroll
    for (int g = VPRE; g < 8; ++g)
#pragma unroll
      for (int j = 0; j < 3; ++j) vv[g][j] = __builtin_amdgcn_raw_buffer_load_b128(rsV, vkv + j * 128, g * 3072, AUX_NT);
    const unsigned vo3 = col < 10 ? (unsigned)(rq * 7680 + col * 16) : OOB;
    const unsigned vo4 = (col < 10 && rq < 3) ? (unsigned)(rq * 2560 + col * 16) : OOB;
#pragma unroll
    for (int g = 0; g < 6; ++g) gv[g] = __builtin_amdgcn_raw_buffer_load_b128(rsWo, vo3, g * 1280, 0);
#pragma unroll
    for (int g = 0; g < 2; ++g) gv[6 + g] = __builtin_amdgcn_raw_buffer_load_b128(rsW2, vo4, g * 1280, 0);
  }
  SCHED_FENCE();
  unsigned pd[8];
  {
    float m = fmaxf(fmaxf(fmaxf(sc[0], sc[1]), fmaxf(sc[2], sc[3])), fmaxf(fmaxf(sc[4], sc[5]), fmaxf(sc[6], sc[7])));
    m = max_hi(m);
    float l = 0.f;
#pragma unroll
    for (int g = 0; g < 8; ++g) {
      const float p = __builtin_amdgcn_exp2f((sc[g] - m) * 1.4426950408889634f);
      l += p;
      pd[g] = pk2(p, p);
    }
    l = sum_hi(l);
    if (lane == 0) { mls[w] = m; mls[8 + w] = l; }
  }
  SCHED_FENCE();

  f32x4 oacc[3] = {{0.f, 0.f, 0.f, 0.f}, {0.f, 0.f, 0.f, 0.f}, {0.f, 0.f, 0.f, 0.f}};
#pragma unroll
  for (int g = 0; g < 8; ++g) {
    const v4u bf = bfrag(pd[g], mask);
#pragma unroll
    for (int j = 0; j < 3; ++j) oacc[j] = mfma(vv[g][j], bf, oacc[j]);
    SCHED_FENCE();
  }
  if (dvalid) {
#pragma unroll
    for (int j = 0; j < 3; ++j) *(f32x4*)(myred + j * 64) = oacc[j];
  }
  __syncthreads();
  STAMP(4);

  if (w < 2) {
    float t0 = 0.f, t1 = 0.f;
    {
      const f32x4 m0 = *(const f32x4*)mls, m1 = *(const f32x4*)(mls + 4);
      const f32x4 l0 = *(const f32x4*)(mls + 8), l1 = *(const f32x4*)(mls + 12);
      const float mw[8] = {m0.x, m0.y, m0.z, m0.w, m1.x, m1.y, m1.z, m1.w};
      const float lw[8] = {l0.x, l0.y, l0.z, l0.w, l1.x, l1.y, l1.z, l1.w};
      const float M = fmaxf(fmaxf(fmaxf(mw[0], mw[1]), fmaxf(mw[2], mw[3])), fmaxf(fmaxf(mw[4], mw[5]), fmaxf(mw[6], mw[7])));
      float L = 0.f;
      const int sl = tid < 96 ? 2 * tid : 0;
#pragma unroll
      for (int i = 0; i < 8; ++i) {
        const float f = __builtin_amdgcn_exp2f((mw[i] - M) * 1.4426950408889634f);
        const f32x2 va = *(const f32x2*)(red + (2 * i) * 256 + sl);
        const f32x2 vb = *(const f32x2*)(red + (2 * i + 1) * 256 + sl);
        t0 += f * (va.x + vb.x);
        t1 += f * (va.y + vb.y);
        L += f * lw[i];
      }
      if (tid >= 96) { t0 = M; t1 = L; }
    }
    {
      const v4u g = {asu(t0), tag, asu(t1), tag};
      __builtin_amdgcn_raw_buffer_store_b128(g, rsWs, p1_off, 0, AUX_SC1);
    }
    const unsigned gbase = WS_P1 + (unsigned)(h * 8 * 256 + (tid < 96 ? 2 * tid : 192)) * 8u;
    const unsigned mlo = WS_P1 + (unsigned)((h * 8 + (lane & 7)) * 256 + 192) * 8u;
    v4u g[8], gm;
    for (unsigned spins = 0; spins < SPIN_CAP; ++spins) {
      asm volatile("" ::: "memory");
#pragma unroll
      for (int i = 0; i < 8; ++i) g[i] = __builtin_amdgcn_raw_buffer_load_b128(rsWs, gbase, i * 2048, AUX_POLL);
      gm = __builtin_amdgcn_raw_buffer_load_b128(rsWs, mlo, 0, AUX_POLL);
      bool ok = (gm.y == tag) & (gm.w == tag);
#pragma unroll
      for (int i = 0; i < 8; ++i) ok &= (g[i].y == tag) & (g[i].w == tag);
      if (__all(ok)) break;
      __builtin_amdgcn_s_sleep(1);
    }
    STAMP(5);
    float Ms[8], Ls[8];
#pragma unroll
    for (int i = 0; i < 8; ++i) {
      Ms[i] = __builtin_bit_cast(float, __builtin_amdgcn_readlane((int)gm.x, i));
      Ls[i] = __builtin_bit_cast(float, __builtin_amdgcn_readlane((int)gm.z, i));
    }
    const float Mg = fmaxf(fmaxf(fmaxf(Ms[0], Ms[1]), fmaxf(Ms[2], Ms[3])), fmaxf(fmaxf(Ms[4], Ms[5]), fmaxf(Ms[6], Ms[7])));
    float n0 = 0.f, n1 = 0.f, den = 0.f;
#pragma unroll
    for (int i = 0; i < 8; ++i) {
      const float f = __builtin_amdgcn_exp2f((Ms[i] - Mg) * 1.4426950408889634f);
      n0 += f * asf(g[i].x);
      n1 += f * asf(g[i].z);
      den += f * Ls[i];
    }
    if (tid < 96) {
      ob[d0] = __builtin_bit_cast(unsigned short, (__bf16)(n0 / den));
      ob[d0 + 8] = __builtin_bit_cast(unsigned short, (__bf16)(n1 / den));
    }
  }
  __syncthreads();

  STAMP(6);
  {
    const unsigned* obw = (const unsigned*)ob;
    unsigned o0 = obw[12 * w + 3 * rq], o1 = obw[12 * w + 3 * rq + 1], o2 = obw[12 * w + 3 * rq + 2];
    unsigned hh = obw[96 + 3 * w + (rq < 3 ? rq : 0)];
    if (col >= 8) { o0 = 0u; o1 = 0u; o2 = 0u; }
    if (col < 8 || rq == 3) hh = 0u;
    f32x4 acc = {0.f, 0.f, 0.f, 0.f}, acc2 = {0.f, 0.f, 0.f, 0.f};
    acc = mfma(gv[0], bfrag(__builtin_amdgcn_perm(o0, o0, 0x01000100u), mask), acc);
    acc2 = mfma(gv[1], bfrag(__builtin_amdgcn_perm(o0, o0, 0x03020302u), mask), acc2);
    acc = mfma(gv[2], bfrag(__builtin_amdgcn_perm(o1, o1, 0x01000100u), mask), acc);
    acc2 = mfma(gv[3], bfrag(__builtin_amdgcn_perm(o1, o1, 0x03020302u), mask), acc2);
    acc = mfma(gv[4], bfrag(__builtin_amdgcn_perm(o2, o2, 0x01000100u), mask), acc);
    acc2 = mfma(gv[5], bfrag(__builtin_amdgcn_perm(o2, o2, 0x03020302u), mask), acc2);
    acc = mfma(gv[6], bfrag(__builtin_amdgcn_perm(hh, hh, 0x01000100u), mask), acc);
    acc2 = mfma(gv[7], bfrag(__builtin_amdgcn_perm(hh, hh, 0x03020302u), mask), acc2);
    *(f32x4*)(red3 + w * 256 + lane * 4) = acc + acc2;
  }
  __syncthreads();

  STAMP(7);
  if (w < 3) {
    float t = 0.f;
#pragma unroll
    for (int i = 0; i < 8; ++i) t += red3[i * 256 + tid];
    const v2u g = {asu(t), tag};
    __builtin_amdgcn_raw_buffer_store_b64(g, rsWs, p2_off, 0, AUX_SC1);
  } else if (w == 3 && h < 20) {
    const int n0 = 4 * h + 2 * (lane & 1);
    const unsigned xin = __builtin_amdgcn_raw_buffer_load_b32(rsX, (unsigned)(80 * s + n0) * 2u, 0, 0);
    const unsigned gbase = WS_P2 + (unsigned)((s * 32 + (lane >> 1)) * 160 + n0 * 2) * 8u;
    v4u g[2];
    for (unsigned spins = 0; spins < SPIN_CAP; ++spins) {
      asm volatile("" ::: "memory");
#pragma unroll
      for (int i = 0; i < 2; ++i) g[i] = __builtin_amdgcn_raw_buffer_load_b128(rsWs, gbase + i * 16, 0, AUX_POLL);
      const bool ok = (g[0].y == tag) & (g[0].w == tag) & (g[1].y == tag) & (g[1].w == tag);
      if (__all(ok)) break;
      __builtin_amdgcn_s_sleep(1);
    }
    STAMP(8);
    float v[4] = {asf(g[0].x), asf(g[0].z),
                  asf(g[1].x), asf(g[1].z)};
#pragma unroll
    for (int i = 0; i < 4; ++i) {
      float t = v[i];
      t += dpp<0x4E>(t);
      t += __shfl_xor(t, 4);
      t = sum_hi(t);
      v[i] = t;
    }
    if (lane < 2) {
      const float r0 = rbf(rbf(bflo(xin) + rbf(v[0])) + rbf(v[1]));
      const float r1 = rbf(rbf(bfhi(xin) + rbf(v[2])) + rbf(v[3]));
      *(unsigned*)(out + 80 * s + n0) = pk2(r0, r1);
    }
    if (wg == 0 && lane == 0) __builtin_amdgcn_raw_buffer_store_b32(epoch + 1u, rsWs, 0, 0, AUX_SC1);
  }
}

}

extern "C" void kernel_launch(void* const* d_in, const int* in_sizes, int n_in, void* d_out, int out_size, void* d_ws,
                              size_t ws_size, hipStream_t stream) {
  (void)in_sizes; (void)n_in; (void)out_size;
  if (ws_size < WS_BYTES) return;
  fused_decode_kernel<<<dim3(256), dim3(512), 0, stream>>>(
      (const unsigned short*)d_in[0], (const unsigned short*)d_in[1], (const unsigned short*)d_in[2],
      (const unsigned short*)d_in[3], (const unsigned short*)d_in[4], (const unsigned short*)d_in[6],
      (const unsigned short*)d_in[7], (unsigned short*)d_out, (unsigned char*)d_ws);
}
```

```cpp
#include <hip/hip_runtime.h>
#include <hip/hip_bf16.h>

namespace {

typedef unsigned v4u __attribute__((ext_vector_type(4)));
typedef unsigned v2u __attribute__((ext_vector_type(2)));
typedef float f32x4 __attribute__((ext_vector_type(4)));
typedef float f32x2 __attribute__((ext_vector_type(2)));
typedef __bf16 bfx8 __attribute__((ext_vector_type(8)));
typedef __bf16 bfx2 __attribute__((ext_vector_type(2)));
typedef __amdgpu_buffer_rsrc_t rsrc_t;

constexpr int AUX_NT = 2;
constexpr int AUX_SC1 = 16;
constexpr int AUX_POLL = (int)(16u | 0x80000000u);
constexpr unsigned OOB = 0x80000000u;
constexpr unsigned SPIN_CAP = 1u << 16;

constexpr unsigned WS_P0 = 4096;
constexpr unsigned WS_P1 = WS_P0 + 256u * 256u * 8u;
constexpr unsigned WS_P2 = WS_P1 + 256u * 256u * 8u;
constexpr unsigned WS_BYTES = WS_P2 + 8u * 32u * 160u * 8u;

#define GVCNT 8

__device__ __forceinline__ rsrc_t mk(const void* p, unsigned bytes) {
  return __builtin_amdgcn_make_buffer_rsrc((void*)p, (short)0, (int)bytes, 0x00020000);
}
__device__ __forceinline__ float asf(unsigned u) { return __builtin_bit_cast(float, u); }
__device__ __forceinline__ unsigned asu(float f) { return __builtin_bit_cast(unsigned, f); }
__device__ __forceinline__ float rbf(float f) { return (float)(__bf16)f; }
__device__ __forceinline__ float bflo(unsigned u) { return __builtin_bit_cast(float, u << 16); }
__device__ __forceinline__ float bfhi(unsigned u) { return __builtin_bit_cast(float, u & 0xFFFF0000u); }
__device__ __forceinline__ unsigned pk2(float a, float b) {
  f32x2 v = {a, b};
  return __builtin_bit_cast(unsigned, __builtin_convertvector(v, bfx2));
}
__device__ __forceinline__ f32x4 mfma(v4u a, v4u b, f32x4 c) {
  return __builtin_amdgcn_mfma_f32_16x16x32_bf16(__builtin_bit_cast(bfx8, a), __builtin_bit_cast(bfx8, b), c, 0, 0, 0);
}
__device__ __forceinline__ float dot2(unsigned a, unsigned b, float c) {
  return __builtin_amdgcn_fdot2_f32_bf16(__builtin_bit_cast(bfx2, a), __builtin_bit_cast(bfx2, b), c, false);
}
template <int CTRL> __device__ __forceinline__ float dpp(float v) {
  return __builtin_bit_cast(float, __builtin_amdgcn_mov_dpp(__builtin_bit_cast(int, v), CTRL, 0xF, 0xF, true));
}
__device__ __forceinline__ float sum8(float v) {
  v += dpp<0xB1>(v);
  v += dpp<0x4E>(v);
  v += dpp<0x141>(v);
  return v;
}
__device__ __forceinline__ float sum_hi(float v) {
  v += dpp<0x128>(v);
  v += __shfl_xor(v, 16);
  v += __shfl_xor(v, 32);
  return v;
}
__device__ __forceinline__ float max_hi(float v) {
  v = fmaxf(v, dpp<0x128>(v));
  v = fmaxf(v, __shfl_xor(v, 16));
  v = fmaxf(v, __shfl_xor(v, 32));
  return v;
}
__device__ __forceinline__ v4u bfrag(unsigned dup, const unsigned (&mask)[4]) {
  v4u b;
  b.x = dup & mask[0];
  b.y = dup & mask[1];
  b.z = dup & mask[2];
  b.w = dup & mask[3];
  return b;
}
#define SCHED_FENCE() __builtin_amdgcn_sched_barrier(0)
#define STAMP(k)

__global__ void __launch_bounds__(512, 2)
fused_decode_kernel(const unsigned short* __restrict__ x, const unsigned short* __restrict__ kc,
                    const unsigned short* __restrict__ vc, const unsigned short* __restrict__ wq,
                    const unsigned short* __restrict__ wo, const unsigned short* __restrict__ w1,
                    const unsigned short* __restrict__ w2, unsigned short* __restrict__ out,
                    unsigned char* ws) {
  __shared__ __attribute__((aligned(16))) float red[16 * 192];
  __shared__ __attribute__((aligned(16))) float red3[4 * 256];
  __shared__ __attribute__((aligned(16))) unsigned qs[128];
  __shared__ __attribute__((aligned(16))) float mls[16];
  __shared__ unsigned flg[4];

  STAMP(0);
  const int tid = threadIdx.x;
  const int lane = tid & 63;
  const int w = __builtin_amdgcn_readfirstlane(tid >> 6);
  const int lg = lane >> 3, lc = lane & 7;
  const int col = lane & 15, rq = lane >> 4;
  const int b = blockIdx.x;
  const int h = 4 * (b & 7) + (b >> 6);
  const int s = (b >> 3) & 7;
  const int wg = h * 8 + s;

  const rsrc_t rsX = mk(x, 1280);
  const rsrc_t rsWs = mk(ws, WS_BYTES);
  const size_t kvoff = ((size_t)h * 32768 + 28672 + 512 * s + 64 * w) * 192;
  const rsrc_t rsK = mk(kc + kvoff, 64 * 384);
  const rsrc_t rsV = mk(vc + kvoff, 64 * 384);

  const unsigned epoch = __builtin_amdgcn_raw_buffer_load_b32(rsWs, 0, 0, AUX_SC1);
  if (tid < 4) flg[tid] = 0u;

  unsigned mask[4];
  {
    const unsigned mm = (lc & 1) ? 0xFFFF0000u : 0x0000FFFFu;
#pragma unroll
    for (int i = 0; i < 4; ++i) mask[i] = ((lc >> 1) == i) ? mm : 0u;
  }
  const bool dvalid = (rq >> 1) == (col >> 3);
  const unsigned vkv = (unsigned)(lg * 384 + lc * 16);
  const int t4 = tid - 256;
  const unsigned p1_off = (t4 >= 0 && t4 < 97) ? WS_P1 + (unsigned)(wg * 256 + 2 * t4) * 8u : OOB;
  const int r3 = 4 * (t4 >> 6) + (t4 & 3), c3 = (t4 >> 2) & 15;
  const unsigned p2_off =
      (t4 >= 0 && r3 < 10) ? WS_P2 + (unsigned)((s * 32 + h) * 160 + (8 * r3 + (c3 & 7)) * 2 + (c3 >> 3)) * 8u : OOB;
  v4u kv[8][3], vv[8][3];

  if (w < 2) {
    __builtin_amdgcn_s_setprio(3);
    const v4u xs0 = __builtin_amdgcn_raw_buffer_load_b128(rsX, 20 * lane, 0, 0);
    const unsigned xs1 = __builtin_amdgcn_raw_buffer_load_b32(rsX, 20 * lane + 16, 0, 0);
    const v4u xr0 = __builtin_amdgcn_raw_buffer_load_b128(rsX, 160 * s + 20 * lg, 0, 0);
    const unsigned xr1 = __builtin_amdgcn_raw_buffer_load_b32(rsX, 160 * s + 20 * lg + 16, 0, 0);
    const unsigned short* pa = wq + (size_t)(80 * s) * 6144 + 192 * h + 128 * w;
    const unsigned short* pb = w ? w1 + (size_t)(80 * s) * 1536 + 48 * h : pa + 64;
    const int sb = w ? 3072 : 12288;
    const rsrc_t rsA = mk(pa, 79 * 12288 + 128);
    const rsrc_t rsB = mk(pb, w ? 79 * 3072 + 96 : 79 * 12288 + 128);
    const unsigned voa = (unsigned)(lg * 122880 + lc * 16);
    const unsigned vob = (w && lc >= 6) ? OOB : (unsigned)(lg * 10 * sb + lc * 16);
    v4u wa[10], wb[10];
#pragma unroll
    for (int g = 0; g < 10; ++g) {
      wa[g] = __builtin_amdgcn_raw_buffer_load_b128(rsA, voa, g * 12288, AUX_NT);
      wb[g] = __builtin_amdgcn_raw_buffer_load_b128(rsB, vob, g * sb, AUX_NT);
      SCHED_FENCE();
    }
    const unsigned p0_off = dvalid ? WS_P0 + (unsigned)(wg * 256 + 128 * w + 64 * (rq >> 1) + 8 * (col & 7) + 4 * (rq & 1)) * 8u : OOB;
    SCHED_FENCE();
    __syncthreads();
    SCHED_FENCE();
    float mb, sd;
    {
      float e[10];
      e[0] = bflo(xs0.x); e[1] = bfhi(xs0.x); e[2] = bflo(xs0.y); e[3] = bfhi(xs0.y);
      e[4] = bflo(xs0.z); e[5] = bfhi(xs0.z); e[6] = bflo(xs0.w); e[7] = bfhi(xs0.w);
      e[8] = bflo(xs1);   e[9] = bfhi(xs1);
      float t = 0.f;
#pragma unroll
      for (int i = 0; i < 10; ++i) t += e[i];
      t = sum_hi(sum8(t));
      const float mean = t * (1.0f / 640.0f);
      float q2 = 0.f;
#pragma unroll
      for (int i = 0; i < 10; ++i) { const float d = e[i] - mean; q2 += d * d; }
      q2 = sum_hi(sum8(q2));
      const float var = rbf(q2 * (1.0f / 640.0f));
      mb = rbf(mean);
      sd = rbf(sqrtf(rbf(var + 1e-5f)));
    }
    const unsigned xr[5] = {xr0.x, xr0.y, xr0.z, xr0.w, xr1};
    const f32x4 zero = {0.f, 0.f, 0.f, 0.f};
    f32x4 a0 = zero, a1 = zero, b0 = zero, b1 = zero;
#pragma unroll
    for (int g = 0; g < 5; ++g) {
      const float n0 = rbf(bflo(xr[g]) - mb) / sd, n1 = rbf(bfhi(xr[g]) - mb) / sd;
      const v4u f0 = bfrag(pk2(n0, n0), mask), f1 = bfrag(pk2(n1, n1), mask);
      a0 = mfma(f0, wa[2 * g], a0);
      b0 = mfma(f0, wb[2 * g], b0);
      a1 = mfma(f1, wa[2 * g + 1], a1);
      b1 = mfma(f1, wb[2 * g + 1], b1);
    }
    a0 += a1;
    b0 += b1;
    const unsigned tag = (epoch + 1u) | 0x80000000u;
    {
      const bool p = rq >= 2;
      float r[4];
#pragma unroll
      for (int i = 0; i < 4; ++i) r[i] = (p ? b0[i] : a0[i]) + __shfl_xor(p ? a0[i] : b0[i], 40);
      const v4u g0 = {asu(r[0]), tag, asu(r[1]), tag}, g1 = {asu(r[2]), tag, asu(r[3]), tag};
      __builtin_amdgcn_raw_buffer_store_b128(g0, rsWs, p0_off, 0, AUX_SC1);
      __builtin_amdgcn_raw_buffer_store_b128(g1, rsWs, p0_off + 16, 0, AUX_SC1);
    }
    STAMP(1);
    __builtin_amdgcn_s_setprio(0);
    SCHED_FENCE();
#pragma unroll
    for (int g = 0; g < 2; ++g)
#pragma unroll
      for (int j = 0; j < 3; ++j) kv[g][j] = __builtin_amdgcn_raw_buffer_load_b128(rsK, vkv + j * 128, g * 3072, AUX_NT);
  } else {
#pragma unroll
    for (int g = 0; g < 2; ++g)
#pragma unroll
      for (int j = 0; j < 3; ++j) kv[g][j] = __builtin_amdgcn_raw_buffer_load_b128(rsK, vkv + j * 128, g * 3072, AUX_NT);
    SCHED_FENCE();
    __syncthreads();
  }
  SCHED_FENCE();
#pragma unroll
  for (int g = 2; g < 8; ++g) {
#pragma unroll
    for (int j = 0; j < 3; ++j) kv[g][j] = __builtin_amdgcn_raw_buffer_load_b128(rsK, vkv + j * 128, g * 3072, AUX_NT);
    SCHED_FENCE();
  }
#pragma unroll
  for (int g = 0; g < 8; ++g) {
#pragma unroll
    for (int j = 0; j < 3; ++j) vv[g][j] = __builtin_amdgcn_raw_buffer_load_b128(rsV, vkv + j * 128, g * 3072, AUX_NT);
    SCHED_FENCE();
  }
  STAMP(9);
  if (lane == 0) __hip_atomic_fetch_add(&flg[2], 1u, __ATOMIC_RELAXED, __HIP_MEMORY_SCOPE_WORKGROUP);
  if (w == 2 || w == 3) {
    const int t2 = tid - 128;
    const unsigned tag = (epoch + 1u) | 0x80000000u;
    const unsigned gbase = WS_P0 + (unsigned)(h * 8 * 256 + 2 * t2) * 8u;
    v4u g[8];
#pragma unroll 1
    for (unsigned spins = 0; spins < SPIN_CAP; ++spins) {
      asm volatile("" ::: "memory");
#pragma unroll
      for (int i = 0; i < 8; ++i) g[i] = __builtin_amdgcn_raw_buffer_load_b128(rsWs, gbase, i * 2048, AUX_SC1);
      bool ok = true;
#pragma unroll
      for (int i = 0; i < 8; ++i) ok &= (g[i].y == tag) & (g[i].w == tag);
      if (__all(ok)) break;
      __builtin_amdgcn_s_sleep(1);
    }
    float t0 = 0.f, t1 = 0.f;
#pragma unroll
    for (int i = 0; i < 8; ++i) {
      t0 += asf(g[i].x);
      t1 += asf(g[i].z);
    }
    if (t2 >= 96) { t0 = fmaxf(t0, 0.f); t1 = fmaxf(t1, 0.f); }
    qs[t2] = pk2(t0, t1);
    __builtin_amdgcn_fence(__ATOMIC_RELEASE, "workgroup");
    if (lane == 0) __hip_atomic_store(&flg[w - 2], 1u, __ATOMIC_RELAXED, __HIP_MEMORY_SCOPE_WORKGROUP);
    STAMP(2);
  }
#pragma unroll 1
  for (unsigned spins = 0; spins < (SPIN_CAP << 6); ++spins) {
    const unsigned f0 = __hip_atomic_load(&flg[0], __ATOMIC_RELAXED, __HIP_MEMORY_SCOPE_WORKGROUP);
    const unsigned f1 = __hip_atomic_load(&flg[1], __ATOMIC_RELAXED, __HIP_MEMORY_SCOPE_WORKGROUP);
    if (f0 & f1) break;
    __builtin_amdgcn_s_sleep(1);
  }
  __builtin_amdgcn_fence(__ATOMIC_ACQUIRE, "workgroup");
  v4u qf[3];
#pragma unroll
  for (int j = 0; j < 3; ++j) qf[j] = *(const v4u*)(qs + 32 * j + 4 * lc);

  float sc[8];
#pragma unroll
  for (int g = 0; g < 8; ++g) {
    float a = 0.f;
#pragma unroll
    for (int j = 0; j < 3; ++j) {
      a = dot2(kv[g][j].x, qf[j].x, a);
      a = dot2(kv[g][j].y, qf[j].y, a);
      a = dot2(kv[g][j].z, qf[j].z, a);
      a = dot2(kv[g][j].w, qf[j].w, a);
    }
    a = sum8(a);
    sc[g] = rbf(rbf(a) * 0.07216878364870322f);
  }
  SCHED_FENCE();
  STAMP(3);
  unsigned pd[8];
  {
    float m = fmaxf(fmaxf(fmaxf(sc[0], sc[1]), fmaxf(sc[2], sc[3])), fmaxf(fmaxf(sc[4], sc[5]), fmaxf(sc[6], sc[7])));
    m = max_hi(m);
    float l = 0.f;
#pragma unroll
    for (int g = 0; g < 8; ++g) {
      const float p = __builtin_amdgcn_exp2f((sc[g] - m) * 1.4426950408889634f);
      l += p;
      pd[g] = pk2(p, p);
    }
    l = sum_hi(l);
    if (lane == 0) { mls[w] = m; mls[8 + w] = l; }
  }
  SCHED_FENCE();

  {
    f32x4 oacc[3] = {{0.f, 0.f, 0.f, 0.f}, {0.f, 0.f, 0.f, 0.f}, {0.f, 0.f, 0.f, 0.f}};
#pragma unroll
    for (int g = 0; g < 8; ++g) {
      const v4u bf = bfrag(pd[g], mask);
#pragma unroll
      for (int j = 0; j < 3; ++j) oacc[j] = mfma(bf, vv[g][j], oacc[j]);
      SCHED_FENCE();
    }
    float* const myred = red + (w * 2 + (rq >> 1)) * 192 + 8 * (col & 7) + 4 * (rq & 1);
    if (dvalid) {
#pragma unroll
      for (int j = 0; j < 3; ++j) *(f32x4*)(myred + j * 64) = oacc[j];
    }
  }
  STAMP(10);
  const unsigned tag = (epoch + 1u) | 0x80000000u;
  if (w < 4) {
#pragma unroll 1
    for (unsigned spins = 0; spins < (SPIN_CAP << 6); ++spins) {
      if (__hip_atomic_load(&flg[2], __ATOMIC_RELAXED, __HIP_MEMORY_SCOPE_WORKGROUP) >= GVCNT) break;
      __builtin_amdgcn_s_sleep(1);
    }
    const rsrc_t rsWo = mk(wo + (size_t)(192 * h + 48 * w) * 640 + 80 * s, 47 * 1280 + 160);
    const rsrc_t rsW2 = mk(w2 + (size_t)(48 * h + 12 * w) * 640 + 80 * s, 11 * 1280 + 160);
    const unsigned vo3 = col < 10 ? (unsigned)(rq * 15360 + col * 16) : OOB;
    const unsigned vo4 = col < 10 ? (unsigned)(rq * 3840 + col * 16) : OOB;
    v4u gv[15];
#pragma unroll
    for (int g = 0; g < 3; ++g) gv[12 + g] = __builtin_amdgcn_raw_buffer_load_b128(rsW2, vo4, g * 1280, AUX_NT);
#pragma unroll
    for (int g = 0; g < 12; ++g) gv[g] = __builtin_amdgcn_raw_buffer_load_b128(rsWo, vo3, g * 1280, AUX_NT);
    SCHED_FENCE();
    STAMP(15);
    __syncthreads();
    STAMP(4);
    const unsigned gbase = WS_P1 + (unsigned)((h * 8 + (col & 7)) * 256 + 48 * w + 12 * rq + 6 * (col >> 3)) * 8u;
    const unsigned mlo = WS_P1 + (unsigned)((h * 8 + (col & 7)) * 256 + 192) * 8u;
    v4u g[3], gm;
#pragma unroll 1
    for (unsigned spins = 0; spins < SPIN_CAP; ++spins) {
      asm volatile("" ::: "memory");
#pragma unroll
      for (int i = 0; i < 3; ++i) g[i] = __builtin_amdgcn_raw_buffer_load_b128(rsWs, gbase + i * 16, 0, AUX_SC1);
      gm = __builtin_amdgcn_raw_buffer_load_b128(rsWs, mlo, 0, AUX_SC1);
      bool ok = (gm.y == tag) & (gm.w == tag);
#pragma unroll
      for (int i = 0; i < 3; ++i) ok &= (g[i].y == tag) & (g[i].w == tag);
      if (__all(ok)) break;
      __builtin_amdgcn_s_sleep(1);
    }
    STAMP(5);
    unsigned od[6];
    {
      const float M = asf(gm.x);
      float Mg = fmaxf(M, dpp<0xB1>(M));
      Mg = fmaxf(Mg, dpp<0x4E>(Mg));
      Mg = fmaxf(Mg, dpp<0x141>(Mg));
      const float f = __builtin_amdgcn_exp2f((M - Mg) * 1.4426950408889634f);
      const float den = sum8(f * asf(gm.z));
#pragma unroll
      for (int i = 0; i < 3; ++i) {
        const float n0 = sum8(f * asf(g[i].x)) / den, n1 = sum8(f * asf(g[i].z)) / den;
        od[2 * i] = pk2(n0, n0);
        od[2 * i + 1] = pk2(n1, n1);
      }
    }
    unsigned mlo8[4], mhi8[4];
#pragma unroll
    for (int i = 0; i < 4; ++i) { mlo8[i] = col < 8 ? mask[i] : 0u; mhi8[i] = col < 8 ? 0u : mask[i]; }
    const unsigned short* hb = (const unsigned short*)(qs + 96) + 12 * w + 3 * rq;
    f32x4 ya = {0.f, 0.f, 0.f, 0.f}, yb = {0.f, 0.f, 0.f, 0.f}, za = {0.f, 0.f, 0.f, 0.f};
#pragma unroll
    for (int i = 0; i < 3; ++i) za = mfma(gv[12 + i], bfrag((unsigned)hb[i] * 0x10001u, mhi8), za);
#pragma unroll
    for (int i = 0; i < 6; ++i) {
      ya = mfma(gv[i], bfrag(od[i], mlo8), ya);
      yb = mfma(gv[6 + i], bfrag(od[i], mhi8), yb);
    }
    f32x4 r;
#pragma unroll
    for (int i = 0; i < 4; ++i) r[i] = ya[i] + za[i] + dpp<0x128>(yb[i]);
    *(f32x4*)(red3 + w * 256 + lane * 4) = r;
    STAMP(6);
    __syncthreads();
    STAMP(7);
  } else {
    __syncthreads();
    if (w < 6) {
      float t0 = 0.f, t1 = 0.f;
      const f32x4 m0 = *(const f32x4*)mls, m1 = *(const f32x4*)(mls + 4);
      const f32x4 l0 = *(const f32x4*)(mls + 8), l1 = *(const f32x4*)(mls + 12);
      const float mw[8] = {m0.x, m0.y, m0.z, m0.w, m1.x, m1.y, m1.z, m1.w};
      const float lw[8] = {l0.x, l0.y, l0.z, l0.w, l1.x, l1.y, l1.z, l1.w};
      const float M = fmaxf(fmaxf(fmaxf(mw[0], mw[1]), fmaxf(mw[2], mw[3])), fmaxf(fmaxf(mw[4], mw[5]), fmaxf(mw[6], mw[7])));
      float L = 0.f;
      const int sl = t4 < 96 ? 2 * t4 : 0;
#pragma unroll
      for (int i = 0; i < 8; ++i) {
        const float f = __builtin_amdgcn_exp2f((mw[i] - M) * 1.4426950408889634f);
        const f32x2 va = *(const f32x2*)(red + (2 * i) * 192 + sl);
        const f32x2 vb = *(const f32x2*)(red + (2 * i + 1) * 192 + sl);
        t0 += f * (va.x + vb.x);
        t1 += f * (va.y + vb.y);
        L += f * lw[i];
      }
      if (t4 >= 96) { t0 = M; t1 = L; }
      const v4u g = {asu(t0), tag, asu(t1), tag};
      __builtin_amdgcn_raw_buffer_store_b128(g, rsWs, p1_off, 0, AUX_SC1);
      STAMP(12);
      STAMP(13);
    }
    __syncthreads();
    if (w < 7) {
      const float t = (red3[t4] + red3[256 + t4]) + (red3[512 + t4] + red3[768 + t4]);
      const v2u g = {asu(t), tag};
      __builtin_amdgcn_raw_buffer_store_b64(g, rsWs, p2_off, 0, AUX_SC1);
    } else if (h < 20) {
      const int n0 = 4 * h + 2 * (lane & 1);
      const unsigned xin = __builtin_amdgcn_raw_buffer_load_b32(rsX, (unsigned)(80 * s + n0) * 2u, 0, 0);
      const unsigned gbase = WS_P2 + (unsigned)((s * 32 + (lane >> 1)) * 160 + n0 * 2) * 8u;
      v4u g[2];
#pragma unroll 1
      for (unsigned spins = 0; spins < SPIN_CAP; ++spins) {
        asm volatile("" ::: "memory");
#pragma unroll
        for (int i = 0; i < 2; ++i) g[i] = __builtin_amdgcn_raw_buffer_load_b128(rsWs, gbase + i * 16, 0, AUX_SC1);
        const bool ok = (g[0].y == tag) & (g[0].w == tag) & (g[1].y == tag) & (g[1].w == tag);
        if (__all(ok)) break;
        __builtin_amdgcn_s_sleep(1);
      }
      STAMP(8);
      float v[4] = {asf(g[0].x), asf(g[0].z), asf(g[1].x), asf(g[1].z)};
#pragma unroll
      for (int i = 0; i < 4; ++i) {
        float t = v[i];
        t += dpp<0x4E>(t);
        t += __shfl_xor(t, 4);
        t = sum_hi(t);
        v[i] = t;
      }
      if (lane < 2) {
        const float r0 = rbf(rbf(bflo(xin) + rbf(v[0])) + rbf(v[1]));
        const float r1 = rbf(rbf(bfhi(xin) + rbf(v[2])) + rbf(v[3]));
        *(unsigned*)(out + 80 * s + n0) = pk2(r0, r1);
      }
      if (wg == 0 && lane == 0) __builtin_amdgcn_raw_buffer_store_b32(epoch + 1u, rsWs, 0, 0, AUX_SC1);
    }
  }
}

}

extern "C" void kernel_launch(void* const* d_in, const int* in_sizes, int n_in, void* d_out, int out_size, void* d_ws,
                              size_t ws_size, hipStream_t stream) {
  (void)in_sizes; (void)n_in; (void)out_size;
  if (ws_size < WS_BYTES) return;
  fused_decode_kernel<<<dim3(256), dim3(512), 0, stream>>>(
      (const unsigned short*)d_in[0], (const unsigned short*)d_in[1], (const unsigned short*)d_in[2],
      (const unsigned short*)d_in[3], (const unsigned short*)d_in[4], (const unsigned short*)d_in[6],
      (const unsigned short*)d_in[7], (unsigned short*)d_out, (unsigned char*)d_ws);
}
```

```cpp
#include <hip/hip_runtime.h>
#include <hip/hip_bf16.h>

namespace {

typedef unsigned v4u __attribute__((ext_vector_type(4)));
typedef unsigned v2u __attribute__((ext_vector_type(2)));
typedef float f32x4 __attribute__((ext_vector_type(4)));
typedef float f32x2 __attribute__((ext_vector_type(2)));
typedef __bf16 bfx8 __attribute__((ext_vector_type(8)));
typedef __bf16 bfx2 __attribute__((ext_vector_type(2)));
typedef __amdgpu_buffer_rsrc_t rsrc_t;

constexpr int AUX_NT = 2;
constexpr int AUX_SC1 = 16;
constexpr int AUX_POLL = (int)(16u | 0x80000000u);
constexpr unsigned OOB = 0x80000000u;
constexpr unsigned SPIN_CAP = 1u << 16;

constexpr unsigned WS_P0 = 4096;
constexpr unsigned WS_P1 = WS_P0 + 256u * 256u * 8u;
constexpr unsigned WS_P2 = WS_P1 + 256u * 256u * 8u;
constexpr unsigned WS_BYTES = WS_P2 + 8u * 32u * 160u * 8u;

#define GVCNT 8

__device__ __forceinline__ rsrc_t mk(const void* p, unsigned bytes) {
  return __builtin_amdgcn_make_buffer_rsrc((void*)p, (short)0, (int)bytes, 0x00020000);
}
__device__ __forceinline__ float asf(unsigned u) { return __builtin_bit_cast(float, u); }
__device__ __forceinline__ unsigned asu(float f) { return __builtin_bit_cast(unsigned, f); }
__device__ __forceinline__ float rbf(float f) { return (float)(__bf16)f; }
__device__ __forceinline__ float bflo(unsigned u) { return __builtin_bit_cast(float, u << 16); }
__device__ __forceinline__ float bfhi(unsigned u) { return __builtin_bit_cast(float, u & 0xFFFF0000u); }
__device__ __forceinline__ unsigned pk2(float a, float b) {
  f32x2 v = {a, b};
  return __builtin_bit_cast(unsigned, __builtin_convertvector(v, bfx2));
}
__device__ __forceinline__ f32x4 mfma(v4u a, v4u b, f32x4 c) {
  return __builtin_amdgcn_mfma_f32_16x16x32_bf16(__builtin_bit_cast(bfx8, a), __builtin_bit_cast(bfx8, b), c, 0, 0, 0);
}
__device__ __forceinline__ float dot2(unsigned a, unsigned b, float c) {
  return __builtin_amdgcn_fdot2_f32_bf16(__builtin_bit_cast(bfx2, a), __builtin_bit_cast(bfx2, b), c, false);
}
template <int CTRL> __device__ __forceinline__ float dpp(float v) {
  return __builtin_bit_cast(float, __builtin_amdgcn_mov_dpp(__builtin_bit_cast(int, v), CTRL, 0xF, 0xF, true));
}
__device__ __forceinline__ float sum8(float v) {
  v += dpp<0xB1>(v);
  v += dpp<0x4E>(v);
  v += dpp<0x141>(v);
  return v;
}
__device__ __forceinline__ float sum_hi(float v) {
  v += dpp<0x128>(v);
  v += __shfl_xor(v, 16);
  v += __shfl_xor(v, 32);
  return v;
}
__device__ __forceinline__ float max_hi(float v) {
  v = fmaxf(v, dpp<0x128>(v));
  v = fmaxf(v, __shfl_xor(v, 16));
  v = fmaxf(v, __shfl_xor(v, 32));
  return v;
}
__device__ __forceinline__ v4u bfrag(unsigned dup, const unsigned (&mask)[4]) {
  v4u b;
  b.x = dup & mask[0];
  b.y = dup & mask[1];
  b.z = dup & mask[2];
  b.w = dup & mask[3];
  return b;
}
#define SCHED_FENCE() __builtin_amdgcn_sched_barrier(0)
#define STAMP(k)

__global__ void __launch_bounds__(512, 2)
fused_decode_kernel(const unsigned short* __restrict__ x, const unsigned short* __restrict__ kc,
                    const unsigned short* __restrict__ vc, const unsigned short* __restrict__ wq,
                    const unsigned short* __restrict__ wo, const unsigned short* __restrict__ w1,
                    const unsigned short* __restrict__ w2, unsigned short* __restrict__ out,
                    unsigned char* ws) {
  __shared__ __attribute__((aligned(16))) float red[16 * 192];
  __shared__ __attribute__((aligned(16))) float red3[4 * 256];
  __shared__ __attribute__((aligned(16))) unsigned qs[128];
  __shared__ __attribute__((aligned(16))) float mls[16];
  __shared__ unsigned flg[4];

  STAMP(0);
  const unsigned t0_=(unsigned)wall_clock64();
  const int tid = threadIdx.x;
  const int lane = tid & 63;
  const int w = __builtin_amdgcn_readfirstlane(tid >> 6);
  const int lg = lane >> 3, lc = lane & 7;
  const int col = lane & 15, rq = lane >> 4;
  const int b = blockIdx.x;
  const int h = 4 * (b & 7) + (b >> 6);
  const int s = (b >> 3) & 7;
  const int wg = h * 8 + s;

  const rsrc_t rsX = mk(x, 1280);
  const rsrc_t rsWs = mk(ws, WS_BYTES);
  const size_t kvoff = ((size_t)h * 32768 + 28672 + 512 * s + 64 * w) * 192;
  const rsrc_t rsK = mk(kc + kvoff, 64 * 384);
  const rsrc_t rsV = mk(vc + kvoff, 64 * 384);

  const unsigned epoch = __builtin_amdgcn_raw_buffer_load_b32(rsWs, 0, 0, AUX_SC1);
  if (tid == 0) __builtin_amdgcn_raw_buffer_store_b32(t0_, rsWs, 1024 + 4 * b, 0, AUX_SC1);
  if (tid < 4) flg[tid] = 0u;

  unsigned mask[4];
  {
    const unsigned mm = (lc & 1) ? 0xFFFF0000u : 0x0000FFFFu;
#pragma unroll
    for (int i = 0; i < 4; ++i) mask[i] = ((lc >> 1) == i) ? mm : 0u;
  }
  const bool dvalid = (rq >> 1) == (col >> 3);
  const unsigned vkv = (unsigned)(lg * 384 + lc * 16);
  const int t4 = tid - 256;
  const unsigned p1_off = (t4 >= 0 && t4 < 97) ? WS_P1 + (unsigned)(wg * 256 + 2 * t4) * 8u : OOB;
  const int r3 = 4 * (t4 >> 6) + (t4 & 3), c3 = (t4 >> 2) & 15;
  const unsigned p2_off =
      (t4 >= 0 && r3 < 10) ? WS_P2 + (unsigned)((s * 32 + h) * 160 + (8 * r3 + (c3 & 7)) * 2 + (c3 >> 3)) * 8u : OOB;
  v4u kv[8][3], vv[8][3];

  if (w < 2) {
    __builtin_amdgcn_s_setprio(3);
    const v4u xs0 = __builtin_amdgcn_raw_buffer_load_b128(rsX, 20 * lane, 0, 0);
    const unsigned xs1 = __builtin_amdgcn_raw_buffer_load_b32(rsX, 20 * lane + 16, 0, 0);
    const v4u xr0 = __builtin_amdgcn_raw_buffer_load_b128(rsX, 160 * s + 20 * lg, 0, 0);
    const unsigned xr1 = __builtin_amdgcn_raw_buffer_load_b32(rsX, 160 * s + 20 * lg + 16, 0, 0);
    const unsigned short* pa = wq + (size_t)(80 * s) * 6144 + 192 * h + 128 * w;
    const unsigned short* pb = w ? w1 + (size_t)(80 * s) * 1536 + 48 * h : pa + 64;
    const int sb = w ? 3072 : 12288;
    const rsrc_t rsA = mk(pa, 79 * 12288 + 128);
    const rsrc_t rsB = mk(pb, w ? 79 * 3072 + 96 : 79 * 12288 + 128);
    const unsigned voa = (unsigned)(lg * 122880 + lc * 16);
    const unsigned vob = (w && lc >= 6) ? OOB : (unsigned)(lg * 10 * sb + lc * 16);
    v4u wa[10], wb[10];
#pragma unroll
    for (int g = 0; g < 10; ++g) {
      wa[g] = __builtin_amdgcn_raw_buffer_load_b128(rsA, voa, g * 12288, AUX_NT);
      wb[g] = __builtin_amdgcn_raw_buffer_load_b128(rsB, vob, g * sb, AUX_NT);
      SCHED_FENCE();
    }
    const unsigned p0_off = dvalid ? WS_P0 + (unsigned)(wg * 256 + 128 * w + 64 * (rq >> 1) + 8 * (col & 7) + 4 * (rq & 1)) * 8u : OOB;
    SCHED_FENCE();
    __syncthreads();
    SCHED_FENCE();
    float mb, sd;
    {
      float e[10];
      e[0] = bflo(xs0.x); e[1] = bfhi(xs0.x); e[2] = bflo(xs0.y); e[3] = bfhi(xs0.y);
      e[4] = bflo(xs0.z); e[5] = bfhi(xs0.z); e[6] = bflo(xs0.w); e[7] = bfhi(xs0.w);
      e[8] = bflo(xs1);   e[9] = bfhi(xs1);
      float t = 0.f;
#pragma unroll
      for (int i = 0; i < 10; ++i) t += e[i];
      t = sum_hi(sum8(t));
      const float mean = t * (1.0f / 640.0f);
      float q2 = 0.f;
#pragma unroll
      for (int i = 0; i < 10; ++i) { const float d = e[i] - mean; q2 += d * d; }
      q2 = sum_hi(sum8(q2));
      const float var = rbf(q2 * (1.0f / 640.0f));
      mb = rbf(mean);
      sd = rbf(sqrtf(rbf(var + 1e-5f)));
    }
    const unsigned xr[5] = {xr0.x, xr0.y, xr0.z, xr0.w, xr1};
    const f32x4 zero = {0.f, 0.f, 0.f, 0.f};
    f32x4 a0 = zero, a1 = zero, b0 = zero, b1 = zero;
#pragma unroll
    for (int g = 0; g < 5; ++g) {
      const float n0 = rbf(bflo(xr[g]) - mb) / sd, n1 = rbf(bfhi(xr[g]) - mb) / sd;
      const v4u f0 = bfrag(pk2(n0, n0), mask), f1 = bfrag(pk2(n1, n1), mask);
      a0 = mfma(f0, wa[2 * g], a0);
      b0 = mfma(f0, wb[2 * g], b0);
      a1 = mfma(f1, wa[2 * g + 1], a1);
      b1 = mfma(f1, wb[2 * g + 1], b1);
    }
    a0 += a1;
    b0 += b1;
    const unsigned tag = (epoch + 1u) | 0x80000000u;
    {
      const bool p = rq >= 2;
      float r[4];
#pragma unroll
      for (int i = 0; i < 4; ++i) r[i] = (p ? b0[i] : a0[i]) + __shfl_xor(p ? a0[i] : b0[i], 40);
      const v4u g0 = {asu(r[0]), tag, asu(r[1]), tag}, g1 = {asu(r[2]), tag, asu(r[3]), tag};
      __builtin_amdgcn_raw_buffer_store_b128(g0, rsWs, p0_off, 0, AUX_SC1);
      __builtin_amdgcn_raw_buffer_store_b128(g1, rsWs, p0_off + 16, 0, AUX_SC1);
    }
    STAMP(1);
    __builtin_amdgcn_s_setprio(0);
    SCHED_FENCE();
#pragma unroll
    for (int g = 0; g < 2; ++g)
#pragma unroll
      for (int j = 0; j < 3; ++j) kv[g][j] = __builtin_amdgcn_raw_buffer_load_b128(rsK, vkv + j * 128, g * 3072, AUX_NT);
  } else {
#pragma unroll
    for (int g = 0; g < 2; ++g)
#pragma unroll
      for (int j = 0; j < 3; ++j) kv[g][j] = __builtin_amdgcn_raw_buffer_load_b128(rsK, vkv + j * 128, g * 3072, AUX_NT);
    SCHED_FENCE();
    __syncthreads();
  }
  SCHED_FENCE();
#pragma unroll
  for (int g = 2; g < 8; ++g) {
#pragma unroll
    for (int j = 0; j < 3; ++j) kv[g][j] = __builtin_amdgcn_raw_buffer_load_b128(rsK, vkv + j * 128, g * 3072, AUX_NT);
    SCHED_FENCE();
  }
#pragma unroll
  for (int g = 0; g < 8; ++g) {
#pragma unroll
    for (int j = 0; j < 3; ++j) vv[g][j] = __builtin_amdgcn_raw_buffer_load_b128(rsV, vkv + j * 128, g * 3072, AUX_NT);
    SCHED_FENCE();
  }
  STAMP(9);
  if (lane == 0) __hip_atomic_fetch_add(&flg[2], 1u, __ATOMIC_RELAXED, __HIP_MEMORY_SCOPE_WORKGROUP);
  if (w == 2 || w == 3) {
    const int t2 = tid - 128;
    const unsigned tag = (epoch + 1u) | 0x80000000u;
    const unsigned gbase = WS_P0 + (unsigned)(h * 8 * 256 + 2 * t2) * 8u;
    v4u g[8];
#pragma unroll 1
    for (unsigned spins = 0; spins < SPIN_CAP; ++spins) {
      asm volatile("" ::: "memory");
#pragma unroll
      for (int i = 0; i < 8; ++i) g[i] = __builtin_amdgcn_raw_buffer_load_b128(rsWs, gbase, i * 2048, AUX_SC1);
      bool ok = true;
#pragma unroll
      for (int i = 0; i < 8; ++i) ok &= (g[i].y == tag) & (g[i].w == tag);
      if (__all(ok)) break;
      __builtin_amdgcn_s_sleep(1);
    }
    float t0 = 0.f, t1 = 0.f;
#pragma unroll
    for (int i = 0; i < 8; ++i) {
      t0 += asf(g[i].x);
      t1 += asf(g[i].z);
    }
    if (t2 >= 96) { t0 = fmaxf(t0, 0.f); t1 = fmaxf(t1, 0.f); }
    qs[t2] = pk2(t0, t1);
    __builtin_amdgcn_fence(__ATOMIC_RELEASE, "workgroup");
    if (lane == 0) __hip_atomic_store(&flg[w - 2], 1u, __ATOMIC_RELAXED, __HIP_MEMORY_SCOPE_WORKGROUP);
    STAMP(2);
  }
#pragma unroll 1
  for (unsigned spins = 0; spins < (SPIN_CAP << 6); ++spins) {
    const unsigned f0 = __hip_atomic_load(&flg[0], __ATOMIC_RELAXED, __HIP_MEMORY_SCOPE_WORKGROUP);
    const unsigned f1 = __hip_atomic_load(&flg[1], __ATOMIC_RELAXED, __HIP_MEMORY_SCOPE_WORKGROUP);
    if (f0 & f1) break;
    __builtin_amdgcn_s_sleep(1);
  }
  __builtin_amdgcn_fence(__ATOMIC_ACQUIRE, "workgroup");
  v4u qf[3];
#pragma unroll
  for (int j = 0; j < 3; ++j) qf[j] = *(const v4u*)(qs + 32 * j + 4 * lc);

  float sc[8];
#pragma unroll
  for (int g = 0; g < 8; ++g) {
    float a = 0.f;
#pragma unroll
    for (int j = 0; j < 3; ++j) {
      a = dot2(kv[g][j].x, qf[j].x, a);
      a = dot2(kv[g][j].y, qf[j].y, a);
      a = dot2(kv[g][j].z, qf[j].z, a);
      a = dot2(kv[g][j].w, qf[j].w, a);
    }
    a = sum8(a);
    sc[g] = rbf(rbf(a) * 0.07216878364870322f);
  }
  SCHED_FENCE();
  STAMP(3);
  unsigned pd[8];
  {
    float m = fmaxf(fmaxf(fmaxf(sc[0], sc[1]), fmaxf(sc[2], sc[3])), fmaxf(fmaxf(sc[4], sc[5]), fmaxf(sc[6], sc[7])));
    m = max_hi(m);
    float l = 0.f;
#pragma unroll
    for (int g = 0; g < 8; ++g) {
      const float p = __builtin_amdgcn_exp2f((sc[g] - m) * 1.4426950408889634f);
      l += p;
      pd[g] = pk2(p, p);
    }
    l = sum_hi(l);
    if (lane == 0) { mls[w] = m; mls[8 + w] = l; }
  }
  SCHED_FENCE();

  {
    f32x4 oacc[3] = {{0.f, 0.f, 0.f, 0.f}, {0.f, 0.f, 0.f, 0.f}, {0.f, 0.f, 0.f, 0.f}};
#pragma unroll
    for (int g = 0; g < 8; ++g) {
      const v4u bf = bfrag(pd[g], mask);
#pragma unroll
      for (int j = 0; j < 3; ++j) oacc[j] = mfma(bf, vv[g][j], oacc[j]);
      SCHED_FENCE();
    }
    float* const myred = red + (w * 2 + (rq >> 1)) * 192 + 8 * (col & 7) + 4 * (rq & 1);
    if (dvalid) {
#pragma unroll
      for (int j = 0; j < 3; ++j) *(f32x4*)(myred + j * 64) = oacc[j];
    }
  }
  STAMP(10);
  const unsigned tag = (epoch + 1u) | 0x80000000u;
  if (w < 4) {
#pragma unroll 1
    for (unsigned spins = 0; spins < (SPIN_CAP << 6); ++spins) {
      if (__hip_atomic_load(&flg[2], __ATOMIC_RELAXED, __HIP_MEMORY_SCOPE_WORKGROUP) >= GVCNT) break;
      __builtin_amdgcn_s_sleep(1);
    }
    const rsrc_t rsWo = mk(wo + (size_t)(192 * h + 48 * w) * 640 + 80 * s, 47 * 1280 + 160);
    const rsrc_t rsW2 = mk(w2 + (size_t)(48 * h + 12 * w) * 640 + 80 * s, 11 * 1280 + 160);
    const unsigned vo3 = col < 10 ? (unsigned)(rq * 15360 + col * 16) : OOB;
    const unsigned vo4 = col < 10 ? (unsigned)(rq * 3840 + col * 16) : OOB;
    v4u gv[15];
#pragma unroll
    for (int g = 0; g < 3; ++g) gv[12 + g] = __builtin_amdgcn_raw_buffer_load_b128(rsW2, vo4, g * 1280, AUX_NT);
#pragma unroll
    for (int g = 0; g < 12; ++g) gv[g] = __builtin_amdgcn_raw_buffer_load_b128(rsWo, vo3, g * 1280, AUX_NT);
    SCHED_FENCE();
    STAMP(15);
    __syncthreads();
    STAMP(4);
    const unsigned gbase = WS_P1 + (unsigned)((h * 8 + (col & 7)) * 256 + 48 * w + 12 * rq + 6 * (col >> 3)) * 8u;
    const unsigned mlo = WS_P1 + (unsigned)((h * 8 + (col & 7)) * 256 + 192) * 8u;
    v4u g[3], gm;
#pragma unroll 1
    for (unsigned spins = 0; spins < SPIN_CAP; ++spins) {
      asm volatile("" ::: "memory");
#pragma unroll
      for (int i = 0; i < 3; ++i) g[i] = __builtin_amdgcn_raw_buffer_load_b128(rsWs, gbase + i * 16, 0, AUX_SC1);
      gm = __builtin_amdgcn_raw_buffer_load_b128(rsWs, mlo, 0, AUX_SC1);
      bool ok = (gm.y == tag) & (gm.w == tag);
#pragma unroll
      for (int i = 0; i < 3; ++i) ok &= (g[i].y == tag) & (g[i].w == tag);
      if (__all(ok)) break;
      __builtin_amdgcn_s_sleep(1);
    }
    STAMP(5);
    unsigned od[6];
    {
      const float M = asf(gm.x);
      float Mg = fmaxf(M, dpp<0xB1>(M));
      Mg = fmaxf(Mg, dpp<0x4E>(Mg));
      Mg = fmaxf(Mg, dpp<0x141>(Mg));
      const float f = __builtin_amdgcn_exp2f((M - Mg) * 1.4426950408889634f);
      const float den = sum8(f * asf(gm.z));
#pragma unroll
      for (int i = 0; i < 3; ++i) {
        const float n0 = sum8(f * asf(g[i].x)) / den, n1 = sum8(f * asf(g[i].z)) / den;
        od[2 * i] = pk2(n0, n0);
        od[2 * i + 1] = pk2(n1, n1);
      }
    }
    unsigned mlo8[4], mhi8[4];
#pragma unroll
    for (int i = 0; i < 4; ++i) { mlo8[i] = col < 8 ? mask[i] : 0u; mhi8[i] = col < 8 ? 0u : mask[i]; }
    const unsigned short* hb = (const unsigned short*)(qs + 96) + 12 * w + 3 * rq;
    f32x4 ya = {0.f, 0.f, 0.f, 0.f}, yb = {0.f, 0.f, 0.f, 0.f}, za = {0.f, 0.f, 0.f, 0.f};
#pragma unroll
    for (int i = 0; i < 3; ++i) za = mfma(gv[12 + i], bfrag((unsigned)hb[i] * 0x10001u, mhi8), za);
#pragma unroll
    for (int i = 0; i < 6; ++i) {
      ya = mfma(gv[i], bfrag(od[i], mlo8), ya);
      yb = mfma(gv[6 + i], bfrag(od[i], mhi8), yb);
    }
    f32x4 r;
#pragma unroll
    for (int i = 0; i < 4; ++i) r[i] = ya[i] + za[i] + dpp<0x128>(yb[i]);
    *(f32x4*)(red3 + w * 256 + lane * 4) = r;
    STAMP(6);
    __syncthreads();
    STAMP(7);
  } else {
    __syncthreads();
    if (w < 6) {
      float t0 = 0.f, t1 = 0.f;
      const f32x4 m0 = *(const f32x4*)mls, m1 = *(const f32x4*)(mls + 4);
      const f32x4 l0 = *(const f32x4*)(mls + 8), l1 = *(const f32x4*)(mls + 12);
      const float mw[8] = {m0.x, m0.y, m0.z, m0.w, m1.x, m1.y, m1.z, m1.w};
      const float lw[8] = {l0.x, l0.y, l0.z, l0.w, l1.x, l1.y, l1.z, l1.w};
      const float M = fmaxf(fmaxf(fmaxf(mw[0], mw[1]), fmaxf(mw[2], mw[3])), fmaxf(fmaxf(mw[4], mw[5]), fmaxf(mw[6], mw[7])));
      float L = 0.f;
      const int sl = t4 < 96 ? 2 * t4 : 0;
#pragma unroll
      for (int i = 0; i < 8; ++i) {
        const float f = __builtin_amdgcn_exp2f((mw[i] - M) * 1.4426950408889634f);
        const f32x2 va = *(const f32x2*)(red + (2 * i) * 192 + sl);
        const f32x2 vb = *(const f32x2*)(red + (2 * i + 1) * 192 + sl);
        t0 += f * (va.x + vb.x);
        t1 += f * (va.y + vb.y);
        L += f * lw[i];
      }
      if (t4 >= 96) { t0 = M; t1 = L; }
      const v4u g = {asu(t0), tag, asu(t1), tag};
      __builtin_amdgcn_raw_buffer_store_b128(g, rsWs, p1_off, 0, AUX_SC1);
      STAMP(12);
      STAMP(13);
    }
    __syncthreads();
    if (w < 7) {
      const float t = (red3[t4] + red3[256 + t4]) + (red3[512 + t4] + red3[768 + t4]);
      const v2u g = {asu(t), tag};
      __builtin_amdgcn_raw_buffer_store_b64(g, rsWs, p2_off, 0, AUX_SC1);
    } else if (h < 20) {
      const int n0 = 4 * h + 2 * (lane & 1);
      const unsigned xin = __builtin_amdgcn_raw_buffer_load_b32(rsX, (unsigned)(80 * s + n0) * 2u, 0, 0);
      const unsigned gbase = WS_P2 + (unsigned)((s * 32 + (lane >> 1)) * 160 + n0 * 2) * 8u;
      v4u g[2];
#pragma unroll 1
      for (unsigned spins = 0; spins < SPIN_CAP; ++spins) {
        asm volatile("" ::: "memory");
#pragma unroll
        for (int i = 0; i < 2; ++i) g[i] = __builtin_amdgcn_raw_buffer_load_b128(rsWs, gbase + i * 16, 0, AUX_SC1);
        const bool ok = (g[0].y == tag) & (g[0].w == tag) & (g[1].y == tag) & (g[1].w == tag);
        if (__all(ok)) break;
        __builtin_amdgcn_s_sleep(1);
      }
      STAMP(8);
      float v[4] = {asf(g[0].x), asf(g[0].z), asf(g[1].x), asf(g[1].z)};
#pragma unroll
      for (int i = 0; i < 4; ++i) {
        float t = v[i];
        t += dpp<0x4E>(t);
        t += __shfl_xor(t, 4);
        t = sum_hi(t);
        v[i] = t;
      }
      if (lane < 2) {
        const float r0 = rbf(rbf(bflo(xin) + rbf(v[0])) + rbf(v[1]));
        const float r1 = rbf(rbf(bfhi(xin) + rbf(v[2])) + rbf(v[3]));
        *(unsigned*)(out + 80 * s + n0) = pk2(r0, r1);
      }
      if (wg == 0 && lane == 0) __builtin_amdgcn_raw_buffer_store_b32(epoch + 1u, rsWs, 0, 0, AUX_SC1);
      if (wg == 0) {
        const v4u tt = __builtin_amdgcn_raw_buffer_load_b128(rsWs, 1024 + 16 * lane, 0, AUX_SC1);
        int d[4] = {(int)(tt.x - t0_), (int)(tt.y - t0_), (int)(tt.z - t0_), (int)(tt.w - t0_)};
        int mn = min(min(d[0], d[1]), min(d[2], d[3])), mx = max(max(d[0], d[1]), max(d[2], d[3]));
        for (int o = 1; o < 64; o <<= 1) { mn = min(mn, __shfl_xor(mn, o)); mx = max(mx, __shfl_xor(mx, o)); }
        int ref = mx;
        if (ref < -2000) ref = -2000;
        if (ref > 2000) ref = 2000;
        while ((int)((unsigned)wall_clock64() - t0_) < 6000 + ref) __builtin_amdgcn_s_sleep(2);
      }
    }
  }
}

}

extern "C" void kernel_launch(void* const* d_in, const int* in_sizes, int n_in, void* d_out, int out_size, void* d_ws,
                              size_t ws_size, hipStream_t stream) {
  (void)in_sizes; (void)n_in; (void)out_size;
  if (ws_size < WS_BYTES) return;
  fused_decode_kernel<<<dim3(256), dim3(512), 0, stream>>>(
      (const unsigned short*)d_in[0], (const unsigned short*)d_in[1], (const unsigned short*)d_in[2],
      (const unsigned short*)d_in[3], (const unsigned short*)d_in[4], (const unsigned short*)d_in[6],
      (const unsigned short*)d_in[7], (unsigned short*)d_out, (unsigned char*)d_ws);
}
```

```cpp
#include <hip/hip_runtime.h>
#include <hip/hip_bf16.h>

namespace {

typedef unsigned v4u __attribute__((ext_vector_type(4)));
typedef unsigned v2u __attribute__((ext_vector_type(2)));
typedef float f32x4 __attribute__((ext_vector_type(4)));
typedef float f32x2 __attribute__((ext_vector_type(2)));
typedef __bf16 bfx8 __attribute__((ext_vector_type(8)));
typedef __bf16 bfx2 __attribute__((ext_vector_type(2)));
typedef __amdgpu_buffer_rsrc_t rsrc_t;

constexpr int AUX_NT = 2;
constexpr int AUX_SC1 = 16;
constexpr int AUX_POLL = (int)(16u | 0x80000000u);
constexpr unsigned OOB = 0x80000000u;
constexpr unsigned SPIN_CAP = 1u << 16;

constexpr unsigned WS_P0 = 4096;
constexpr unsigned WS_P1 = WS_P0 + 256u * 256u * 8u;
constexpr unsigned WS_P2 = WS_P1 + 256u * 256u * 8u;
constexpr unsigned WS_BYTES = WS_P2 + 8u * 32u * 160u * 8u;

#define GVCNT 8

__device__ __forceinline__ rsrc_t mk(const void* p, unsigned bytes) {
  return __builtin_amdgcn_make_buffer_rsrc((void*)p, (short)0, (int)bytes, 0x00020000);
}
__device__ __forceinline__ float asf(unsigned u) { return __builtin_bit_cast(float, u); }
__device__ __forceinline__ unsigned asu(float f) { return __builtin_bit_cast(unsigned, f); }
__device__ __forceinline__ float rbf(float f) { return (float)(__bf16)f; }
__device__ __forceinline__ float bflo(unsigned u) { return __builtin_bit_cast(float, u << 16); }
__device__ __forceinline__ float bfhi(unsigned u) { return __builtin_bit_cast(float, u & 0xFFFF0000u); }
__device__ __forceinline__ unsigned pk2(float a, float b) {
  f32x2 v = {a, b};
  return __builtin_bit_cast(unsigned, __builtin_convertvector(v, bfx2));
}
__device__ __forceinline__ f32x4 mfma(v4u a, v4u b, f32x4 c) {
  return __builtin_amdgcn_mfma_f32_16x16x32_bf16(__builtin_bit_cast(bfx8, a), __builtin_bit_cast(bfx8, b), c, 0, 0, 0);
}
__device__ __forceinline__ float dot2(unsigned a, unsigned b, float c) {
  return __builtin_amdgcn_fdot2_f32_bf16(__builtin_bit_cast(bfx2, a), __builtin_bit_cast(bfx2, b), c, false);
}
template <int CTRL> __device__ __forceinline__ float dpp(float v) {
  return __builtin_bit_cast(float, __builtin_amdgcn_mov_dpp(__builtin_bit_cast(int, v), CTRL, 0xF, 0xF, true));
}
__device__ __forceinline__ float sum8(float v) {
  v += dpp<0xB1>(v);
  v += dpp<0x4E>(v);
  v += dpp<0x141>(v);
  return v;
}
__device__ __forceinline__ float sum_hi(float v) {
  v += dpp<0x128>(v);
  v += __shfl_xor(v, 16);
  v += __shfl_xor(v, 32);
  return v;
}
__device__ __forceinline__ float max_hi(float v) {
  v = fmaxf(v, dpp<0x128>(v));
  v = fmaxf(v, __shfl_xor(v, 16));
  v = fmaxf(v, __shfl_xor(v, 32));
  return v;
}
__device__ __forceinline__ v4u bfrag(unsigned dup, const unsigned (&mask)[4]) {
  v4u b;
  b.x = dup & mask[0];
  b.y = dup & mask[1];
  b.z = dup & mask[2];
  b.w = dup & mask[3];
  return b;
}
#define SCHED_FENCE() __builtin_amdgcn_sched_barrier(0)
#define PK 0
#define PW 0
#define STAMP(k) do { if ((k) == 0 || (k) == PK) { SCHED_FENCE(); if ((k) == 0) st0_ = wall_clock64(); if ((k) == PK) stk_ = wall_clock64(); SCHED_FENCE(); } } while (0)

__global__ void __launch_bounds__(512, 2)
fused_decode_kernel(const unsigned short* __restrict__ x, const unsigned short* __restrict__ kc,
                    const unsigned short* __restrict__ vc, const unsigned short* __restrict__ wq,
                    const unsigned short* __restrict__ wo, const unsigned short* __restrict__ w1,
                    const unsigned short* __restrict__ w2, unsigned short* __restrict__ out,
                    unsigned char* ws) {
  __shared__ __attribute__((aligned(16))) float red[16 * 192];
  __shared__ __attribute__((aligned(16))) float red3[4 * 256];
  __shared__ __attribute__((aligned(16))) unsigned qs[128];
  __shared__ __attribute__((aligned(16))) float mls[16];
  __shared__ unsigned flg[4];

  unsigned long long st0_ = 0, stk_ = 0;
  STAMP(0);
  const int tid = threadIdx.x;
  const int lane = tid & 63;
  const int w = __builtin_amdgcn_readfirstlane(tid >> 6);
  const int lg = lane >> 3, lc = lane & 7;
  const int col = lane & 15, rq = lane >> 4;
  const int b = blockIdx.x;
  const int h = 4 * (b & 7) + (b >> 6);
  const int s = (b >> 3) & 7;
  const int wg = h * 8 + s;

  const rsrc_t rsX = mk(x, 1280);
  const rsrc_t rsWs = mk(ws, WS_BYTES);
  const size_t kvoff = ((size_t)h * 32768 + 28672 + 512 * s + 64 * w) * 192;
  const rsrc_t rsK = mk(kc + kvoff, 64 * 384);
  const rsrc_t rsV = mk(vc + kvoff, 64 * 384);

  const unsigned epoch = __builtin_amdgcn_raw_buffer_load_b32(rsWs, 0, 0, AUX_SC1);
  if (tid < 4) flg[tid] = 0u;

  unsigned mask[4];
  {
    const unsigned mm = (lc & 1) ? 0xFFFF0000u : 0x0000FFFFu;
#pragma unroll
    for (int i = 0; i < 4; ++i) mask[i] = ((lc >> 1) == i) ? mm : 0u;
  }
  const bool dvalid = (rq >> 1) == (col >> 3);
  const unsigned vkv = (unsigned)(lg * 384 + lc * 16);
  const int t4 = tid - 256;
  const unsigned p1_off = (t4 >= 0 && t4 < 97) ? WS_P1 + (unsigned)(wg * 256 + 2 * t4) * 8u : OOB;
  const int r3 = 4 * (t4 >> 6) + (t4 & 3), c3 = (t4 >> 2) & 15;
  const unsigned p2_off =
      (t4 >= 0 && r3 < 10) ? WS_P2 + (unsigned)((s * 32 + h) * 160 + (8 * r3 + (c3 & 7)) * 2 + (c3 >> 3)) * 8u : OOB;
  v4u kv[8][3], vv[8][3];

  if (w < 2) {
    __builtin_amdgcn_s_setprio(3);
    const v4u xs0 = __builtin_amdgcn_raw_buffer_load_b128(rsX, 20 * lane, 0, 0);
    const unsigned xs1 = __builtin_amdgcn_raw_buffer_load_b32(rsX, 20 * lane + 16, 0, 0);
    const v4u xr0 = __builtin_amdgcn_raw_buffer_load_b128(rsX, 160 * s + 20 * lg, 0, 0);
    const unsigned xr1 = __builtin_amdgcn_raw_buffer_load_b32(rsX, 160 * s + 20 * lg + 16, 0, 0);
    const unsigned short* pa = wq + (size_t)(80 * s) * 6144 + 192 * h + 128 * w;
    const unsigned short* pb = w ? w1 + (size_t)(80 * s) * 1536 + 48 * h : pa + 64;
    const int sb = w ? 3072 : 12288;
    const rsrc_t rsA = mk(pa, 79 * 12288 + 128);
    const rsrc_t rsB = mk(pb, w ? 79 * 3072 + 96 : 79 * 12288 + 128);
    const unsigned voa = (unsigned)(lg * 122880 + lc * 16);
    const unsigned vob = (w && lc >= 6) ? OOB : (unsigned)(lg * 10 * sb + lc * 16);
    v4u wa[10], wb[10];
#pragma unroll
    for (int g = 0; g < 10; ++g) {
      wa[g] = __builtin_amdgcn_raw_buffer_load_b128(rsA, voa, g * 12288, AUX_NT);
      wb[g] = __builtin_amdgcn_raw_buffer_load_b128(rsB, vob, g * sb, AUX_NT);
      SCHED_FENCE();
    }
    const unsigned p0_off = dvalid ? WS_P0 + (unsigned)(wg * 256 + 128 * w + 64 * (rq >> 1) + 8 * (col & 7) + 4 * (rq & 1)) * 8u : OOB;
    SCHED_FENCE();
    __syncthreads();
    SCHED_FENCE();
    float mb, sd;
    {
      float e[10];
      e[0] = bflo(xs0.x); e[1] = bfhi(xs0.x); e[2] = bflo(xs0.y); e[3] = bfhi(xs0.y);
      e[4] = bflo(xs0.z); e[5] = bfhi(xs0.z); e[6] = bflo(xs0.w); e[7] = bfhi(xs0.w);
      e[8] = bflo(xs1);   e[9] = bfhi(xs1);
      float t = 0.f;
#pragma unroll
      for (int i = 0; i < 10; ++i) t += e[i];
      t = sum_hi(sum8(t));
      const float mean = t * (1.0f / 640.0f);
      float q2 = 0.f;
#pragma unroll
      for (int i = 0; i < 10; ++i) { const float d = e[i] - mean; q2 += d * d; }
      q2 = sum_hi(sum8(q2));
      const float var = rbf(q2 * (1.0f / 640.0f));
      mb = rbf(mean);
      sd = rbf(sqrtf(rbf(var + 1e-5f)));
    }
    const unsigned xr[5] = {xr0.x, xr0.y, xr0.z, xr0.w, xr1};
    const f32x4 zero = {0.f, 0.f, 0.f, 0.f};
    f32x4 a0 = zero, a1 = zero, b0 = zero, b1 = zero;
#pragma unroll
    for (int g = 0; g < 5; ++g) {
      const float n0 = rbf(bflo(xr[g]) - mb) / sd, n1 = rbf(bfhi(xr[g]) - mb) / sd;
      const v4u f0 = bfrag(pk2(n0, n0), mask), f1 = bfrag(pk2(n1, n1), mask);
      a0 = mfma(f0, wa[2 * g], a0);
      b0 = mfma(f0, wb[2 * g], b0);
      a1 = mfma(f1, wa[2 * g + 1], a1);
      b1 = mfma(f1, wb[2 * g + 1], b1);
    }
    a0 += a1;
    b0 += b1;
    const unsigned tag = (epoch + 1u) | 0x80000000u;
    {
      const bool p = rq >= 2;
      float r[4];
#pragma unroll
      for (int i = 0; i < 4; ++i) r[i] = (p ? b0[i] : a0[i]) + __shfl_xor(p ? a0[i] : b0[i], 40);
      const v4u g0 = {asu(r[0]), tag, asu(r[1]), tag}, g1 = {asu(r[2]), tag, asu(r[3]), tag};
      __builtin_amdgcn_raw_buffer_store_b128(g0, rsWs, p0_off, 0, AUX_SC1);
      __builtin_amdgcn_raw_buffer_store_b128(g1, rsWs, p0_off + 16, 0, AUX_SC1);
    }
    STAMP(1);
    __builtin_amdgcn_s_setprio(0);
    SCHED_FENCE();
#pragma unroll
    for (int g = 0; g < 2; ++g)
#pragma unroll
      for (int j = 0; j < 3; ++j) kv[g][j] = __builtin_amdgcn_raw_buffer_load_b128(rsK, vkv + j * 128, g * 3072, AUX_NT);
  } else {
#pragma unroll
    for (int g = 0; g < 2; ++g)
#pragma unroll
      for (int j = 0; j < 3; ++j) kv[g][j] = __builtin_amdgcn_raw_buffer_load_b128(rsK, vkv + j * 128, g * 3072, AUX_NT);
    SCHED_FENCE();
    __syncthreads();
  }
  SCHED_FENCE();
#pragma unroll
  for (int g = 2; g < 8; ++g) {
#pragma unroll
    for (int j = 0; j < 3; ++j) kv[g][j] = __builtin_amdgcn_raw_buffer_load_b128(rsK, vkv + j * 128, g * 3072, AUX_NT);
    SCHED_FENCE();
  }
#pragma unroll
  for (int g = 0; g < 8; ++g) {
#pragma unroll
    for (int j = 0; j < 3; ++j) vv[g][j] = __builtin_amdgcn_raw_buffer_load_b128(rsV, vkv + j * 128, g * 3072, AUX_NT);
    SCHED_FENCE();
  }
  STAMP(9);
  if (lane == 0) __hip_atomic_fetch_add(&flg[2], 1u, __ATOMIC_RELAXED, __HIP_MEMORY_SCOPE_WORKGROUP);
  if (w == 2 || w == 3) {
    const int t2 = tid - 128;
    const unsigned tag = (epoch + 1u) | 0x80000000u;
    const unsigned gbase = WS_P0 + (unsigned)(h * 8 * 256 + 2 * t2) * 8u;
    v4u g[8];
#pragma unroll 1
    for (unsigned spins = 0; spins < SPIN_CAP; ++spins) {
      asm volatile("" ::: "memory");
#pragma unroll
      for (int i = 0; i < 8; ++i) g[i] = __builtin_amdgcn_raw_buffer_load_b128(rsWs, gbase, i * 2048, AUX_SC1);
      bool ok = true;
#pragma unroll
      for (int i = 0; i < 8; ++i) ok &= (g[i].y == tag) & (g[i].w == tag);
      if (__all(ok)) break;
      __builtin_amdgcn_s_sleep(1);
    }
    float t0 = 0.f, t1 = 0.f;
#pragma unroll
    for (int i = 0; i < 8; ++i) {
      t0 += asf(g[i].x);
      t1 += asf(g[i].z);
    }
    if (t2 >= 96) { t0 = fmaxf(t0, 0.f); t1 = fmaxf(t1, 0.f); }
    qs[t2] = pk2(t0, t1);
    __builtin_amdgcn_fence(__ATOMIC_RELEASE, "workgroup");
    if (lane == 0) __hip_atomic_store(&flg[w - 2], 1u, __ATOMIC_RELAXED, __HIP_MEMORY_SCOPE_WORKGROUP);
    STAMP(2);
  }
#pragma unroll 1
  for (unsigned spins = 0; spins < (SPIN_CAP << 6); ++spins) {
    const unsigned f0 = __hip_atomic_load(&flg[0], __ATOMIC_RELAXED, __HIP_MEMORY_SCOPE_WORKGROUP);
    const unsigned f1 = __hip_atomic_load(&flg[1], __ATOMIC_RELAXED, __HIP_MEMORY_SCOPE_WORKGROUP);
    if (f0 & f1) break;
    __builtin_amdgcn_s_sleep(1);
  }
  __builtin_amdgcn_fence(__ATOMIC_ACQUIRE, "workgroup");
  v4u qf[3];
#pragma unroll
  for (int j = 0; j < 3; ++j) qf[j] = *(const v4u*)(qs + 32 * j + 4 * lc);

  float sc[8];
#pragma unroll
  for (int g = 0; g < 8; ++g) {
    float a = 0.f;
#pragma unroll
    for (int j = 0; j < 3; ++j) {
      a = dot2(kv[g][j].x, qf[j].x, a);
      a = dot2(kv[g][j].y, qf[j].y, a);
      a = dot2(kv[g][j].z, qf[j].z, a);
      a = dot2(kv[g][j].w, qf[j].w, a);
    }
    a = sum8(a);
    sc[g] = rbf(rbf(a) * 0.07216878364870322f);
  }
  SCHED_FENCE();
  STAMP(3);
  unsigned pd[8];
  {
    float m = fmaxf(fmaxf(fmaxf(sc[0], sc[1]), fmaxf(sc[2], sc[3])), fmaxf(fmaxf(sc[4], sc[5]), fmaxf(sc[6], sc[7])));
    m = max_hi(m);
    float l = 0.f;
#pragma unroll
    for (int g = 0; g < 8; ++g) {
      const float p = __builtin_amdgcn_exp2f((sc[g] - m) * 1.4426950408889634f);
      l += p;
      pd[g] = pk2(p, p);
    }
    l = sum_hi(l);
    if (lane == 0) { mls[w] = m; mls[8 + w] = l; }
  }
  SCHED_FENCE();

  {
    f32x4 oacc[3] = {{0.f, 0.f, 0.f, 0.f}, {0.f, 0.f, 0.f, 0.f}, {0.f, 0.f, 0.f, 0.f}};
#pragma unroll
    for (int g = 0; g < 8; ++g) {
      const v4u bf = bfrag(pd[g], mask);
#pragma unroll
      for (int j = 0; j < 3; ++j) oacc[j] = mfma(bf, vv[g][j], oacc[j]);
      SCHED_FENCE();
    }
    float* const myred = red + (w * 2 + (rq >> 1)) * 192 + 8 * (col & 7) + 4 * (rq & 1);
    if (dvalid) {
#pragma unroll
      for (int j = 0; j < 3; ++j) *(f32x4*)(myred + j * 64) = oacc[j];
    }
  }
  STAMP(10);
  const unsigned tag = (epoch + 1u) | 0x80000000u;
  if (w < 4) {
#pragma unroll 1
    for (unsigned spins = 0; spins < (SPIN_CAP << 6); ++spins) {
      if (__hip_atomic_load(&flg[2], __ATOMIC_RELAXED, __HIP_MEMORY_SCOPE_WORKGROUP) >= GVCNT) break;
      __builtin_amdgcn_s_sleep(1);
    }
    const rsrc_t rsWo = mk(wo + (size_t)(192 * h + 48 * w) * 640 + 80 * s, 47 * 1280 + 160);
    const rsrc_t rsW2 = mk(w2 + (size_t)(48 * h + 12 * w) * 640 + 80 * s, 11 * 1280 + 160);
    const unsigned vo3 = col < 10 ? (unsigned)(rq * 15360 + col * 16) : OOB;
    const unsigned vo4 = col < 10 ? (unsigned)(rq * 3840 + col * 16) : OOB;
    v4u gv[15];
#pragma unroll
    for (int g = 0; g < 3; ++g) gv[12 + g] = __builtin_amdgcn_raw_buffer_load_b128(rsW2, vo4, g * 1280, AUX_NT);
#pragma unroll
    for (int g = 0; g < 12; ++g) gv[g] = __builtin_amdgcn_raw_buffer_load_b128(rsWo, vo3, g * 1280, AUX_NT);
    SCHED_FENCE();
    STAMP(15);
    __syncthreads();
    STAMP(4);
    const unsigned gbase = WS_P1 + (unsigned)((h * 8 + (col & 7)) * 256 + 48 * w + 12 * rq + 6 * (col >> 3)) * 8u;
    const unsigned mlo = WS_P1 + (unsigned)((h * 8 + (col & 7)) * 256 + 192) * 8u;
    v4u g[3], gm;
#pragma unroll 1
    for (unsigned spins = 0; spins < SPIN_CAP; ++spins) {
      asm volatile("" ::: "memory");
#pragma unroll
      for (int i = 0; i < 3; ++i) g[i] = __builtin_amdgcn_raw_buffer_load_b128(rsWs, gbase + i * 16, 0, AUX_SC1);
      gm = __builtin_amdgcn_raw_buffer_load_b128(rsWs, mlo, 0, AUX_SC1);
      bool ok = (gm.y == tag) & (gm.w == tag);
#pragma unroll
      for (int i = 0; i < 3; ++i) ok &= (g[i].y == tag) & (g[i].w == tag);
      if (__all(ok)) break;
      __builtin_amdgcn_s_sleep(1);
    }
    STAMP(5);
    unsigned od[6];
    {
      const float M = asf(gm.x);
      float Mg = fmaxf(M, dpp<0xB1>(M));
      Mg = fmaxf(Mg, dpp<0x4E>(Mg));
      Mg = fmaxf(Mg, dpp<0x141>(Mg));
      const float f = __builtin_amdgcn_exp2f((M - Mg) * 1.4426950408889634f);
      const float den = sum8(f * asf(gm.z));
#pragma unroll
      for (int i = 0; i < 3; ++i) {
        const float n0 = sum8(f * asf(g[i].x)) / den, n1 = sum8(f * asf(g[i].z)) / den;
        od[2 * i] = pk2(n0, n0);
        od[2 * i + 1] = pk2(n1, n1);
      }
    }
    unsigned mlo8[4], mhi8[4];
#pragma unroll
    for (int i = 0; i < 4; ++i) { mlo8[i] = col < 8 ? mask[i] : 0u; mhi8[i] = col < 8 ? 0u : mask[i]; }
    const unsigned short* hb = (const unsigned short*)(qs + 96) + 12 * w + 3 * rq;
    f32x4 ya = {0.f, 0.f, 0.f, 0.f}, yb = {0.f, 0.f, 0.f, 0.f}, za = {0.f, 0.f, 0.f, 0.f};
#pragma unroll
    for (int i = 0; i < 3; ++i) za = mfma(gv[12 + i], bfrag((unsigned)hb[i] * 0x10001u, mhi8), za);
#pragma unroll
    for (int i = 0; i < 6; ++i) {
      ya = mfma(gv[i], bfrag(od[i], mlo8), ya);
      yb = mfma(gv[6 + i], bfrag(od[i], mhi8), yb);
    }
    f32x4 r;
#pragma unroll
    for (int i = 0; i < 4; ++i) r[i] = ya[i] + za[i] + dpp<0x128>(yb[i]);
    *(f32x4*)(red3 + w * 256 + lane * 4) = r;
    STAMP(6);
    __syncthreads();
    STAMP(7);
  } else {
    __syncthreads();
    if (w < 6) {
      float t0 = 0.f, t1 = 0.f;
      const f32x4 m0 = *(const f32x4*)mls, m1 = *(const f32x4*)(mls + 4);
      const f32x4 l0 = *(const f32x4*)(mls + 8), l1 = *(const f32x4*)(mls + 12);
      const float mw[8] = {m0.x, m0.y, m0.z, m0.w, m1.x, m1.y, m1.z, m1.w};
      const float lw[8] = {l0.x, l0.y, l0.z, l0.w, l1.x, l1.y, l1.z, l1.w};
      const float M = fmaxf(fmaxf(fmaxf(mw[0], mw[1]), fmaxf(mw[2], mw[3])), fmaxf(fmaxf(mw[4], mw[5]), fmaxf(mw[6], mw[7])));
      float L = 0.f;
      const int sl = t4 < 96 ? 2 * t4 : 0;
#pragma unroll
      for (int i = 0; i < 8; ++i) {
        const float f = __builtin_amdgcn_exp2f((mw[i] - M) * 1.4426950408889634f);
        const f32x2 va = *(const f32x2*)(red + (2 * i) * 192 + sl);
        const f32x2 vb = *(const f32x2*)(red + (2 * i + 1) * 192 + sl);
        t0 += f * (va.x + vb.x);
        t1 += f * (va.y + vb.y);
        L += f * lw[i];
      }
      if (t4 >= 96) { t0 = M; t1 = L; }
      const v4u g = {asu(t0), tag, asu(t1), tag};
      __builtin_amdgcn_raw_buffer_store_b128(g, rsWs, p1_off, 0, AUX_SC1);
      STAMP(12);
      STAMP(13);
    }
    __syncthreads();
    if (w < 7) {
      const float t = (red3[t4] + red3[256 + t4]) + (red3[512 + t4] + red3[768 + t4]);
      const v2u g = {asu(t), tag};
      __builtin_amdgcn_raw_buffer_store_b64(g, rsWs, p2_off, 0, AUX_SC1);
    } else if (h < 20) {
      const int n0 = 4 * h + 2 * (lane & 1);
      const unsigned xin = __builtin_amdgcn_raw_buffer_load_b32(rsX, (unsigned)(80 * s + n0) * 2u, 0, 0);
      const unsigned gbase = WS_P2 + (unsigned)((s * 32 + (lane >> 1)) * 160 + n0 * 2) * 8u;
      v4u g[2];
#pragma unroll 1
      for (unsigned spins = 0; spins < SPIN_CAP; ++spins) {
        asm volatile("" ::: "memory");
#pragma unroll
        for (int i = 0; i < 2; ++i) g[i] = __builtin_amdgcn_raw_buffer_load_b128(rsWs, gbase + i * 16, 0, AUX_SC1);
        const bool ok = (g[0].y == tag) & (g[0].w == tag) & (g[1].y == tag) & (g[1].w == tag);
        if (__all(ok)) break;
        __builtin_amdgcn_s_sleep(1);
      }
      STAMP(8);
      float v[4] = {asf(g[0].x), asf(g[0].z), asf(g[1].x), asf(g[1].z)};
#pragma unroll
      for (int i = 0; i < 4; ++i) {
        float t = v[i];
        t += dpp<0x4E>(t);
        t += __shfl_xor(t, 4);
        t = sum_hi(t);
        v[i] = t;
      }
      if (lane < 2) {
        const float r0 = rbf(rbf(bflo(xin) + rbf(v[0])) + rbf(v[1]));
        const float r1 = rbf(rbf(bfhi(xin) + rbf(v[2])) + rbf(v[3]));
        *(unsigned*)(out + 80 * s + n0) = pk2(r0, r1);
      }
      if (wg == 0 && lane == 0) __builtin_amdgcn_raw_buffer_store_b32(epoch + 1u, rsWs, 0, 0, AUX_SC1);
    }
  }
  STAMP(11);
  if (w == PW && true && stk_ >= st0_ && stk_ - st0_ < 20000ull) {
    const unsigned long long target = st0_ + 8000ull + 1ull * (stk_ - st0_);
#pragma unroll 1
    for (unsigned spins = 0; spins < (1u << 20); ++spins) {
      if (wall_clock64() >= target) break;
      __builtin_amdgcn_s_sleep(1);
    }
  }

}

}

extern "C" void kernel_launch(void* const* d_in, const int* in_sizes, int n_in, void* d_out, int out_size, void* d_ws,
                              size_t ws_size, hipStream_t stream) {
  (void)in_sizes; (void)n_in; (void)out_size;
  if (ws_size < WS_BYTES) return;
  fused_decode_kernel<<<dim3(256), dim3(512), 0, stream>>>(
      (const unsigned short*)d_in[0], (const unsigned short*)d_in[1], (const unsigned short*)d_in[2],
      (const unsigned short*)d_in[3], (const unsigned short*)d_in[4], (const unsigned short*)d_in[6],
      (const unsigned short*)d_in[7], (unsigned short*)d_out, (unsigned char*)d_ws);
}
```

```cpp
#include <hip/hip_runtime.h>
#include <hip/hip_bf16.h>

namespace {

typedef unsigned v4u __attribute__((ext_vector_type(4)));
typedef unsigned v2u __attribute__((ext_vector_type(2)));
typedef float f32x4 __attribute__((ext_vector_type(4)));
typedef float f32x2 __attribute__((ext_vector_type(2)));
typedef __bf16 bfx8 __attribute__((ext_vector_type(8)));
typedef __bf16 bfx2 __attribute__((ext_vector_type(2)));
typedef __amdgpu_buffer_rsrc_t rsrc_t;

constexpr int AUX_NT = 2;
constexpr int AUX_SC1 = 16;
constexpr int AUX_POLL = (int)(16u | 0x80000000u);
constexpr unsigned OOB = 0x80000000u;
constexpr unsigned SPIN_CAP = 1u << 16;

constexpr unsigned WS_P0 = 4096;
constexpr unsigned WS_P1 = WS_P0 + 256u * 256u * 8u;
constexpr unsigned WS_P2 = WS_P1 + 256u * 256u * 8u;
constexpr unsigned WS_BYTES = WS_P2 + 8u * 32u * 160u * 8u;

#define GVCNT 8

__device__ __forceinline__ rsrc_t mk(const void* p, unsigned bytes) {
  return __builtin_amdgcn_make_buffer_rsrc((void*)p, (short)0, (int)bytes, 0x00020000);
}
__device__ __forceinline__ float asf(unsigned u) { return __builtin_bit_cast(float, u); }
__device__ __forceinline__ unsigned asu(float f) { return __builtin_bit_cast(unsigned, f); }
__device__ __forceinline__ float rbf(float f) { return (float)(__bf16)f; }
__device__ __forceinline__ float bflo(unsigned u) { return __builtin_bit_cast(float, u << 16); }
__device__ __forceinline__ float bfhi(unsigned u) { return __builtin_bit_cast(float, u & 0xFFFF0000u); }
__device__ __forceinline__ unsigned pk2(float a, float b) {
  f32x2 v = {a, b};
  return __builtin_bit_cast(unsigned, __builtin_convertvector(v, bfx2));
}
__device__ __forceinline__ f32x4 mfma(v4u a, v4u b, f32x4 c) {
  return __builtin_amdgcn_mfma_f32_16x16x32_bf16(__builtin_bit_cast(bfx8, a), __builtin_bit_cast(bfx8, b), c, 0, 0, 0);
}
__device__ __forceinline__ float dot2(unsigned a, unsigned b, float c) {
  return __builtin_amdgcn_fdot2_f32_bf16(__builtin_bit_cast(bfx2, a), __builtin_bit_cast(bfx2, b), c, false);
}
template <int CTRL> __device__ __forceinline__ float dpp(float v) {
  return __builtin_bit_cast(float, __builtin_amdgcn_mov_dpp(__builtin_bit_cast(int, v), CTRL, 0xF, 0xF, true));
}
__device__ __forceinline__ float sum8(float v) {
  v += dpp<0xB1>(v);
  v += dpp<0x4E>(v);
  v += dpp<0x141>(v);
  return v;
}
__device__ __forceinline__ float sum_hi(float v) {
  v += dpp<0x128>(v);
  v += __shfl_xor(v, 16);
  v += __shfl_xor(v, 32);
  return v;
}
__device__ __forceinline__ float max_hi(float v) {
  v = fmaxf(v, dpp<0x128>(v));
  v = fmaxf(v, __shfl_xor(v, 16));
  v = fmaxf(v, __shfl_xor(v, 32));
  return v;
}
__device__ __forceinline__ v4u bfrag(unsigned dup, const unsigned (&mask)[4]) {
  v4u b;
  b.x = dup & mask[0];
  b.y = dup & mask[1];
  b.z = dup & mask[2];
  b.w = dup & mask[3];
  return b;
}
#define SCHED_FENCE() __builtin_amdgcn_sched_barrier(0)
constexpr unsigned WS_TS = 8u << 20;
#define STAMP(k) do { if ((k) == 0 && 0) { SCHED_FENCE(); const unsigned long long t_ = wall_clock64(); SCHED_FENCE(); if (blockIdx.x == 0 && threadIdx.x == 0) *(unsigned long long*)(ws + WS_TS) = t_; } } while (0)
__global__ void __launch_bounds__(64) ka_tiny(unsigned char* ws) {
  const unsigned long long t_ = wall_clock64();
  if (threadIdx.x == 0) *(unsigned long long*)(ws + WS_TS) = t_;
}
__global__ void __launch_bounds__(512, 2) ka_big(unsigned char* ws) {
  __shared__ float lds[4300];
  const unsigned long long t_ = wall_clock64();
  asm volatile("" ::: "v240");
  lds[threadIdx.x] = 1.f;
  __syncthreads();
  if (blockIdx.x == 0 && threadIdx.x == 0) *(unsigned long long*)(ws + WS_TS) = t_ + (lds[5] != 1.f);
}
__device__ __forceinline__ void waitts(const unsigned char* ws) {
  const unsigned long long t0 = *(const volatile unsigned long long*)(ws + WS_TS);
  const unsigned long long now = wall_clock64();
  if (now >= t0 && now - t0 < 15000ull) {
#pragma unroll 1
    for (unsigned spins = 0; spins < (1u << 20); ++spins) {
      if (wall_clock64() >= t0 + 15000ull) break;
      __builtin_amdgcn_s_sleep(1);
    }
  }
}
__global__ void __launch_bounds__(64) kc_tiny(unsigned char* ws) { waitts(ws); }
__global__ void __launch_bounds__(512, 2) kc_big(unsigned char* ws) {
  __shared__ float lds[4300];
  asm volatile("" ::: "v240");
  lds[threadIdx.x] = 1.f;
  __syncthreads();
  waitts(ws);
  if (lds[5] != 1.f) ws[WS_TS + 64] = 1;
}

__global__ void __launch_bounds__(512, 2)
fused_decode_kernel(const unsigned short* __restrict__ x, const unsigned short* __restrict__ kc,
                    const unsigned short* __restrict__ vc, const unsigned short* __restrict__ wq,
                    const unsigned short* __restrict__ wo, const unsigned short* __restrict__ w1,
                    const unsigned short* __restrict__ w2, unsigned short* __restrict__ out,
                    unsigned char* ws) {
  __shared__ __attribute__((aligned(16))) float red[16 * 192];
  __shared__ __attribute__((aligned(16))) float red3[4 * 256];
  __shared__ __attribute__((aligned(16))) unsigned qs[128];
  __shared__ __attribute__((aligned(16))) float mls[16];
  __shared__ unsigned flg[4];

  STAMP(0);
  const int tid = threadIdx.x;
  const int lane = tid & 63;
  const int w = __builtin_amdgcn_readfirstlane(tid >> 6);
  const int lg = lane >> 3, lc = lane & 7;
  const int col = lane & 15, rq = lane >> 4;
  const int b = blockIdx.x;
  const int h = 4 * (b & 7) + (b >> 6);
  const int s = (b >> 3) & 7;
  const int wg = h * 8 + s;

  const rsrc_t rsX = mk(x, 1280);
  const rsrc_t rsWs = mk(ws, WS_BYTES);
  const size_t kvoff = ((size_t)h * 32768 + 28672 + 512 * s + 64 * w) * 192;
  const rsrc_t rsK = mk(kc + kvoff, 64 * 384);
  const rsrc_t rsV = mk(vc + kvoff, 64 * 384);

  const unsigned epoch = __builtin_amdgcn_raw_buffer_load_b32(rsWs, 0, 0, AUX_SC1);
  if (tid < 4) flg[tid] = 0u;

  unsigned mask[4];
  {
    const unsigned mm = (lc & 1) ? 0xFFFF0000u : 0x0000FFFFu;
#pragma unroll
    for (int i = 0; i < 4; ++i) mask[i] = ((lc >> 1) == i) ? mm : 0u;
  }
  const bool dvalid = (rq >> 1) == (col >> 3);
  const unsigned vkv = (unsigned)(lg * 384 + lc * 16);
  const int t4 = tid - 256;
  const unsigned p1_off = (t4 >= 0 && t4 < 97) ? WS_P1 + (unsigned)(wg * 256 + 2 * t4) * 8u : OOB;
  const int r3 = 4 * (t4 >> 6) + (t4 & 3), c3 = (t4 >> 2) & 15;
  const unsigned p2_off =
      (t4 >= 0 && r3 < 10) ? WS_P2 + (unsigned)((s * 32 + h) * 160 + (8 * r3 + (c3 & 7)) * 2 + (c3 >> 3)) * 8u : OOB;
  v4u kv[8][3], vv[8][3];

  if (w < 2) {
    __builtin_amdgcn_s_setprio(3);
    const v4u xs0 = __builtin_amdgcn_raw_buffer_load_b128(rsX, 20 * lane, 0, 0);
    const unsigned xs1 = __builtin_amdgcn_raw_buffer_load_b32(rsX, 20 * lane + 16, 0, 0);
    const v4u xr0 = __builtin_amdgcn_raw_buffer_load_b128(rsX, 160 * s + 20 * lg, 0, 0);
    const unsigned xr1 = __builtin_amdgcn_raw_buffer_load_b32(rsX, 160 * s + 20 * lg + 16, 0, 0);
    const unsigned short* pa = wq + (size_t)(80 * s) * 6144 + 192 * h + 128 * w;
    const unsigned short* pb = w ? w1 + (size_t)(80 * s) * 1536 + 48 * h : pa + 64;
    const int sb = w ? 3072 : 12288;
    const rsrc_t rsA = mk(pa, 79 * 12288 + 128);
    const rsrc_t rsB = mk(pb, w ? 79 * 3072 + 96 : 79 * 12288 + 128);
    const unsigned voa = (unsigned)(lg * 122880 + lc * 16);
    const unsigned vob = (w && lc >= 6) ? OOB : (unsigned)(lg * 10 * sb + lc * 16);
    v4u wa[10], wb[10];
#pragma unroll
    for (int g = 0; g < 10; ++g) {
      wa[g] = __builtin_amdgcn_raw_buffer_load_b128(rsA, voa, g * 12288, AUX_NT);
      wb[g] = __builtin_amdgcn_raw_buffer_load_b128(rsB, vob, g * sb, AUX_NT);
      SCHED_FENCE();
    }
    const unsigned p0_off = dvalid ? WS_P0 + (unsigned)(wg * 256 + 128 * w + 64 * (rq >> 1) + 8 * (col & 7) + 4 * (rq & 1)) * 8u : OOB;
    SCHED_FENCE();
    __syncthreads();
    SCHED_FENCE();
    float mb, sd;
    {
      float e[10];
      e[0] = bflo(xs0.x); e[1] = bfhi(xs0.x); e[2] = bflo(xs0.y); e[3] = bfhi(xs0.y);
      e[4] = bflo(xs0.z); e[5] = bfhi(xs0.z); e[6] = bflo(xs0.w); e[7] = bfhi(xs0.w);
      e[8] = bflo(xs1);   e[9] = bfhi(xs1);
      float t = 0.f;
#pragma unroll
      for (int i = 0; i < 10; ++i) t += e[i];
      t = sum_hi(sum8(t));
      const float mean = t * (1.0f / 640.0f);
      float q2 = 0.f;
#pragma unroll
      for (int i = 0; i < 10; ++i) { const float d = e[i] - mean; q2 += d * d; }
      q2 = sum_hi(sum8(q2));
      const float var = rbf(q2 * (1.0f / 640.0f));
      mb = rbf(mean);
      sd = rbf(sqrtf(rbf(var + 1e-5f)));
    }
    const unsigned xr[5] = {xr0.x, xr0.y, xr0.z, xr0.w, xr1};
    const f32x4 zero = {0.f, 0.f, 0.f, 0.f};
    f32x4 a0 = zero, a1 = zero, b0 = zero, b1 = zero;
#pragma unroll
    for (int g = 0; g < 5; ++g) {
      const float n0 = rbf(bflo(xr[g]) - mb) / sd, n1 = rbf(bfhi(xr[g]) - mb) / sd;
      const v4u f0 = bfrag(pk2(n0, n0), mask), f1 = bfrag(pk2(n1, n1), mask);
      a0 = mfma(f0, wa[2 * g], a0);
      b0 = mfma(f0, wb[2 * g], b0);
      a1 = mfma(f1, wa[2 * g + 1], a1);
      b1 = mfma(f1, wb[2 * g + 1], b1);
    }
    a0 += a1;
    b0 += b1;
    const unsigned tag = (epoch + 1u) | 0x80000000u;
    {
      const bool p = rq >= 2;
      float r[4];
#pragma unroll
      for (int i = 0; i < 4; ++i) r[i] = (p ? b0[i] : a0[i]) + __shfl_xor(p ? a0[i] : b0[i], 40);
      const v4u g0 = {asu(r[0]), tag, asu(r[1]), tag}, g1 = {asu(r[2]), tag, asu(r[3]), tag};
      __builtin_amdgcn_raw_buffer_store_b128(g0, rsWs, p0_off, 0, AUX_SC1);
      __builtin_amdgcn_raw_buffer_store_b128(g1, rsWs, p0_off + 16, 0, AUX_SC1);
    }
    STAMP(1);
    __builtin_amdgcn_s_setprio(0);
    SCHED_FENCE();
#pragma unroll
    for (int g = 0; g < 2; ++g)
#pragma unroll
      for (int j = 0; j < 3; ++j) kv[g][j] = __builtin_amdgcn_raw_buffer_load_b128(rsK, vkv + j * 128, g * 3072, AUX_NT);
  } else {
#pragma unroll
    for (int g = 0; g < 2; ++g)
#pragma unroll
      for (int j = 0; j < 3; ++j) kv[g][j] = __builtin_amdgcn_raw_buffer_load_b128(rsK, vkv + j * 128, g * 3072, AUX_NT);
    SCHED_FENCE();
    __syncthreads();
  }
  SCHED_FENCE();
#pragma unroll
  for (int g = 2; g < 8; ++g) {
#pragma unroll
    for (int j = 0; j < 3; ++j) kv[g][j] = __builtin_amdgcn_raw_buffer_load_b128(rsK, vkv + j * 128, g * 3072, AUX_NT);
    SCHED_FENCE();
  }
#pragma unroll
  for (int g = 0; g < 8; ++g) {
#pragma unroll
    for (int j = 0; j < 3; ++j) vv[g][j] = __builtin_amdgcn_raw_buffer_load_b128(rsV, vkv + j * 128, g * 3072, AUX_NT);
    SCHED_FENCE();
  }
  STAMP(9);
  if (lane == 0) __hip_atomic_fetch_add(&flg[2], 1u, __ATOMIC_RELAXED, __HIP_MEMORY_SCOPE_WORKGROUP);
  if (w == 2 || w == 3) {
    const int t2 = tid - 128;
    const unsigned tag = (epoch + 1u) | 0x80000000u;
    const unsigned gbase = WS_P0 + (unsigned)(h * 8 * 256 + 2 * t2) * 8u;
    v4u g[8];
#pragma unroll 1
    for (unsigned spins = 0; spins < SPIN_CAP; ++spins) {
      asm volatile("" ::: "memory");
#pragma unroll
      for (int i = 0; i < 8; ++i) g[i] = __builtin_amdgcn_raw_buffer_load_b128(rsWs, gbase, i * 2048, AUX_SC1);
      bool ok = true;
#pragma unroll
      for (int i = 0; i < 8; ++i) ok &= (g[i].y == tag) & (g[i].w == tag);
      if (__all(ok)) break;
      __builtin_amdgcn_s_sleep(1);
    }
    float t0 = 0.f, t1 = 0.f;
#pragma unroll
    for (int i = 0; i < 8; ++i) {
      t0 += asf(g[i].x);
      t1 += asf(g[i].z);
    }
    if (t2 >= 96) { t0 = fmaxf(t0, 0.f); t1 = fmaxf(t1, 0.f); }
    qs[t2] = pk2(t0, t1);
    __builtin_amdgcn_fence(__ATOMIC_RELEASE, "workgroup");
    if (lane == 0) __hip_atomic_store(&flg[w - 2], 1u, __ATOMIC_RELAXED, __HIP_MEMORY_SCOPE_WORKGROUP);
    STAMP(2);
  }
#pragma unroll 1
  for (unsigned spins = 0; spins < (SPIN_CAP << 6); ++spins) {
    const unsigned f0 = __hip_atomic_load(&flg[0], __ATOMIC_RELAXED, __HIP_MEMORY_SCOPE_WORKGROUP);
    const unsigned f1 = __hip_atomic_load(&flg[1], __ATOMIC_RELAXED, __HIP_MEMORY_SCOPE_WORKGROUP);
    if (f0 & f1) break;
    __builtin_amdgcn_s_sleep(1);
  }
  __builtin_amdgcn_fence(__ATOMIC_ACQUIRE, "workgroup");
  v4u qf[3];
#pragma unroll
  for (int j = 0; j < 3; ++j) qf[j] = *(const v4u*)(qs + 32 * j + 4 * lc);

  float sc[8];
#pragma unroll
  for (int g = 0; g < 8; ++g) {
    float a = 0.f;
#pragma unroll
    for (int j = 0; j < 3; ++j) {
      a = dot2(kv[g][j].x, qf[j].x, a);
      a = dot2(kv[g][j].y, qf[j].y, a);
      a = dot2(kv[g][j].z, qf[j].z, a);
      a = dot2(kv[g][j].w, qf[j].w, a);
    }
    a = sum8(a);
    sc[g] = rbf(rbf(a) * 0.07216878364870322f);
  }
  SCHED_FENCE();
  STAMP(3);
  unsigned pd[8];
  {
    float m = fmaxf(fmaxf(fmaxf(sc[0], sc[1]), fmaxf(sc[2], sc[3])), fmaxf(fmaxf(sc[4], sc[5]), fmaxf(sc[6], sc[7])));
    m = max_hi(m);
    float l = 0.f;
#pragma unroll
    for (int g = 0; g < 8; ++g) {
      const float p = __builtin_amdgcn_exp2f((sc[g] - m) * 1.4426950408889634f);
      l += p;
      pd[g] = pk2(p, p);
    }
    l = sum_hi(l);
    if (lane == 0) { mls[w] = m; mls[8 + w] = l; }
  }
  SCHED_FENCE();

  {
    f32x4 oacc[3] = {{0.f, 0.f, 0.f, 0.f}, {0.f, 0.f, 0.f, 0.f}, {0.f, 0.f, 0.f, 0.f}};
#pragma unroll
    for (int g = 0; g < 8; ++g) {
      const v4u bf = bfrag(pd[g], mask);
#pragma unroll
      for (int j = 0; j < 3; ++j) oacc[j] = mfma(bf, vv[g][j], oacc[j]);
      SCHED_FENCE();
    }
    float* const myred = red + (w * 2 + (rq >> 1)) * 192 + 8 * (col & 7) + 4 * (rq & 1);
    if (dvalid) {
#pragma unroll
      for (int j = 0; j < 3; ++j) *(f32x4*)(myred + j * 64) = oacc[j];
    }
  }
  STAMP(10);
  const unsigned tag = (epoch + 1u) | 0x80000000u;
  if (w < 4) {
#pragma unroll 1
    for (unsigned spins = 0; spins < (SPIN_CAP << 6); ++spins) {
      if (__hip_atomic_load(&flg[2], __ATOMIC_RELAXED, __HIP_MEMORY_SCOPE_WORKGROUP) >= GVCNT) break;
      __builtin_amdgcn_s_sleep(1);
    }
    const rsrc_t rsWo = mk(wo + (size_t)(192 * h + 48 * w) * 640 + 80 * s, 47 * 1280 + 160);
    const rsrc_t rsW2 = mk(w2 + (size_t)(48 * h + 12 * w) * 640 + 80 * s, 11 * 1280 + 160);
    const unsigned vo3 = col < 10 ? (unsigned)(rq * 15360 + col * 16) : OOB;
    const unsigned vo4 = col < 10 ? (unsigned)(rq * 3840 + col * 16) : OOB;
    v4u gv[15];
#pragma unroll
    for (int g = 0; g < 3; ++g) gv[12 + g] = __builtin_amdgcn_raw_buffer_load_b128(rsW2, vo4, g * 1280, AUX_NT);
#pragma unroll
    for (int g = 0; g < 12; ++g) gv[g] = __builtin_amdgcn_raw_buffer_load_b128(rsWo, vo3, g * 1280, AUX_NT);
    SCHED_FENCE();
    STAMP(15);
    __syncthreads();
    STAMP(4);
    const unsigned gbase = WS_P1 + (unsigned)((h * 8 + (col & 7)) * 256 + 48 * w + 12 * rq + 6 * (col >> 3)) * 8u;
    const unsigned mlo = WS_P1 + (unsigned)((h * 8 + (col & 7)) * 256 + 192) * 8u;
    v4u g[3], gm;
#pragma unroll 1
    for (unsigned spins = 0; spins < SPIN_CAP; ++spins) {
      asm volatile("" ::: "memory");
#pragma unroll
      for (int i = 0; i < 3; ++i) g[i] = __builtin_amdgcn_raw_buffer_load_b128(rsWs, gbase + i * 16, 0, AUX_SC1);
      gm = __builtin_amdgcn_raw_buffer_load_b128(rsWs, mlo, 0, AUX_SC1);
      bool ok = (gm.y == tag) & (gm.w == tag);
#pragma unroll
      for (int i = 0; i < 3; ++i) ok &= (g[i].y == tag) & (g[i].w == tag);
      if (__all(ok)) break;
      __builtin_amdgcn_s_sleep(1);
    }
    STAMP(5);
    unsigned od[6];
    {
      const float M = asf(gm.x);
      float Mg = fmaxf(M, dpp<0xB1>(M));
      Mg = fmaxf(Mg, dpp<0x4E>(Mg));
      Mg = fmaxf(Mg, dpp<0x141>(Mg));
      const float f = __builtin_amdgcn_exp2f((M - Mg) * 1.4426950408889634f);
      const float den = sum8(f * asf(gm.z));
#pragma unroll
      for (int i = 0; i < 3; ++i) {
        const float n0 = sum8(f * asf(g[i].x)) / den, n1 = sum8(f * asf(g[i].z)) / den;
        od[2 * i] = pk2(n0, n0);
        od[2 * i + 1] = pk2(n1, n1);
      }
    }
    unsigned mlo8[4], mhi8[4];
#pragma unroll
    for (int i = 0; i < 4; ++i) { mlo8[i] = col < 8 ? mask[i] : 0u; mhi8[i] = col < 8 ? 0u : mask[i]; }
    const unsigned short* hb = (const unsigned short*)(qs + 96) + 12 * w + 3 * rq;
    f32x4 ya = {0.f, 0.f, 0.f, 0.f}, yb = {0.f, 0.f, 0.f, 0.f}, za = {0.f, 0.f, 0.f, 0.f};
#pragma unroll
    for (int i = 0; i < 3; ++i) za = mfma(gv[12 + i], bfrag((unsigned)hb[i] * 0x10001u, mhi8), za);
#pragma unroll
    for (int i = 0; i < 6; ++i) {
      ya = mfma(gv[i], bfrag(od[i], mlo8), ya);
      yb = mfma(gv[6 + i], bfrag(od[i], mhi8), yb);
    }
    f32x4 r;
#pragma unroll
    for (int i = 0; i < 4; ++i) r[i] = ya[i] + za[i] + dpp<0x128>(yb[i]);
    *(f32x4*)(red3 + w * 256 + lane * 4) = r;
    STAMP(6);
    __syncthreads();
    STAMP(7);
  } else {
    __syncthreads();
    if (w < 6) {
      float t0 = 0.f, t1 = 0.f;
      const f32x4 m0 = *(const f32x4*)mls, m1 = *(const f32x4*)(mls + 4);
      const f32x4 l0 = *(const f32x4*)(mls + 8), l1 = *(const f32x4*)(mls + 12);
      const float mw[8] = {m0.x, m0.y, m0.z, m0.w, m1.x, m1.y, m1.z, m1.w};
      const float lw[8] = {l0.x, l0.y, l0.z, l0.w, l1.x, l1.y, l1.z, l1.w};
      const float M = fmaxf(fmaxf(fmaxf(mw[0], mw[1]), fmaxf(mw[2], mw[3])), fmaxf(fmaxf(mw[4], mw[5]), fmaxf(mw[6], mw[7])));
      float L = 0.f;
      const int sl = t4 < 96 ? 2 * t4 : 0;
#pragma unroll
      for (int i = 0; i < 8; ++i) {
        const float f = __builtin_amdgcn_exp2f((mw[i] - M) * 1.4426950408889634f);
        const f32x2 va = *(const f32x2*)(red + (2 * i) * 192 + sl);
        const f32x2 vb = *(const f32x2*)(red + (2 * i + 1) * 192 + sl);
        t0 += f * (va.x + vb.x);
        t1 += f * (va.y + vb.y);
        L += f * lw[i];
      }
      if (t4 >= 96) { t0 = M; t1 = L; }
      const v4u g = {asu(t0), tag, asu(t1), tag};
      __builtin_amdgcn_raw_buffer_store_b128(g, rsWs, p1_off, 0, AUX_SC1);
      STAMP(12);
      STAMP(13);
    }
    __syncthreads();
    if (w < 7) {
      const float t = (red3[t4] + red3[256 + t4]) + (red3[512 + t4] + red3[768 + t4]);
      const v2u g = {asu(t), tag};
      __builtin_amdgcn_raw_buffer_store_b64(g, rsWs, p2_off, 0, AUX_SC1);
    } else if (h < 20) {
      const int n0 = 4 * h + 2 * (lane & 1);
      const unsigned xin = __builtin_amdgcn_raw_buffer_load_b32(rsX, (unsigned)(80 * s + n0) * 2u, 0, 0);
      const unsigned gbase = WS_P2 + (unsigned)((s * 32 + (lane >> 1)) * 160 + n0 * 2) * 8u;
      v4u g[2];
#pragma unroll 1
      for (unsigned spins = 0; spins < SPIN_CAP; ++spins) {
        asm volatile("" ::: "memory");
#pragma unroll
        for (int i = 0; i < 2; ++i) g[i] = __builtin_amdgcn_raw_buffer_load_b128(rsWs, gbase + i * 16, 0, AUX_SC1);
        const bool ok = (g[0].y == tag) & (g[0].w == tag) & (g[1].y == tag) & (g[1].w == tag);
        if (__all(ok)) break;
        __builtin_amdgcn_s_sleep(1);
      }
      STAMP(8);
      float v[4] = {asf(g[0].x), asf(g[0].z), asf(g[1].x), asf(g[1].z)};
#pragma unroll
      for (int i = 0; i < 4; ++i) {
        float t = v[i];
        t += dpp<0x4E>(t);
        t += __shfl_xor(t, 4);
        t = sum_hi(t);
        v[i] = t;
      }
      if (lane < 2) {
        const float r0 = rbf(rbf(bflo(xin) + rbf(v[0])) + rbf(v[1]));
        const float r1 = rbf(rbf(bfhi(xin) + rbf(v[2])) + rbf(v[3]));
        *(unsigned*)(out + 80 * s + n0) = pk2(r0, r1);
      }
      if (wg == 0 && lane == 0) __builtin_amdgcn_raw_buffer_store_b32(epoch + 1u, rsWs, 0, 0, AUX_SC1);
    }
  }

}

}

extern "C" void kernel_launch(void* const* d_in, const int* in_sizes, int n_in, void* d_out, int out_size, void* d_ws,
                              size_t ws_size, hipStream_t stream) {
  (void)in_sizes; (void)n_in; (void)out_size;
  if (ws_size < WS_BYTES) return;
  ka_tiny<<<dim3(1), dim3(64), 0, stream>>>((unsigned char*)d_ws);
  fused_decode_kernel<<<dim3(256), dim3(512), 0, stream>>>(
      (const unsigned short*)d_in[0], (const unsigned short*)d_in[1], (const unsigned short*)d_in[2],
      (const unsigned short*)d_in[3], (const unsigned short*)d_in[4], (const unsigned short*)d_in[6],
      (const unsigned short*)d_in[7], (unsigned short*)d_out, (unsigned char*)d_ws);
  kc_tiny<<<dim3(1), dim3(64), 0, stream>>>((unsigned char*)d_ws);
}
```

```cpp
#include <hip/hip_runtime.h>
#include <hip/hip_bf16.h>

namespace {

typedef unsigned v4u __attribute__((ext_vector_type(4)));
typedef unsigned v2u __attribute__((ext_vector_type(2)));
typedef float f32x4 __attribute__((ext_vector_type(4)));
typedef float f32x2 __attribute__((ext_vector_type(2)));
typedef __bf16 bfx8 __attribute__((ext_vector_type(8)));
typedef __bf16 bfx2 __attribute__((ext_vector_type(2)));
typedef __amdgpu_buffer_rsrc_t rsrc_t;

constexpr int AUX_NT = 2;
constexpr int AUX_SC1 = 16;
constexpr int AUX_POLL = (int)(16u | 0x80000000u);
constexpr unsigned OOB = 0x80000000u;
constexpr unsigned SPIN_CAP = 1u << 16;

constexpr unsigned WS_P0 = 4096;
constexpr unsigned P1S = 1664;
constexpr unsigned WS_P1 = WS_P0 + 256u * 256u * 8u;
constexpr unsigned WS_ML = WS_P1 + 256u * P1S;
constexpr unsigned WS_Y = WS_ML + 4096u;
constexpr unsigned WS_Z = WS_Y + 8u * 32u * 640u;
constexpr unsigned WS_BYTES = WS_Z + 8u * 32u * 640u;
constexpr int RS = 392;

#define PG2 4
#define WE 3
#define GVCNT 8
#define WOSL 0
#define FEW 2
#define DIRECT0 1
#define NY 4
#define YFEW 64
#define YSL 8
#define NSET 1
#define PSL 0
#define STAG1 8
#define STAG2 6

__device__ __forceinline__ rsrc_t mk(const void* p, unsigned bytes) {
  return __builtin_amdgcn_make_buffer_rsrc((void*)p, (short)0, (int)bytes, 0x00020000);
}
__device__ __forceinline__ float asf(unsigned u) { return __builtin_bit_cast(float, u); }
__device__ __forceinline__ unsigned asu(float f) { return __builtin_bit_cast(unsigned, f); }
__device__ __forceinline__ float rbf(float f) { return (float)(__bf16)f; }
__device__ __forceinline__ float bflo(unsigned u) { return __builtin_bit_cast(float, u << 16); }
__device__ __forceinline__ float bfhi(unsigned u) { return __builtin_bit_cast(float, u & 0xFFFF0000u); }
__device__ __forceinline__ unsigned pk2(float a, float b) {
  f32x2 v = {a, b};
  return __builtin_bit_cast(unsigned, __builtin_convertvector(v, bfx2));
}
__device__ __forceinline__ f32x4 mfma(v4u a, v4u b, f32x4 c) {
  return __builtin_amdgcn_mfma_f32_16x16x32_bf16(__builtin_bit_cast(bfx8, a), __builtin_bit_cast(bfx8, b), c, 0, 0, 0);
}
__device__ __forceinline__ float dot2(unsigned a, unsigned b, float c) {
  return __builtin_amdgcn_fdot2_f32_bf16(__builtin_bit_cast(bfx2, a), __builtin_bit_cast(bfx2, b), c, false);
}
template <int CTRL> __device__ __forceinline__ float dpp(float v) {
  return __builtin_bit_cast(float, __builtin_amdgcn_mov_dpp(__builtin_bit_cast(int, v), CTRL, 0xF, 0xF, true));
}
__device__ __forceinline__ float sum8(float v) {
  v += dpp<0xB1>(v);
  v += dpp<0x4E>(v);
  v += dpp<0x141>(v);
  return v;
}
__device__ __forceinline__ float max8(float v) {
  v = fmaxf(v, dpp<0xB1>(v));
  v = fmaxf(v, dpp<0x4E>(v));
  v = fmaxf(v, dpp<0x141>(v));
  return v;
}
__device__ __forceinline__ float sum_hi(float v) {
  v += dpp<0x128>(v);
  v += __shfl_xor(v, 16);
  v += __shfl_xor(v, 32);
  return v;
}
__device__ __forceinline__ float max_hi(float v) {
  v = fmaxf(v, dpp<0x128>(v));
  v = fmaxf(v, __shfl_xor(v, 16));
  v = fmaxf(v, __shfl_xor(v, 32));
  return v;
}
__device__ __forceinline__ v4u bfrag(unsigned dup, const unsigned (&mask)[4]) {
  v4u b;
  b.x = dup & mask[0];
  b.y = dup & mask[1];
  b.z = dup & mask[2];
  b.w = dup & mask[3];
  return b;
}
__device__ __forceinline__ float fold32(float a, float b) {
  const auto r = __builtin_amdgcn_permlane32_swap(asu(a), asu(b), false, false);
  return asf(r[0]) + asf(r[1]);
}
__device__ __forceinline__ float sum32(float v) {
  v = sum8(v);
  v += dpp<0x128>(v);
  const auto r = __builtin_amdgcn_permlane16_swap(asu(v), asu(v), false, false);
  return asf(r[0]) + asf(r[1]);
}
#define SCHED_FENCE() __builtin_amdgcn_sched_barrier(0)
#define CBAR() asm volatile("" ::: "memory")
#define STR_(x) #x
#define STR(x) STR_(x)
#define PSL_TXT ""
__device__ __forceinline__ v4u srd(const void* p, unsigned bytes) {
  const unsigned long long a = (unsigned long long)p;
  const v4u r = {(unsigned)a, (unsigned)(a >> 32) & 0xFFFFu, bytes, 0x00020000u};
  return r;
}
#define LWO0 if (WE > 0) asm volatile("s_nop 4\n buffer_load_dwordx4 v[160:163], %1, %2, %3 offen offset:0 nt" : "={v[160:163]}"(gv[0]) : "v"(vo3), "s"(srdWo), "s"(0) : "memory")
#define RWO0 if (WE > 0) asm volatile("" : "+{v[160:163]}"(gv[0]) :: "memory")
#define LWO1 if (WE > 1) asm volatile("s_nop 4\n buffer_load_dwordx4 v[164:167], %1, %2, %3 offen offset:1280 nt" : "={v[164:167]}"(gv[1]) : "v"(vo3), "s"(srdWo), "s"(0) : "memory")
#define RWO1 if (WE > 1) asm volatile("" : "+{v[164:167]}"(gv[1]) :: "memory")
#define LWO2 if (WE > 2) asm volatile("s_nop 4\n buffer_load_dwordx4 v[168:171], %1, %2, %3 offen offset:2560 nt" : "={v[168:171]}"(gv[2]) : "v"(vo3), "s"(srdWo), "s"(0) : "memory")
#define RWO2 if (WE > 2) asm volatile("" : "+{v[168:171]}"(gv[2]) :: "memory")
#define LWO3 if (WE > 3) asm volatile("s_nop 4\n buffer_load_dwordx4 v[172:175], %1, %2, %3 offen offset:3840 nt" : "={v[172:175]}"(gv[3]) : "v"(vo3), "s"(srdWo), "s"(0) : "memory")
#define RWO3 if (WE > 3) asm volatile("" : "+{v[172:175]}"(gv[3]) :: "memory")
#define LWO4 if (WE > 4) asm volatile("s_nop 4\n buffer_load_dwordx4 v[176:179], %1, %2, %3 offen offset:0 nt" : "={v[176:179]}"(gv[4]) : "v"(vo3), "s"(srdWo), "s"(5120) : "memory")
#define RWO4 if (WE > 4) asm volatile("" : "+{v[176:179]}"(gv[4]) :: "memory")
#define LWO5 if (WE > 5) asm volatile("s_nop 4\n buffer_load_dwordx4 v[180:183], %1, %2, %3 offen offset:1280 nt" : "={v[180:183]}"(gv[5]) : "v"(vo3), "s"(srdWo), "s"(5120) : "memory")
#define RWO5 if (WE > 5) asm volatile("" : "+{v[180:183]}"(gv[5]) :: "memory")
#define LWO6 if (WE > 6) asm volatile("s_nop 4\n buffer_load_dwordx4 v[184:187], %1, %2, %3 offen offset:2560 nt" : "={v[184:187]}"(gv[6]) : "v"(vo3), "s"(srdWo), "s"(5120) : "memory")
#define RWO6 if (WE > 6) asm volatile("" : "+{v[184:187]}"(gv[6]) :: "memory")
#define LWO7 if (WE > 7) asm volatile("s_nop 4\n buffer_load_dwordx4 v[188:191], %1, %2, %3 offen offset:3840 nt" : "={v[188:191]}"(gv[7]) : "v"(vo3), "s"(srdWo), "s"(5120) : "memory")
#define RWO7 if (WE > 7) asm volatile("" : "+{v[188:191]}"(gv[7]) :: "memory")
#define LWO8 if (WE > 8) asm volatile("s_nop 4\n buffer_load_dwordx4 v[192:195], %1, %2, %3 offen offset:0 nt" : "={v[192:195]}"(gv[8]) : "v"(vo3), "s"(srdWo), "s"(10240) : "memory")
#define RWO8 if (WE > 8) asm volatile("" : "+{v[192:195]}"(gv[8]) :: "memory")
#define LWO9 if (WE > 9) asm volatile("s_nop 4\n buffer_load_dwordx4 v[196:199], %1, %2, %3 offen offset:1280 nt" : "={v[196:199]}"(gv[9]) : "v"(vo3), "s"(srdWo), "s"(10240) : "memory")
#define RWO9 if (WE > 9) asm volatile("" : "+{v[196:199]}"(gv[9]) :: "memory")
#define LWO10 if (WE > 10) asm volatile("s_nop 4\n buffer_load_dwordx4 v[200:203], %1, %2, %3 offen offset:2560 nt" : "={v[200:203]}"(gv[10]) : "v"(vo3), "s"(srdWo), "s"(10240) : "memory")
#define RWO10 if (WE > 10) asm volatile("" : "+{v[200:203]}"(gv[10]) :: "memory")
#define LWO11 if (WE > 11) asm volatile("s_nop 4\n buffer_load_dwordx4 v[204:207], %1, %2, %3 offen offset:3840 nt" : "={v[204:207]}"(gv[11]) : "v"(vo3), "s"(srdWo), "s"(10240) : "memory")
#define RWO11 if (WE > 11) asm volatile("" : "+{v[204:207]}"(gv[11]) :: "memory")
#define LOAD_WO() do { LWO0; LWO1; LWO2; LWO3; LWO4; LWO5; LWO6; LWO7; LWO8; LWO9; LWO10; LWO11; } while (0)
#define READY_WO() do { RWO0; RWO1; RWO2; RWO3; RWO4; RWO5; RWO6; RWO7; RWO8; RWO9; RWO10; RWO11; } while (0)
#define P1_ISSUE0(G) asm volatile("s_nop 4\n" "buffer_load_dwordx4 v[220:223], %5, %6, 0 offen sc1\n" "buffer_load_dwordx4 v[208:211], %4, %6, 0 offen offset:0 sc1\n" "buffer_load_dwordx4 v[212:215], %4, %6, 0 offen offset:128 sc1\n" "buffer_load_dwordx4 v[216:219], %4, %6, 0 offen offset:256 sc1\n"  \
  : "={v[208:211]}"(G[0]), "={v[212:215]}"(G[1]), "={v[216:219]}"(G[2]), "={v[220:223]}"(G[3]) : "v"(g0), "v"(mo), "s"(srdWs) : "memory")
#define P1_ISSUE1(G) asm volatile("s_nop 4\n" "buffer_load_dwordx4 v[236:239], %5, %6, 0 offen sc1\n" "buffer_load_dwordx4 v[224:227], %4, %6, 0 offen offset:0 sc1\n" "buffer_load_dwordx4 v[228:231], %4, %6, 0 offen offset:128 sc1\n" "buffer_load_dwordx4 v[232:235], %4, %6, 0 offen offset:256 sc1\n"  \
  : "={v[224:227]}"(G[4]), "={v[228:231]}"(G[5]), "={v[232:235]}"(G[6]), "={v[236:239]}"(G[7]) : "v"(g0), "v"(mo), "s"(srdWs) : "memory")
#define P1_ISSUE2(G) asm volatile("s_nop 4\n" "buffer_load_dwordx4 v[252:255], %5, %6, 0 offen sc1\n" "buffer_load_dwordx4 v[240:243], %4, %6, 0 offen offset:0 sc1\n" "buffer_load_dwordx4 v[244:247], %4, %6, 0 offen offset:128 sc1\n" "buffer_load_dwordx4 v[248:251], %4, %6, 0 offen offset:256 sc1\n"  \
  : "={v[240:243]}"(G[8]), "={v[244:247]}"(G[9]), "={v[248:251]}"(G[10]), "={v[252:255]}"(G[11]) : "v"(g0), "v"(mo), "s"(srdWs) : "memory")
#define ZCNT 16
#define P1_LOOP(G) asm volatile( \
      ".Lp1l%=:\n" \
      "s_waitcnt vmcnt(0)\n" \
      "v_bitop3_b32 %[t0], v221, v223, %[tag] bitop3:0x7e\n" \
      "v_bitop3_b32 %[t1], v209, v211, %[tag] bitop3:0x7e\n" \
      "v_bitop3_b32 %[t2], v213, v215, %[tag] bitop3:0x7e\n" \
      "v_bitop3_b32 %[t3], v217, v219, %[tag] bitop3:0x7e\n" \
      "v_cmp_eq_u32 %[sa], 0, %[t0]\n" \
      "v_or3_b32 %[t1], %[t1], %[t2], %[t3]\n" \
      "v_cmp_eq_u32 vcc, 0, %[t1]\n" \
      "s_nop 0\n" \
      "v_cndmask_b32 %[f6], %[f6], v220, %[sa]\n" \
      "v_cndmask_b32 %[f7], %[f7], v222, %[sa]\n" \
      "v_cndmask_b32 %[mo], %[mo], %[oob], %[sa]\n" \
      "v_cndmask_b32 %[f0], %[f0], v208, vcc\n" \
      "v_cndmask_b32 %[f1], %[f1], v210, vcc\n" \
      "v_cndmask_b32 %[f2], %[f2], v212, vcc\n" \
      "v_cndmask_b32 %[f3], %[f3], v214, vcc\n" \
      "v_cndmask_b32 %[f4], %[f4], v216, vcc\n" \
      "v_cndmask_b32 %[f5], %[f5], v218, vcc\n" \
      "s_andn2_b64 %[pm], %[pm], %[sa]\n" \
      "s_andn2_b64 %[pd], %[pd], vcc\n" \
      "s_or_b64 %[sa], %[pm], %[pd]\n" \
      "s_cbranch_scc0 .Lp1x%=\n" \
      "s_bcnt1_i32_b64 %[sc], %[pd]\n" \
      "s_cmp_le_u32 %[sc], " STR(FEW) " * 8\n" \
      "s_cselect_b64 %[sa], 0, %[pm]\n" \
      "s_andn2_b64 %[sa], %[pd], %[sa]\n" \
      "v_cndmask_b32 %[t0], %[oob], %[go], %[sa]\n" \
      PSL_TXT \
      "buffer_load_dwordx4 v[220:223], %[mo], %[rs], 0 offen sc1\n" "buffer_load_dwordx4 v[208:211], %[t0], %[rs], 0 offen offset:0 sc1\n" "buffer_load_dwordx4 v[212:215], %[t0], %[rs], 0 offen offset:128 sc1\n" "buffer_load_dwordx4 v[216:219], %[t0], %[rs], 0 offen offset:256 sc1\n"  \
      "s_sub_u32 %[cnt], %[cnt], 1\n" "s_cbranch_scc0 .Lp1l%=\n" ".Lp1x%=:\n" "s_nop 1\n" \
      : "+{v[208:211]}"(G[0]), "+{v[212:215]}"(G[1]), "+{v[216:219]}"(G[2]), "+{v[220:223]}"(G[3]), \
        [f0] "+v"(fv[0]), [f1] "+v"(fv[1]), [f2] "+v"(fv[2]), [f3] "+v"(fv[3]), [f4] "+v"(fv[4]), [f5] "+v"(fv[5]), [f6] "+v"(fv[6]), [f7] "+v"(fv[7]), \
        [mo] "+v"(mo), [pm] "+s"(pm), [pd] "+s"(pd), [cnt] "+s"(cnt), [sa] "=&s"(sa), [sc] "=&s"(sc), \
        [t0] "=&v"(t0), [t1] "=&v"(t1), [t2] "=&v"(t2), [t3] "=&v"(t3) \
      : [go] "v"(go), [tag] "v"(tag), [oob] "v"(OOB), [rs] "s"(srdWs) : "vcc", "scc", "memory")
#define P1_DRAIN(G) asm volatile("s_waitcnt vmcnt(0)" :: "{v[208:211]}"(G[0]), "{v[212:215]}"(G[1]), "{v[216:219]}"(G[2]), "{v[220:223]}"(G[3]) : "memory")
#define P2_LOOP(G) asm volatile( \
      "s_nop 4\n" "buffer_load_dwordx4 v[224:227], %[go], %[rs], %[sz] offen sc1\n" \
      "buffer_load_dwordx4 v[208:211], %[go], %[rs], %[so] offen sc1\n" "s_sleep " STR(STAG2) "\n" \
      "buffer_load_dwordx4 v[212:215], %[go], %[rs], %[so] offen sc1\n" "s_sleep " STR(STAG2) "\n" \
      "buffer_load_dwordx4 v[216:219], %[go], %[rs], %[so] offen sc1\n" "s_sleep " STR(STAG2) "\n" \
      "buffer_load_dwordx4 v[220:223], %[go], %[rs], %[so] offen sc1\n" \
      ".Lp2l%=:\n" \
      "s_waitcnt vmcnt(3)\n" \
      "v_bitop3_b32 %[t0], v209, v211, %[tag] bitop3:0x7e\n" \
      "v_cmp_eq_u32 vcc, 0, %[t0]\n" \
      "s_nop 1\n" \
      "v_cndmask_b32 %[go], %[go], %[oob], vcc\n" \
      "v_cndmask_b32 %[y0], %[y0], v208, vcc\n" \
      "v_cndmask_b32 %[y1], %[y1], v210, vcc\n" \
      "s_andn2_b64 %[pend], %[pend], vcc\n" \
      "s_cbranch_scc0 .Lp2x%=\n" \
      "s_bcnt1_i32_b64 %[sc], %[pend]\n" \
      "s_cmp_le_u32 %[sc], " STR(YFEW) "\n" \
      "s_cbranch_scc1 .Lp2s0%=\n" \
      "s_sleep " STR(YSL) "\n" \
      ".Lp2s0%=:\n" \
      "buffer_load_dwordx4 v[208:211], %[go], %[rs], %[so] offen sc1\n" \
      "s_waitcnt vmcnt(3)\n" \
      "v_bitop3_b32 %[t0], v213, v215, %[tag] bitop3:0x7e\n" \
      "v_cmp_eq_u32 vcc, 0, %[t0]\n" \
      "s_nop 1\n" \
      "v_cndmask_b32 %[go], %[go], %[oob], vcc\n" \
      "v_cndmask_b32 %[y0], %[y0], v212, vcc\n" \
      "v_cndmask_b32 %[y1], %[y1], v214, vcc\n" \
      "s_andn2_b64 %[pend], %[pend], vcc\n" \
      "s_cbranch_scc0 .Lp2x%=\n" \
      "s_bcnt1_i32_b64 %[sc], %[pend]\n" \
      "s_cmp_le_u32 %[sc], " STR(YFEW) "\n" \
      "s_cbranch_scc1 .Lp2s1%=\n" \
      "s_sleep " STR(YSL) "\n" \
      ".Lp2s1%=:\n" \
      "buffer_load_dwordx4 v[212:215], %[go], %[rs], %[so] offen sc1\n" \
      "s_waitcnt vmcnt(3)\n" \
      "v_bitop3_b32 %[t0], v217, v219, %[tag] bitop3:0x7e\n" \
      "v_cmp_eq_u32 vcc, 0, %[t0]\n" \
      "s_nop 1\n" \
      "v_cndmask_b32 %[go], %[go], %[oob], vcc\n" \
      "v_cndmask_b32 %[y0], %[y0], v216, vcc\n" \
      "v_cndmask_b32 %[y1], %[y1], v218, vcc\n" \
      "s_andn2_b64 %[pend], %[pend], vcc\n" \
      "s_cbranch_scc0 .Lp2x%=\n" \
      "s_bcnt1_i32_b64 %[sc], %[pend]\n" \
      "s_cmp_le_u32 %[sc], " STR(YFEW) "\n" \
      "s_cbranch_scc1 .Lp2s2%=\n" \
      "s_sleep " STR(YSL) "\n" \
      ".Lp2s2%=:\n" \
      "buffer_load_dwordx4 v[216:219], %[go], %[rs], %[so] offen sc1\n" \
      "s_waitcnt vmcnt(3)\n" \
      "v_bitop3_b32 %[t0], v221, v223, %[tag] bitop3:0x7e\n" \
      "v_cmp_eq_u32 vcc, 0, %[t0]\n" \
      "s_nop 1\n" \
      "v_cndmask_b32 %[go], %[go], %[oob], vcc\n" \
      "v_cndmask_b32 %[y0], %[y0], v220, vcc\n" \
      "v_cndmask_b32 %[y1], %[y1], v222, vcc\n" \
      "s_andn2_b64 %[pend], %[pend], vcc\n" \
      "s_cbranch_scc0 .Lp2x%=\n" \
      "s_bcnt1_i32_b64 %[sc], %[pend]\n" \
      "s_cmp_le_u32 %[sc], " STR(YFEW) "\n" \
      "s_cbranch_scc1 .Lp2s3%=\n" \
      "s_sleep " STR(YSL) "\n" \
      ".Lp2s3%=:\n" \
      "buffer_load_dwordx4 v[220:223], %[go], %[rs], %[so] offen sc1\n" \
      "s_sub_u32 %[cnt], %[cnt], 1\n" "s_cbranch_scc0 .Lp2l%=\n" ".Lp2x%=:\n" "s_nop 1\n" \
      : "=&{v[208:211]}"(G[0]), "=&{v[212:215]}"(G[1]), "=&{v[216:219]}"(G[2]), "=&{v[220:223]}"(G[3]), "=&{v[224:227]}"(G[4]), \
        [y0] "+v"(y0), [y1] "+v"(y1), [go] "+v"(go), [pend] "+s"(pend), [cnt] "+s"(cnt), [sc] "=&s"(sc), [t0] "=&v"(t0) \
      : [tag] "v"(tag), [oob] "v"(OOB), [rs] "s"(srdWs), [so] "s"(WS_Y), [sz] "s"(WS_Z) : "vcc", "scc", "memory")
#define P2_DRAIN(G) asm volatile("s_waitcnt vmcnt(0)" :: "{v[208:211]}"(G[0]), "{v[212:215]}"(G[1]), "{v[216:219]}"(G[2]), "{v[220:223]}"(G[3]) : "memory")
#define STAMP(k)

#define Y_FINISH() do {                                                                                         \
    ya += yb;                                                                                                   \
    f32x2 r;                                                                                                    \
    r.x = fold32(ya[0], ya[2]);                                                                                 \
    r.y = fold32(ya[1], ya[3]);                                                                                 \
    *(f32x2*)(red3 + w * 128 + lane * 2) = r;                                                                   \
    STAMP(6);                                                                                                   \
    CBAR();                                                                                                     \
    unsigned old = 0u;                                                                                          \
    if (lane == 0) old = __hip_atomic_fetch_add(&flg[5], 1u, __ATOMIC_RELAXED, __HIP_MEMORY_SCOPE_WORKGROUP);   \
    old = __builtin_amdgcn_readfirstlane(old);                                                                  \
    CBAR();                                                                                                     \
    if (old == (WE > 0 && WE < 12 ? 7u : 3u)) {                                                                 \
      const float* rp = red3 + lane * 2;                                                                        \
      f32x2 t = {0.f, 0.f};                                                                                     \
      if (WE > 0) t += (*(const f32x2*)(rp) + *(const f32x2*)(rp + 128)) + (*(const f32x2*)(rp + 256) + *(const f32x2*)(rp + 384)); \
      if (WE < 12) t += (*(const f32x2*)(rp + 512) + *(const f32x2*)(rp + 640)) + (*(const f32x2*)(rp + 768) + *(const f32x2*)(rp + 896)); \
      const v4u g = {asu(t.x), tag, asu(t.y), tag};                                                             \
      __builtin_amdgcn_raw_buffer_store_b128(g, rsWs, yz_off + WS_Y, 0, AUX_SC1);                               \
      CBAR();                                                                                                   \
      if (lane == 0) __hip_atomic_store(&flg[6], 1u, __ATOMIC_RELAXED, __HIP_MEMORY_SCOPE_WORKGROUP);           \
      CBAR();                                                                                                   \
      STAMP(7);                                                                                                 \
    }                                                                                                           \
  } while (0)

__global__ void __launch_bounds__(512, 2)
fused_decode_kernel(const unsigned short* __restrict__ x, const unsigned short* __restrict__ kc,
                    const unsigned short* __restrict__ vc, const unsigned short* __restrict__ wq,
                    const unsigned short* __restrict__ wo, const unsigned short* __restrict__ w1,
                    const unsigned short* __restrict__ w2, unsigned short* __restrict__ out,
                    unsigned char* ws) {
  __shared__ __attribute__((aligned(16))) float red[8 * RS];
  __shared__ __attribute__((aligned(16))) float red3[8 * 128];
  __shared__ __attribute__((aligned(16))) unsigned qs[128];
  __shared__ __attribute__((aligned(16))) float mls[16];
  __shared__ __attribute__((aligned(16))) unsigned osh[4 * 64];
  __shared__ unsigned flg[8];

  STAMP(0);
  const int tid = threadIdx.x;
  const int lane = tid & 63;
  const int w = __builtin_amdgcn_readfirstlane(tid >> 6);
  const int lg = lane >> 3, lc = lane & 7;
  const int col = lane & 15, rq = lane >> 4;
  const int b = blockIdx.x;
  const int h = 4 * (b & 7) + (b >> 6);
  const int s = (b >> 3) & 7;
  const int wg = h * 8 + s;

  const rsrc_t rsX = mk(x, 1280);
  const rsrc_t rsWs = mk(ws, WS_BYTES);
  const v4u srdWs = srd(ws, WS_BYTES);
  const size_t kvoff = ((size_t)h * 32768 + 28672 + 512 * s + 64 * w) * 192;
  const rsrc_t rsK = mk(kc + kvoff, 64 * 384);
  const rsrc_t rsV = mk(vc + kvoff, 64 * 384);

  const unsigned epoch = __builtin_amdgcn_raw_buffer_load_b32(rsWs, 0, 0, AUX_SC1);
  if (tid < 8) flg[tid] = 0u;
  if (tid >= 256) osh[tid - 256] = 0u;

  unsigned mask[4];
  {
    const unsigned mm = (lc & 1) ? 0xFFFF0000u : 0x0000FFFFu;
#pragma unroll
    for (int i = 0; i < 4; ++i) mask[i] = ((lc >> 1) == i) ? mm : 0u;
  }
  const bool dvalid = (rq >> 1) == (col >> 3);
  const unsigned vkv = (unsigned)(lg * 384 + lc * 16);
  v4u kv[8][3], vv[8][3];

  if (w < 2) {
    __builtin_amdgcn_s_setprio(3);
    const v4u xs0 = __builtin_amdgcn_raw_buffer_load_b128(rsX, 20 * lane, 0, 0);
    const unsigned xs1 = __builtin_amdgcn_raw_buffer_load_b32(rsX, 20 * lane + 16, 0, 0);
    const v4u xr0 = __builtin_amdgcn_raw_buffer_load_b128(rsX, 160 * s + 20 * lg, 0, 0);
    const unsigned xr1 = __builtin_amdgcn_raw_buffer_load_b32(rsX, 160 * s + 20 * lg + 16, 0, 0);
    const unsigned short* pa = wq + (size_t)(80 * s) * 6144 + 192 * h + 128 * w;
    const unsigned short* pb = w ? w1 + (size_t)(80 * s) * 1536 + 48 * h : pa + 64;
    const int sb = w ? 3072 : 12288;
    const rsrc_t rsA = mk(pa, 79 * 12288 + 128);
    const rsrc_t rsB = mk(pb, w ? 79 * 3072 + 96 : 79 * 12288 + 128);
    const unsigned voa = (unsigned)(lg * 122880 + lc * 16);
    const unsigned vob = (w && lc >= 6) ? OOB : (unsigned)(lg * 10 * sb + lc * 16);
    v4u wa[10], wb[10];
#pragma unroll
    for (int g = 0; g < 10; ++g) {
      wa[g] = __builtin_amdgcn_raw_buffer_load_b128(rsA, voa, g * 12288, AUX_NT);
      wb[g] = __builtin_amdgcn_raw_buffer_load_b128(rsB, vob, g * sb, AUX_NT);
      SCHED_FENCE();
    }
    const unsigned p0_off = dvalid ? WS_P0 + (unsigned)(wg * 256 + 128 * w + 64 * (rq >> 1) + 8 * (col & 7) + 4 * (rq & 1)) * 8u : OOB;
    SCHED_FENCE();
    __syncthreads();
    SCHED_FENCE();
    float mb, sd;
    {
      float e[10];
      e[0] = bflo(xs0.x); e[1] = bfhi(xs0.x); e[2] = bflo(xs0.y); e[3] = bfhi(xs0.y);
      e[4] = bflo(xs0.z); e[5] = bfhi(xs0.z); e[6] = bflo(xs0.w); e[7] = bfhi(xs0.w);
      e[8] = bflo(xs1);   e[9] = bfhi(xs1);
      float t = 0.f;
#pragma unroll
      for (int i = 0; i < 10; ++i) t += e[i];
      t = sum_hi(sum8(t));
      const float mean = t * (1.0f / 640.0f);
      float q2 = 0.f;
#pragma unroll
      for (int i = 0; i < 10; ++i) { const float d = e[i] - mean; q2 += d * d; }
      q2 = sum_hi(sum8(q2));
      const float var = rbf(q2 * (1.0f / 640.0f));
      mb = rbf(mean);
      sd = rbf(sqrtf(rbf(var + 1e-5f)));
    }
    const unsigned xr[5] = {xr0.x, xr0.y, xr0.z, xr0.w, xr1};
    const f32x4 zero = {0.f, 0.f, 0.f, 0.f};
    f32x4 a0 = zero, a1 = zero, b0 = zero, b1 = zero;
#pragma unroll
    for (int g = 0; g < 5; ++g) {
      const float n0 = rbf(bflo(xr[g]) - mb) / sd, n1 = rbf(bfhi(xr[g]) - mb) / sd;
      const v4u f0 = bfrag(pk2(n0, n0), mask), f1 = bfrag(pk2(n1, n1), mask);
      a0 = mfma(f0, wa[2 * g], a0);
      b0 = mfma(f0, wb[2 * g], b0);
      a1 = mfma(f1, wa[2 * g + 1], a1);
      b1 = mfma(f1, wb[2 * g + 1], b1);
    }
    a0 += a1;
    b0 += b1;
    const unsigned tag = (epoch + 1u) | 0x80000000u;
    {
      const bool p = rq >= 2;
      float r[4];
#pragma unroll
      for (int i = 0; i < 4; ++i) r[i] = (p ? b0[i] : a0[i]) + __shfl_xor(p ? a0[i] : b0[i], 40);
      const v4u g0 = {asu(r[0]), tag, asu(r[1]), tag}, g1 = {asu(r[2]), tag, asu(r[3]), tag};
      __builtin_amdgcn_raw_buffer_store_b128(g0, rsWs, p0_off, 0, AUX_SC1);
      __builtin_amdgcn_raw_buffer_store_b128(g1, rsWs, p0_off + 16, 0, AUX_SC1);
    }
    STAMP(1);
    __builtin_amdgcn_s_setprio(0);
    SCHED_FENCE();
#pragma unroll
    for (int g = 0; g < 2; ++g)
#pragma unroll
      for (int j = 0; j < 3; ++j) kv[g][j] = __builtin_amdgcn_raw_buffer_load_b128(rsK, vkv + j * 128, g * 3072, AUX_NT);
  } else {
#pragma unroll
    for (int g = 0; g < 2; ++g)
#pragma unroll
      for (int j = 0; j < 3; ++j) kv[g][j] = __builtin_amdgcn_raw_buffer_load_b128(rsK, vkv + j * 128, g * 3072, AUX_NT);
    SCHED_FENCE();
    __syncthreads();
  }
  SCHED_FENCE();
#pragma unroll
  for (int g = 2; g < 8; ++g) {
#pragma unroll
    for (int j = 0; j < 3; ++j) kv[g][j] = __builtin_amdgcn_raw_buffer_load_b128(rsK, vkv + j * 128, g * 3072, AUX_NT);
    SCHED_FENCE();
  }
#pragma unroll
  for (int g = 0; g < 8; ++g) {
#pragma unroll
    for (int j = 0; j < 3; ++j) vv[g][j] = __builtin_amdgcn_raw_buffer_load_b128(rsV, vkv + j * 128, g * 3072, AUX_NT);
    SCHED_FENCE();
  }
  STAMP(9);
  if (WE && lane == 0) __hip_atomic_fetch_add(&flg[2], 1u, __ATOMIC_RELAXED, __HIP_MEMORY_SCOPE_WORKGROUP);
  if (w == 2 || w == 3) {
    const int t2 = tid - 128;
    const unsigned tag = (epoch + 1u) | 0x80000000u;
    const unsigned gbase = WS_P0 + (unsigned)(h * 8 * 256 + 2 * t2) * 8u;
    v4u g[8];
#pragma unroll 1
    for (unsigned spins = 0; spins < SPIN_CAP; ++spins) {
      asm volatile("" ::: "memory");
#pragma unroll
      for (int i = 0; i < 8; ++i) g[i] = __builtin_amdgcn_raw_buffer_load_b128(rsWs, gbase, i * 2048, AUX_SC1);
      bool ok = true;
#pragma unroll
      for (int i = 0; i < 8; ++i) ok &= (g[i].y == tag) & (g[i].w == tag);
      if (__all(ok)) break;
      __builtin_amdgcn_s_sleep(1);
    }
    float t0 = 0.f, t1 = 0.f;
#pragma unroll
    for (int i = 0; i < 8; ++i) {
      t0 += asf(g[i].x);
      t1 += asf(g[i].z);
    }
    if (t2 >= 96) { t0 = fmaxf(t0, 0.f); t1 = fmaxf(t1, 0.f); }
    qs[t2] = pk2(t0, t1);
    __builtin_amdgcn_fence(__ATOMIC_RELEASE, "workgroup");
    if (lane == 0) __hip_atomic_store(&flg[w - 2], 1u, __ATOMIC_RELAXED, __HIP_MEMORY_SCOPE_WORKGROUP);
    STAMP(2);
  }
#pragma unroll 1
  for (unsigned spins = 0; spins < (SPIN_CAP << 6); ++spins) {
    const unsigned f0 = __hip_atomic_load(&flg[0], __ATOMIC_RELAXED, __HIP_MEMORY_SCOPE_WORKGROUP);
    const unsigned f1 = __hip_atomic_load(&flg[1], __ATOMIC_RELAXED, __HIP_MEMORY_SCOPE_WORKGROUP);
    if (f0 & f1) break;
    __builtin_amdgcn_s_sleep(1);
  }
  __builtin_amdgcn_fence(__ATOMIC_ACQUIRE, "workgroup");
  v4u qf[3];
#pragma unroll
  for (int j = 0; j < 3; ++j) qf[j] = *(const v4u*)(qs + 32 * j + 4 * lc);

  float sc[8];
#pragma unroll
  for (int g = 0; g < 8; ++g) {
    float a = 0.f;
#pragma unroll
    for (int j = 0; j < 3; ++j) {
      a = dot2(kv[g][j].x, qf[j].x, a);
      a = dot2(kv[g][j].y, qf[j].y, a);
      a = dot2(kv[g][j].z, qf[j].z, a);
      a = dot2(kv[g][j].w, qf[j].w, a);
    }
    a = sum8(a);
    sc[g] = rbf(rbf(a) * 0.07216878364870322f);
  }
  SCHED_FENCE();
  STAMP(3);
  unsigned pd[8];
  {
    float m = fmaxf(fmaxf(fmaxf(sc[0], sc[1]), fmaxf(sc[2], sc[3])), fmaxf(fmaxf(sc[4], sc[5]), fmaxf(sc[6], sc[7])));
    m = max_hi(m);
    float l = 0.f;
#pragma unroll
    for (int g = 0; g < 8; ++g) {
      const float p = __builtin_amdgcn_exp2f((sc[g] - m) * 1.4426950408889634f);
      l += p;
      pd[g] = pk2(p, p);
    }
    l = sum_hi(l);
    if (lane == 0) { mls[w] = m; mls[8 + w] = l; }
  }
  SCHED_FENCE();

  {
    f32x4 oacc[3] = {{0.f, 0.f, 0.f, 0.f}, {0.f, 0.f, 0.f, 0.f}, {0.f, 0.f, 0.f, 0.f}};
#pragma unroll
    for (int g = 0; g < 8; ++g) {
      const v4u bf = bfrag(pd[g], mask);
#pragma unroll
      for (int j = 0; j < 3; ++j) oacc[j] = mfma(bf, vv[g][j], oacc[j]);
      SCHED_FENCE();
    }
    float* const myred = red + w * RS + (rq >> 1) * 192 + 8 * (col & 7) + 4 * (rq & 1);
    if (dvalid) {
#pragma unroll
      for (int j = 0; j < 3; ++j) *(f32x4*)(myred + j * 64) = oacc[j];
    }
  }
  STAMP(10);
  const unsigned tag = (epoch + 1u) | 0x80000000u;
  CBAR();
  if (lane == 0) __hip_atomic_fetch_add(&flg[3], 1u, __ATOMIC_RELAXED, __HIP_MEMORY_SCOPE_WORKGROUP);
  CBAR();
  const int pw = col & 7, j8 = 2 * rq + (col >> 3);
  const float* const lsrc = red + pw * RS + 48 * (w & 3) + 6 * j8;
  const unsigned yz_off = col < 10 ? (unsigned)((s * 32 + h) * 640 + (4 * col + 2 * (rq & 1) + (rq >> 1)) * 16) : OOB;
  const unsigned vo3 = col < 10 ? (unsigned)(rq * 15360 + col * 16) : OOB;
  const unsigned oaddr = (unsigned)(size_t)((__attribute__((address_space(3))) unsigned*)osh) + (unsigned)(256 * (w & 3) + 32 * j8);
  unsigned mlo8[4], mhi8[4];
#pragma unroll
  for (int i = 0; i < 4; ++i) { mlo8[i] = col < 8 ? mask[i] : 0u; mhi8[i] = col < 8 ? 0u : mask[i]; }
  if (w >= 4) {
    unsigned po = pw < 3 ? WS_P1 + (unsigned)(wg * P1S + (24 * (w & 3) + 8 * pw + j8) * 16) : OOB;
    if (w == 4 && lane == 3) po = WS_ML + (unsigned)wg * 16u;
    const rsrc_t rsWo = mk(wo + (size_t)(192 * h + 48 * (w & 3)) * 640 + 80 * s, 47 * 1280 + 160);
#pragma unroll 1
    for (unsigned spins = 0; spins < (SPIN_CAP << 6); ++spins) {
      if (__hip_atomic_load(&flg[3], __ATOMIC_RELAXED, __HIP_MEMORY_SCOPE_WORKGROUP) >= 8u) break;
      __builtin_amdgcn_s_sleep(1);
    }
    CBAR();
    {
      f32x2 t[3];
#pragma unroll
      for (int i = 0; i < 3; ++i) t[i] = *(const f32x2*)(lsrc + 2 * i) + *(const f32x2*)(lsrc + 192 + 2 * i);
      const float m = mls[pw], l = mls[8 + pw];
      const float M = max8(m);
      const float f = __builtin_amdgcn_exp2f((m - M) * 1.4426950408889634f);
      const float L = sum8(f * l);
#pragma unroll
      for (int i = 0; i < 3; ++i) { t[i].x = sum8(f * t[i].x); t[i].y = sum8(f * t[i].y); }
      const float e0 = pw == 0 ? t[0].x : pw == 1 ? t[1].x : pw == 2 ? t[2].x : M;
      const float e1 = pw == 0 ? t[0].y : pw == 1 ? t[1].y : pw == 2 ? t[2].y : L;
      const v4u ga = {asu(e0), tag, asu(e1), tag};
      __builtin_amdgcn_raw_buffer_store_b128(ga, rsWs, po, 0, AUX_SC1);
    }
    CBAR();
    if (lane == 0) __hip_atomic_fetch_add(&flg[7], 1u, __ATOMIC_RELAXED, __HIP_MEMORY_SCOPE_WORKGROUP);
    CBAR();
    STAMP(12);
    SCHED_FENCE();
    if (WOSL) __builtin_amdgcn_s_sleep(WOSL);
    if (WE < 12) {
    v4u gv[12];
#pragma unroll
    for (int g = WE; g < 12; ++g) gv[g] = __builtin_amdgcn_raw_buffer_load_b128(rsWo, vo3, g * 1280, AUX_NT);
    SCHED_FENCE();
    STAMP(4);
    v4u oa, ob;
#pragma unroll 1
    for (unsigned spins = 0; spins < (SPIN_CAP << 6); ++spins) {
      asm volatile("ds_read_b128 %0, %2 offset:16\n ds_read_b128 %1, %2\n s_waitcnt lgkmcnt(0)"
                   : "=&v"(ob), "=&v"(oa) : "v"(oaddr) : "memory");
      if (__all(ob.z != 0u)) break;
      __builtin_amdgcn_s_sleep(1);
    }
    STAMP(13);
    const unsigned od[6] = {oa.x, oa.y, oa.z, oa.w, ob.x, ob.y};
    f32x4 ya = {0.f, 0.f, 0.f, 0.f}, yb = {0.f, 0.f, 0.f, 0.f};
#pragma unroll
    for (int i = WE; i < 12; ++i) {
      if (i & 1) yb = mfma(bfrag(od[i % 6], i < 6 ? mlo8 : mhi8), gv[i], yb);
      else ya = mfma(bfrag(od[i % 6], i < 6 ? mlo8 : mhi8), gv[i], ya);
    }
    Y_FINISH();
    }
  } else {
    if (w == 3) {
      const rsrc_t rsW2 = mk(w2 + (size_t)(48 * h) * 640 + 80 * s, 47 * 1280 + 160);
      v4u gz[12];
#pragma unroll
      for (int g = 0; g < 12; ++g) gz[g] = __builtin_amdgcn_raw_buffer_load_b128(rsW2, vo3, g * 1280, AUX_NT);
      const v2u* hb = (const v2u*)(qs + 96 + 6 * rq);
      const v2u h0 = hb[0], h1 = hb[1], h2 = hb[2];
      const unsigned hv[6] = {h0.x, h0.y, h1.x, h1.y, h2.x, h2.y};
      f32x4 za = {0.f, 0.f, 0.f, 0.f}, zb = {0.f, 0.f, 0.f, 0.f};
#pragma unroll
      for (int i = 0; i < 6; ++i) {
        za = mfma(bfrag(__builtin_amdgcn_perm(hv[i], hv[i], 0x01000100u), mask), gz[2 * i], za);
        zb = mfma(bfrag(__builtin_amdgcn_perm(hv[i], hv[i], 0x03020302u), mask), gz[2 * i + 1], zb);
      }
      za += zb;
      const v4u g = {asu(rq < 2 ? za[0] : za[2]), tag, asu(rq < 2 ? za[1] : za[3]), tag};
      __builtin_amdgcn_raw_buffer_store_b128(g, rsWs, yz_off + WS_Z, 0, AUX_SC1);
      asm volatile("s_waitcnt vmcnt(0)" ::: "memory");
      SCHED_FENCE();
      STAMP(14);
    }
    const int n0 = 80 * s + 4 * h + 2 * (lane >> 5);
    unsigned xin = 0u;
    if (w == 0) {
      xin = __builtin_amdgcn_raw_buffer_load_b32(rsX, (unsigned)n0 * 2u, 0, 0);
      asm volatile("" : "+v"(xin));
    }
    v4u gp[4 * NSET + 1], gv[12];
    if (WE) {
      const v4u srdWo = srd(wo + (size_t)(192 * h + 48 * w) * 640 + 80 * s, 47 * 1280 + 160);
#pragma unroll 1
      for (unsigned spins = 0; spins < (SPIN_CAP << 6); ++spins) {
        if (__hip_atomic_load(&flg[2], __ATOMIC_RELAXED, __HIP_MEMORY_SCOPE_WORKGROUP) >= GVCNT) break;
        __builtin_amdgcn_s_sleep(1);
      }
      CBAR();
      LOAD_WO();
      STAMP(4);
    }
    const unsigned go = pw == s ? OOB : WS_P1 + (unsigned)((h * 8 + pw) * P1S + (24 * w + j8) * 16);
    const unsigned g0 = DIRECT0 ? go : OOB;
    unsigned mo = pw == s ? OOB : WS_ML + (unsigned)(h * 8 + pw) * 16u;
    unsigned long long pm = ~(0x0101010101010101ull << s), pd = pm, sa;
    unsigned cnt = SPIN_CAP, sc, t0, t1, t2, t3;
#pragma unroll 1
    for (unsigned spins = 0; spins < (SPIN_CAP << 6); ++spins) {
      if (__hip_atomic_load(&flg[PG2 ? 7 : 3], __ATOMIC_RELAXED, __HIP_MEMORY_SCOPE_WORKGROUP) >= (PG2 ? PG2 : 8u)) break;
      __builtin_amdgcn_s_sleep(1);
    }
    CBAR();
    STAMP(15);
    P1_ISSUE0(gp);
    unsigned fv[8];
    {
      f32x2 t[3];
#pragma unroll
      for (int i = 0; i < 3; ++i) t[i] = *(const f32x2*)(lsrc + 2 * i) + *(const f32x2*)(lsrc + 192 + 2 * i);
      const float m = mls[pw], l = mls[8 + pw];
      const float M = max8(m);
      const float f = __builtin_amdgcn_exp2f((m - M) * 1.4426950408889634f);
      fv[6] = asu(M);
      fv[7] = asu(sum8(f * l));
#pragma unroll
      for (int i = 0; i < 3; ++i) { fv[2 * i] = asu(sum8(f * t[i].x)); fv[2 * i + 1] = asu(sum8(f * t[i].y)); }
    }
    P1_LOOP(gp);
    READY_WO();
    STAMP(5);
    {
      const float M = asf(fv[6]);
      const float Mg = max8(M);
      const float fr = __builtin_amdgcn_exp2f((M - Mg) * 1.4426950408889634f);
      const float rden = __builtin_amdgcn_rcpf(sum8(fr * asf(fv[7])));
      unsigned od[6];
#pragma unroll
      for (int i = 0; i < 6; ++i) {
        const float n = sum8(fr * asf(fv[i])) * rden;
        od[i] = pk2(n, n);
      }
      const v4u oa = {od[0], od[1], od[2], od[3]}, ob = {od[4], od[5], 1u, 1u};
      if (WE < 12 && pw == 0)
        asm volatile("ds_write_b128 %0, %1\n ds_write_b128 %0, %2 offset:16" :: "v"(oaddr), "v"(oa), "v"(ob) : "memory");
      if (WE) {
        f32x4 ya = {0.f, 0.f, 0.f, 0.f}, yb = {0.f, 0.f, 0.f, 0.f};
#pragma unroll
        for (int i = 0; i < WE; ++i) {
          if (i & 1) yb = mfma(bfrag(od[i % 6], i < 6 ? mlo8 : mhi8), gv[i], yb);
          else ya = mfma(bfrag(od[i % 6], i < 6 ? mlo8 : mhi8), gv[i], ya);
        }
        Y_FINISH();
      }
    }
    P1_DRAIN(gp);
    if (w == 0 && h < 20) {
      const unsigned gbase = (unsigned)((s * 32 + (lane & 31)) * 640 + (2 * h + (lane >> 5)) * 16);
#pragma unroll 1
      for (unsigned spins = 0; spins < (SPIN_CAP << 6); ++spins) {
        if (__hip_atomic_load(&flg[6], __ATOMIC_RELAXED, __HIP_MEMORY_SCOPE_WORKGROUP)) break;
        __builtin_amdgcn_s_sleep(1);
      }
      STAMP(13);
      unsigned y0 = 0u, y1 = 0u, go = gbase, cnt = SPIN_CAP, sc, t0;
      unsigned long long pend = ~0ull;
      v4u gq[5];
      P2_LOOP(gq);
      STAMP(8);
      v4u gzz = gq[4];
#pragma unroll 1
      for (unsigned spins = 0; spins < SPIN_CAP; ++spins) {
        if (__all((gzz.y == tag) & (gzz.w == tag))) break;
        CBAR();
        gzz = __builtin_amdgcn_raw_buffer_load_b128(rsWs, gbase, WS_Z, AUX_SC1);
        asm volatile("" : "+v"(gzz));
      }
      const float z0 = rbf(sum32(asf(gzz.x))), z1 = rbf(sum32(asf(gzz.z)));
      const float a0 = sum32(asf(y0)), a1 = sum32(asf(y1));
      if ((lane & 31) == 0) {
        const float r0 = rbf(rbf(bflo(xin) + rbf(a0)) + z0);
        const float r1 = rbf(rbf(bfhi(xin) + rbf(a1)) + z1);
        *(unsigned*)(out + n0) = pk2(r0, r1);
      }
      if (wg == 0 && lane == 0) __builtin_amdgcn_raw_buffer_store_b32(epoch + 1u, rsWs, 0, 0, AUX_SC1);
      P2_DRAIN(gq);
    }
  }
}

}

extern "C" void kernel_launch(void* const* d_in, const int* in_sizes, int n_in, void* d_out, int out_size, void* d_ws,
                              size_t ws_size, hipStream_t stream) {
  (void)in_sizes; (void)n_in; (void)out_size;
  if (ws_size < WS_BYTES) return;
  fused_decode_kernel<<<dim3(256), dim3(512), 0, stream>>>(
      (const unsigned short*)d_in[0], (const unsigned short*)d_in[1], (const unsigned short*)d_in[2],
      (const unsigned short*)d_in[3], (const unsigned short*)d_in[4], (const unsigned short*)d_in[6],
      (const unsigned short*)d_in[7], (unsigned short*)d_out, (unsigned char*)d_ws);
}
```

```cpp
#include <hip/hip_runtime.h>
#include <hip/hip_bf16.h>

namespace {

typedef unsigned v4u __attribute__((ext_vector_type(4)));
typedef unsigned v2u __attribute__((ext_vector_type(2)));
typedef float f32x4 __attribute__((ext_vector_type(4)));
typedef float f32x2 __attribute__((ext_vector_type(2)));
typedef __bf16 bfx8 __attribute__((ext_vector_type(8)));
typedef __bf16 bfx2 __attribute__((ext_vector_type(2)));
typedef __amdgpu_buffer_rsrc_t rsrc_t;

constexpr int AUX_NT = 2;
constexpr int AUX_SC1 = 16;
constexpr int AUX_POLL = (int)(16u | 0x80000000u);
constexpr unsigned OOB = 0x80000000u;
constexpr unsigned SPIN_CAP = 1u << 16;

constexpr unsigned WS_P0 = 4096;
constexpr unsigned P1S = 1664;
constexpr unsigned WS_P1 = WS_P0 + 256u * 256u * 8u;
constexpr unsigned WS_ML = WS_P1 + 256u * P1S;
constexpr unsigned WS_Y = WS_ML + 4096u;
constexpr unsigned WS_Z = WS_Y + 8u * 32u * 640u;
constexpr unsigned WS_BYTES = WS_Z + 8u * 32u * 640u;
constexpr int RS = 392;

#define PG2 4
#define WE 3
#define GVCNT 8
#define WOSL 0
#define FEW 2
#define DIRECT0 1
#define NY 4
#define YFEW 64
#define YSL 8
#define NSET 1
#define PSL 0
#define STAG1 8
#define STAG2 6

__device__ __forceinline__ rsrc_t mk(const void* p, unsigned bytes) {
  return __builtin_amdgcn_make_buffer_rsrc((void*)p, (short)0, (int)bytes, 0x00020000);
}
__device__ __forceinline__ float asf(unsigned u) { return __builtin_bit_cast(float, u); }
__device__ __forceinline__ unsigned asu(float f) { return __builtin_bit_cast(unsigned, f); }
__device__ __forceinline__ float rbf(float f) { return (float)(__bf16)f; }
__device__ __forceinline__ float bflo(unsigned u) { return __builtin_bit_cast(float, u << 16); }
__device__ __forceinline__ float bfhi(unsigned u) { return __builtin_bit_cast(float, u & 0xFFFF0000u); }
__device__ __forceinline__ unsigned pk2(float a, float b) {
  f32x2 v = {a, b};
  return __builtin_bit_cast(unsigned, __builtin_convertvector(v, bfx2));
}
__device__ __forceinline__ f32x4 mfma(v4u a, v4u b, f32x4 c) {
  return __builtin_amdgcn_mfma_f32_16x16x32_bf16(__builtin_bit_cast(bfx8, a), __builtin_bit_cast(bfx8, b), c, 0, 0, 0);
}
__device__ __forceinline__ float dot2(unsigned a, unsigned b, float c) {
  return __builtin_amdgcn_fdot2_f32_bf16(__builtin_bit_cast(bfx2, a), __builtin_bit_cast(bfx2, b), c, false);
}
template <int CTRL> __device__ __forceinline__ float dpp(float v) {
  return __builtin_bit_cast(float, __builtin_amdgcn_mov_dpp(__builtin_bit_cast(int, v), CTRL, 0xF, 0xF, true));
}
__device__ __forceinline__ float sum8(float v) {
  v += dpp<0xB1>(v);
  v += dpp<0x4E>(v);
  v += dpp<0x141>(v);
  return v;
}
__device__ __forceinline__ float max8(float v) {
  v = fmaxf(v, dpp<0xB1>(v));
  v = fmaxf(v, dpp<0x4E>(v));
  v = fmaxf(v, dpp<0x141>(v));
  return v;
}
__device__ __forceinline__ float sum_hi(float v) {
  v += dpp<0x128>(v);
  v += __shfl_xor(v, 16);
  v += __shfl_xor(v, 32);
  return v;
}
__device__ __forceinline__ float max_hi(float v) {
  v = fmaxf(v, dpp<0x128>(v));
  v = fmaxf(v, __shfl_xor(v, 16));
  v = fmaxf(v, __shfl_xor(v, 32));
  return v;
}
__device__ __forceinline__ v4u bfrag(unsigned dup, const unsigned (&mask)[4]) {
  v4u b;
  b.x = dup & mask[0];
  b.y = dup & mask[1];
  b.z = dup & mask[2];
  b.w = dup & mask[3];
  return b;
}
__device__ __forceinline__ float fold32(float a, float b) {
  const auto r = __builtin_amdgcn_permlane32_swap(asu(a), asu(b), false, false);
  return asf(r[0]) + asf(r[1]);
}
__device__ __forceinline__ float sum32(float v) {
  v = sum8(v);
  v += dpp<0x128>(v);
  const auto r = __builtin_amdgcn_permlane16_swap(asu(v), asu(v), false, false);
  return asf(r[0]) + asf(r[1]);
}
#define SCHED_FENCE() __builtin_amdgcn_sched_barrier(0)
#define CBAR() asm volatile("" ::: "memory")
#define STR_(x) #x
#define STR(x) STR_(x)
#define PSL_TXT ""
__device__ __forceinline__ v4u srd(const void* p, unsigned bytes) {
  const unsigned long long a = (unsigned long long)p;
  const v4u r = {(unsigned)a, (unsigned)(a >> 32) & 0xFFFFu, bytes, 0x00020000u};
  return r;
}
#define LWO0 if (WE > 0) asm volatile("s_nop 4\n buffer_load_dwordx4 v[160:163], %1, %2, %3 offen offset:0 nt" : "={v[160:163]}"(gv[0]) : "v"(vo3), "s"(srdWo), "s"(0) : "memory")
#define RWO0 if (WE > 0) asm volatile("" : "+{v[160:163]}"(gv[0]) :: "memory")
#define LWO1 if (WE > 1) asm volatile("s_nop 4\n buffer_load_dwordx4 v[164:167], %1, %2, %3 offen offset:1280 nt" : "={v[164:167]}"(gv[1]) : "v"(vo3), "s"(srdWo), "s"(0) : "memory")
#define RWO1 if (WE > 1) asm volatile("" : "+{v[164:167]}"(gv[1]) :: "memory")
#define LWO2 if (WE > 2) asm volatile("s_nop 4\n buffer_load_dwordx4 v[168:171], %1, %2, %3 offen offset:2560 nt" : "={v[168:171]}"(gv[2]) : "v"(vo3), "s"(srdWo), "s"(0) : "memory")
#define RWO2 if (WE > 2) asm volatile("" : "+{v[168:171]}"(gv[2]) :: "memory")
#define LWO3 if (WE > 3) asm volatile("s_nop 4\n buffer_load_dwordx4 v[172:175], %1, %2, %3 offen offset:3840 nt" : "={v[172:175]}"(gv[3]) : "v"(vo3), "s"(srdWo), "s"(0) : "memory")
#define RWO3 if (WE > 3) asm volatile("" : "+{v[172:175]}"(gv[3]) :: "memory")
#define LWO4 if (WE > 4) asm volatile("s_nop 4\n buffer_load_dwordx4 v[176:179], %1, %2, %3 offen offset:0 nt" : "={v[176:179]}"(gv[4]) : "v"(vo3), "s"(srdWo), "s"(5120) : "memory")
#define RWO4 if (WE > 4) asm volatile("" : "+{v[176:179]}"(gv[4]) :: "memory")
#define LWO5 if (WE > 5) asm volatile("s_nop 4\n buffer_load_dwordx4 v[180:183], %1, %2, %3 offen offset:1280 nt" : "={v[180:183]}"(gv[5]) : "v"(vo3), "s"(srdWo), "s"(5120) : "memory")
#define RWO5 if (WE > 5) asm volatile("" : "+{v[180:183]}"(gv[5]) :: "memory")
#define LWO6 if (WE > 6) asm volatile("s_nop 4\n buffer_load_dwordx4 v[184:187], %1, %2, %3 offen offset:2560 nt" : "={v[184:187]}"(gv[6]) : "v"(vo3), "s"(srdWo), "s"(5120) : "memory")
#define RWO6 if (WE > 6) asm volatile("" : "+{v[184:187]}"(gv[6]) :: "memory")
#define LWO7 if (WE > 7) asm volatile("s_nop 4\n buffer_load_dwordx4 v[188:191], %1, %2, %3 offen offset:3840 nt" : "={v[188:191]}"(gv[7]) : "v"(vo3), "s"(srdWo), "s"(5120) : "memory")
#define RWO7 if (WE > 7) asm volatile("" : "+{v[188:191]}"(gv[7]) :: "memory")
#define LWO8 if (WE > 8) asm volatile("s_nop 4\n buffer_load_dwordx4 v[192:195], %1, %2, %3 offen offset:0 nt" : "={v[192:195]}"(gv[8]) : "v"(vo3), "s"(srdWo), "s"(10240) : "memory")
#define RWO8 if (WE > 8) asm volatile("" : "+{v[192:195]}"(gv[8]) :: "memory")
#define LWO9 if (WE > 9) asm volatile("s_nop 4\n buffer_load_dwordx4 v[196:199], %1, %2, %3 offen offset:1280 nt" : "={v[196:199]}"(gv[9]) : "v"(vo3), "s"(srdWo), "s"(10240) : "memory")
#define RWO9 if (WE > 9) asm volatile("" : "+{v[196:199]}"(gv[9]) :: "memory")
#define LWO10 if (WE > 10) asm volatile("s_nop 4\n buffer_load_dwordx4 v[200:203], %1, %2, %3 offen offset:2560 nt" : "={v[200:203]}"(gv[10]) : "v"(vo3), "s"(srdWo), "s"(10240) : "memory")
#define RWO10 if (WE > 10) asm volatile("" : "+{v[200:203]}"(gv[10]) :: "memory")
#define LWO11 if (WE > 11) asm volatile("s_nop 4\n buffer_load_dwordx4 v[204:207], %1, %2, %3 offen offset:3840 nt" : "={v[204:207]}"(gv[11]) : "v"(vo3), "s"(srdWo), "s"(10240) : "memory")
#define RWO11 if (WE > 11) asm volatile("" : "+{v[204:207]}"(gv[11]) :: "memory")
#define LOAD_WO() do { LWO0; LWO1; LWO2; LWO3; LWO4; LWO5; LWO6; LWO7; LWO8; LWO9; LWO10; LWO11; } while (0)
#define READY_WO() do { RWO0; RWO1; RWO2; RWO3; RWO4; RWO5; RWO6; RWO7; RWO8; RWO9; RWO10; RWO11; } while (0)
#define P1_ISSUE0(G) asm volatile("s_nop 4\n" "buffer_load_dwordx4 v[220:223], %5, %6, 0 offen sc1\n" "buffer_load_dwordx4 v[208:211], %4, %6, 0 offen offset:0 sc1\n" "buffer_load_dwordx4 v[212:215], %4, %6, 0 offen offset:128 sc1\n" "buffer_load_dwordx4 v[216:219], %4, %6, 0 offen offset:256 sc1\n"  \
  : "={v[208:211]}"(G[0]), "={v[212:215]}"(G[1]), "={v[216:219]}"(G[2]), "={v[220:223]}"(G[3]) : "v"(g0), "v"(mo), "s"(srdWs) : "memory")
#define P1_ISSUE1(G) asm volatile("s_nop 4\n" "buffer_load_dwordx4 v[236:239], %5, %6, 0 offen sc1\n" "buffer_load_dwordx4 v[224:227], %4, %6, 0 offen offset:0 sc1\n" "buffer_load_dwordx4 v[228:231], %4, %6, 0 offen offset:128 sc1\n" "buffer_load_dwordx4 v[232:235], %4, %6, 0 offen offset:256 sc1\n"  \
  : "={v[224:227]}"(G[4]), "={v[228:231]}"(G[5]), "={v[232:235]}"(G[6]), "={v[236:239]}"(G[7]) : "v"(g0), "v"(mo), "s"(srdWs) : "memory")
#define P1_ISSUE2(G) asm volatile("s_nop 4\n" "buffer_load_dwordx4 v[252:255], %5, %6, 0 offen sc1\n" "buffer_load_dwordx4 v[240:243], %4, %6, 0 offen offset:0 sc1\n" "buffer_load_dwordx4 v[244:247], %4, %6, 0 offen offset:128 sc1\n" "buffer_load_dwordx4 v[248:251], %4, %6, 0 offen offset:256 sc1\n"  \
  : "={v[240:243]}"(G[8]), "={v[244:247]}"(G[9]), "={v[248:251]}"(G[10]), "={v[252:255]}"(G[11]) : "v"(g0), "v"(mo), "s"(srdWs) : "memory")
#define ZCNT 16
#define P1_LOOP(G) asm volatile( \
      ".Lp1l%=:\n" \
      "s_waitcnt vmcnt(0)\n" \
      "v_bitop3_b32 %[t0], v221, v223, %[tag] bitop3:0x7e\n" \
      "v_bitop3_b32 %[t1], v209, v211, %[tag] bitop3:0x7e\n" \
      "v_bitop3_b32 %[t2], v213, v215, %[tag] bitop3:0x7e\n" \
      "v_bitop3_b32 %[t3], v217, v219, %[tag] bitop3:0x7e\n" \
      "v_cmp_eq_u32 %[sa], 0, %[t0]\n" \
      "v_or3_b32 %[t1], %[t1], %[t2], %[t3]\n" \
      "v_cmp_eq_u32 vcc, 0, %[t1]\n" \
      "s_nop 0\n" \
      "v_cndmask_b32 %[f6], %[f6], v220, %[sa]\n" \
      "v_cndmask_b32 %[f7], %[f7], v222, %[sa]\n" \
      "v_cndmask_b32 %[mo], %[mo], %[oob], %[sa]\n" \
      "v_cndmask_b32 %[f0], %[f0], v208, vcc\n" \
      "v_cndmask_b32 %[f1], %[f1], v210, vcc\n" \
      "v_cndmask_b32 %[f2], %[f2], v212, vcc\n" \
      "v_cndmask_b32 %[f3], %[f3], v214, vcc\n" \
      "v_cndmask_b32 %[f4], %[f4], v216, vcc\n" \
      "v_cndmask_b32 %[f5], %[f5], v218, vcc\n" \
      "s_andn2_b64 %[pm], %[pm], %[sa]\n" \
      "s_andn2_b64 %[pd], %[pd], vcc\n" \
      "s_or_b64 %[sa], %[pm], %[pd]\n" \
      "s_cbranch_scc0 .Lp1x%=\n" \
      "s_bcnt1_i32_b64 %[sc], %[pd]\n" \
      "s_cmp_le_u32 %[sc], " STR(FEW) " * 8\n" \
      "s_cselect_b64 %[sa], 0, %[pm]\n" \
      "s_andn2_b64 %[sa], %[pd], %[sa]\n" \
      "v_cndmask_b32 %[t0], %[oob], %[go], %[sa]\n" \
      PSL_TXT \
      "buffer_load_dwordx4 v[220:223], %[mo], %[rs], 0 offen sc1\n" "buffer_load_dwordx4 v[208:211], %[t0], %[rs], 0 offen offset:0 sc1\n" "buffer_load_dwordx4 v[212:215], %[t0], %[rs], 0 offen offset:128 sc1\n" "buffer_load_dwordx4 v[216:219], %[t0], %[rs], 0 offen offset:256 sc1\n"  \
      "s_sub_u32 %[cnt], %[cnt], 1\n" "s_cbranch_scc0 .Lp1l%=\n" ".Lp1x%=:\n" "s_nop 1\n" \
      : "+{v[208:211]}"(G[0]), "+{v[212:215]}"(G[1]), "+{v[216:219]}"(G[2]), "+{v[220:223]}"(G[3]), \
        [f0] "+v"(fv[0]), [f1] "+v"(fv[1]), [f2] "+v"(fv[2]), [f3] "+v"(fv[3]), [f4] "+v"(fv[4]), [f5] "+v"(fv[5]), [f6] "+v"(fv[6]), [f7] "+v"(fv[7]), \
        [mo] "+v"(mo), [pm] "+s"(pm), [pd] "+s"(pd), [cnt] "+s"(cnt), [sa] "=&s"(sa), [sc] "=&s"(sc), \
        [t0] "=&v"(t0), [t1] "=&v"(t1), [t2] "=&v"(t2), [t3] "=&v"(t3) \
      : [go] "v"(go), [tag] "v"(tag), [oob] "v"(OOB), [rs] "s"(srdWs) : "vcc", "scc", "memory")
#define P1_DRAIN(G) asm volatile("s_waitcnt vmcnt(0)" :: "{v[208:211]}"(G[0]), "{v[212:215]}"(G[1]), "{v[216:219]}"(G[2]), "{v[220:223]}"(G[3]) : "memory")
#define P2_LOOP(G) asm volatile( \
      "s_nop 4\n" "buffer_load_dwordx4 v[224:227], %[go], %[rs], %[sz] offen sc1\n" \
      "buffer_load_dwordx4 v[208:211], %[go], %[rs], %[so] offen sc1\n" "s_sleep " STR(STAG2) "\n" \
      "buffer_load_dwordx4 v[212:215], %[go], %[rs], %[so] offen sc1\n" "s_sleep " STR(STAG2) "\n" \
      "buffer_load_dwordx4 v[216:219], %[go], %[rs], %[so] offen sc1\n" "s_sleep " STR(STAG2) "\n" \
      "buffer_load_dwordx4 v[220:223], %[go], %[rs], %[so] offen sc1\n" \
      ".Lp2l%=:\n" \
      "s_waitcnt vmcnt(3)\n" \
      "v_bitop3_b32 %[t0], v209, v211, %[tag] bitop3:0x7e\n" \
      "v_cmp_eq_u32 vcc, 0, %[t0]\n" \
      "s_nop 1\n" \
      "v_cndmask_b32 %[go], %[go], %[oob], vcc\n" \
      "v_cndmask_b32 %[y0], %[y0], v208, vcc\n" \
      "v_cndmask_b32 %[y1], %[y1], v210, vcc\n" \
      "s_andn2_b64 %[pend], %[pend], vcc\n" \
      "s_cbranch_scc0 .Lp2x%=\n" \
      "s_bcnt1_i32_b64 %[sc], %[pend]\n" \
      "s_cmp_le_u32 %[sc], " STR(YFEW) "\n" \
      "s_cbranch_scc1 .Lp2s0%=\n" \
      "s_sleep " STR(YSL) "\n" \
      ".Lp2s0%=:\n" \
      "buffer_load_dwordx4 v[208:211], %[go], %[rs], %[so] offen sc1\n" \
      "s_waitcnt vmcnt(3)\n" \
      "v_bitop3_b32 %[t0], v213, v215, %[tag] bitop3:0x7e\n" \
      "v_cmp_eq_u32 vcc, 0, %[t0]\n" \
      "s_nop 1\n" \
      "v_cndmask_b32 %[go], %[go], %[oob], vcc\n" \
      "v_cndmask_b32 %[y0], %[y0], v212, vcc\n" \
      "v_cndmask_b32 %[y1], %[y1], v214, vcc\n" \
      "s_andn2_b64 %[pend], %[pend], vcc\n" \
      "s_cbranch_scc0 .Lp2x%=\n" \
      "s_bcnt1_i32_b64 %[sc], %[pend]\n" \
      "s_cmp_le_u32 %[sc], " STR(YFEW) "\n" \
      "s_cbranch_scc1 .Lp2s1%=\n" \
      "s_sleep " STR(YSL) "\n" \
      ".Lp2s1%=:\n" \
      "buffer_load_dwordx4 v[212:215], %[go], %[rs], %[so] offen sc1\n" \
      "s_waitcnt vmcnt(3)\n" \
      "v_bitop3_b32 %[t0], v217, v219, %[tag] bitop3:0x7e\n" \
      "v_cmp_eq_u32 vcc, 0, %[t0]\n" \
      "s_nop 1\n" \
      "v_cndmask_b32 %[go], %[go], %[oob], vcc\n" \
      "v_cndmask_b32 %[y0], %[y0], v216, vcc\n" \
      "v_cndmask_b32 %[y1], %[y1], v218, vcc\n" \
      "s_andn2_b64 %[pend], %[pend], vcc\n" \
      "s_cbranch_scc0 .Lp2x%=\n" \
      "s_bcnt1_i32_b64 %[sc], %[pend]\n" \
      "s_cmp_le_u32 %[sc], " STR(YFEW) "\n" \
      "s_cbranch_scc1 .Lp2s2%=\n" \
      "s_sleep " STR(YSL) "\n" \
      ".Lp2s2%=:\n" \
      "buffer_load_dwordx4 v[216:219], %[go], %[rs], %[so] offen sc1\n" \
      "s_waitcnt vmcnt(3)\n" \
      "v_bitop3_b32 %[t0], v221, v223, %[tag] bitop3:0x7e\n" \
      "v_cmp_eq_u32 vcc, 0, %[t0]\n" \
      "s_nop 1\n" \
      "v_cndmask_b32 %[go], %[go], %[oob], vcc\n" \
      "v_cndmask_b32 %[y0], %[y0], v220, vcc\n" \
      "v_cndmask_b32 %[y1], %[y1], v222, vcc\n" \
      "s_andn2_b64 %[pend], %[pend], vcc\n" \
      "s_cbranch_scc0 .Lp2x%=\n" \
      "s_bcnt1_i32_b64 %[sc], %[pend]\n" \
      "s_cmp_le_u32 %[sc], " STR(YFEW) "\n" \
      "s_cbranch_scc1 .Lp2s3%=\n" \
      "s_sleep " STR(YSL) "\n" \
      ".Lp2s3%=:\n" \
      "buffer_load_dwordx4 v[220:223], %[go], %[rs], %[so] offen sc1\n" \
      "s_sub_u32 %[cnt], %[cnt], 1\n" "s_cbranch_scc0 .Lp2l%=\n" ".Lp2x%=:\n" "s_nop 1\n" \
      : "=&{v[208:211]}"(G[0]), "=&{v[212:215]}"(G[1]), "=&{v[216:219]}"(G[2]), "=&{v[220:223]}"(G[3]), "=&{v[224:227]}"(G[4]), \
        [y0] "+v"(y0), [y1] "+v"(y1), [go] "+v"(go), [pend] "+s"(pend), [cnt] "+s"(cnt), [sc] "=&s"(sc), [t0] "=&v"(t0) \
      : [tag] "v"(tag), [oob] "v"(OOB), [rs] "s"(srdWs), [so] "s"(WS_Y), [sz] "s"(WS_Z) : "vcc", "scc", "memory")
#define P2_DRAIN(G) asm volatile("s_waitcnt vmcnt(0)" :: "{v[208:211]}"(G[0]), "{v[212:215]}"(G[1]), "{v[216:219]}"(G[2]), "{v[220:223]}"(G[3]) : "memory")
#define PK 0
#define PW 0
#define STAMP(k) do { if ((k) == 0 || (k) == PK) { SCHED_FENCE(); if ((k) == 0) st0_ = wall_clock64(); if ((k) == PK) stk_ = wall_clock64(); SCHED_FENCE(); } } while (0)

#define Y_FINISH() do {                                                                                         \
    ya += yb;                                                                                                   \
    f32x2 r;                                                                                                    \
    r.x = fold32(ya[0], ya[2]);                                                                                 \
    r.y = fold32(ya[1], ya[3]);                                                                                 \
    *(f32x2*)(red3 + w * 128 + lane * 2) = r;                                                                   \
    STAMP(6);                                                                                                   \
    CBAR();                                                                                                     \
    unsigned old = 0u;                                                                                          \
    if (lane == 0) old = __hip_atomic_fetch_add(&flg[5], 1u, __ATOMIC_RELAXED, __HIP_MEMORY_SCOPE_WORKGROUP);   \
    old = __builtin_amdgcn_readfirstlane(old);                                                                  \
    CBAR();                                                                                                     \
    if (old == (WE > 0 && WE < 12 ? 7u : 3u)) {                                                                 \
      const float* rp = red3 + lane * 2;                                                                        \
      f32x2 t = {0.f, 0.f};                                                                                     \
      if (WE > 0) t += (*(const f32x2*)(rp) + *(const f32x2*)(rp + 128)) + (*(const f32x2*)(rp + 256) + *(const f32x2*)(rp + 384)); \
      if (WE < 12) t += (*(const f32x2*)(rp + 512) + *(const f32x2*)(rp + 640)) + (*(const f32x2*)(rp + 768) + *(const f32x2*)(rp + 896)); \
      const v4u g = {asu(t.x), tag, asu(t.y), tag};                                                             \
      __builtin_amdgcn_raw_buffer_store_b128(g, rsWs, yz_off + WS_Y, 0, AUX_SC1);                               \
      CBAR();                                                                                                   \
      if (lane == 0) __hip_atomic_store(&flg[6], 1u, __ATOMIC_RELAXED, __HIP_MEMORY_SCOPE_WORKGROUP);           \
      CBAR();                                                                                                   \
      STAMP(7);                                                                                                 \
    }                                                                                                           \
  } while (0)

__global__ void __launch_bounds__(512, 2)
fused_decode_kernel(const unsigned short* __restrict__ x, const unsigned short* __restrict__ kc,
                    const unsigned short* __restrict__ vc, const unsigned short* __restrict__ wq,
                    const unsigned short* __restrict__ wo, const unsigned short* __restrict__ w1,
                    const unsigned short* __restrict__ w2, unsigned short* __restrict__ out,
                    unsigned char* ws) {
  __shared__ __attribute__((aligned(16))) float red[8 * RS];
  __shared__ __attribute__((aligned(16))) float red3[8 * 128];
  __shared__ __attribute__((aligned(16))) unsigned qs[128];
  __shared__ __attribute__((aligned(16))) float mls[16];
  __shared__ __attribute__((aligned(16))) unsigned osh[4 * 64];
  __shared__ unsigned flg[8];

  unsigned long long st0_ = 0, stk_ = 0;
  STAMP(0);
  const int tid = threadIdx.x;
  const int lane = tid & 63;
  const int w = __builtin_amdgcn_readfirstlane(tid >> 6);
  const int lg = lane >> 3, lc = lane & 7;
  const int col = lane & 15, rq = lane >> 4;
  const int b = blockIdx.x;
  const int h = 4 * (b & 7) + (b >> 6);
  const int s = (b >> 3) & 7;
  const int wg = h * 8 + s;

  const rsrc_t rsX = mk(x, 1280);
  const rsrc_t rsWs = mk(ws, WS_BYTES);
  const v4u srdWs = srd(ws, WS_BYTES);
  const size_t kvoff = ((size_t)h * 32768 + 28672 + 512 * s + 64 * w) * 192;
  const rsrc_t rsK = mk(kc + kvoff, 64 * 384);
  const rsrc_t rsV = mk(vc + kvoff, 64 * 384);

  const unsigned epoch = __builtin_amdgcn_raw_buffer_load_b32(rsWs, 0, 0, AUX_SC1);
  if (tid < 8) flg[tid] = 0u;
  if (tid >= 256) osh[tid - 256] = 0u;

  unsigned mask[4];
  {
    const unsigned mm = (lc & 1) ? 0xFFFF0000u : 0x0000FFFFu;
#pragma unroll
    for (int i = 0; i < 4; ++i) mask[i] = ((lc >> 1) == i) ? mm : 0u;
  }
  const bool dvalid = (rq >> 1) == (col >> 3);
  const unsigned vkv = (unsigned)(lg * 384 + lc * 16);
  v4u kv[8][3], vv[8][3];

  if (w < 2) {
    __builtin_amdgcn_s_setprio(3);
    const v4u xs0 = __builtin_amdgcn_raw_buffer_load_b128(rsX, 20 * lane, 0, 0);
    const unsigned xs1 = __builtin_amdgcn_raw_buffer_load_b32(rsX, 20 * lane + 16, 0, 0);
    const v4u xr0 = __builtin_amdgcn_raw_buffer_load_b128(rsX, 160 * s + 20 * lg, 0, 0);
    const unsigned xr1 = __builtin_amdgcn_raw_buffer_load_b32(rsX, 160 * s + 20 * lg + 16, 0, 0);
    const unsigned short* pa = wq + (size_t)(80 * s) * 6144 + 192 * h + 128 * w;
    const unsigned short* pb = w ? w1 + (size_t)(80 * s) * 1536 + 48 * h : pa + 64;
    const int sb = w ? 3072 : 12288;
    const rsrc_t rsA = mk(pa, 79 * 12288 + 128);
    const rsrc_t rsB = mk(pb, w ? 79 * 3072 + 96 : 79 * 12288 + 128);
    const unsigned voa = (unsigned)(lg * 122880 + lc * 16);
    const unsigned vob = (w && lc >= 6) ? OOB : (unsigned)(lg * 10 * sb + lc * 16);
    v4u wa[10], wb[10];
#pragma unroll
    for (int g = 0; g < 10; ++g) {
      wa[g] = __builtin_amdgcn_raw_buffer_load_b128(rsA, voa, g * 12288, AUX_NT);
      wb[g] = __builtin_amdgcn_raw_buffer_load_b128(rsB, vob, g * sb, AUX_NT);
      SCHED_FENCE();
    }
    const unsigned p0_off = dvalid ? WS_P0 + (unsigned)(wg * 256 + 128 * w + 64 * (rq >> 1) + 8 * (col & 7) + 4 * (rq & 1)) * 8u : OOB;
    SCHED_FENCE();
    __syncthreads();
    SCHED_FENCE();
    float mb, sd;
    {
      float e[10];
      e[0] = bflo(xs0.x); e[1] = bfhi(xs0.x); e[2] = bflo(xs0.y); e[3] = bfhi(xs0.y);
      e[4] = bflo(xs0.z); e[5] = bfhi(xs0.z); e[6] = bflo(xs0.w); e[7] = bfhi(xs0.w);
      e[8] = bflo(xs1);   e[9] = bfhi(xs1);
      float t = 0.f;
#pragma unroll
      for (int i = 0; i < 10; ++i) t += e[i];
      t = sum_hi(sum8(t));
      const float mean = t * (1.0f / 640.0f);
      float q2 = 0.f;
#pragma unroll
      for (int i = 0; i < 10; ++i) { const float d = e[i] - mean; q2 += d * d; }
      q2 = sum_hi(sum8(q2));
      const float var = rbf(q2 * (1.0f / 640.0f));
      mb = rbf(mean);
      sd = rbf(sqrtf(rbf(var + 1e-5f)));
    }
    const unsigned xr[5] = {xr0.x, xr0.y, xr0.z, xr0.w, xr1};
    const f32x4 zero = {0.f, 0.f, 0.f, 0.f};
    f32x4 a0 = zero, a1 = zero, b0 = zero, b1 = zero;
#pragma unroll
    for (int g = 0; g < 5; ++g) {
      const float n0 = rbf(bflo(xr[g]) - mb) / sd, n1 = rbf(bfhi(xr[g]) - mb) / sd;
      const v4u f0 = bfrag(pk2(n0, n0), mask), f1 = bfrag(pk2(n1, n1), mask);
      a0 = mfma(f0, wa[2 * g], a0);
      b0 = mfma(f0, wb[2 * g], b0);
      a1 = mfma(f1, wa[2 * g + 1], a1);
      b1 = mfma(f1, wb[2 * g + 1], b1);
    }
    a0 += a1;
    b0 += b1;
    const unsigned tag = (epoch + 1u) | 0x80000000u;
    {
      const bool p = rq >= 2;
      float r[4];
#pragma unroll
      for (int i = 0; i < 4; ++i) r[i] = (p ? b0[i] : a0[i]) + __shfl_xor(p ? a0[i] : b0[i], 40);
      const v4u g0 = {asu(r[0]), tag, asu(r[1]), tag}, g1 = {asu(r[2]), tag, asu(r[3]), tag};
      __builtin_amdgcn_raw_buffer_store_b128(g0, rsWs, p0_off, 0, AUX_SC1);
      __builtin_amdgcn_raw_buffer_store_b128(g1, rsWs, p0_off + 16, 0, AUX_SC1);
    }
    STAMP(1);
    __builtin_amdgcn_s_setprio(0);
    SCHED_FENCE();
#pragma unroll
    for (int g = 0; g < 2; ++g)
#pragma unroll
      for (int j = 0; j < 3; ++j) kv[g][j] = __builtin_amdgcn_raw_buffer_load_b128(rsK, vkv + j * 128, g * 3072, AUX_NT);
  } else {
#pragma unroll
    for (int g = 0; g < 2; ++g)
#pragma unroll
      for (int j = 0; j < 3; ++j) kv[g][j] = __builtin_amdgcn_raw_buffer_load_b128(rsK, vkv + j * 128, g * 3072, AUX_NT);
    SCHED_FENCE();
    __syncthreads();
  }
  SCHED_FENCE();
#pragma unroll
  for (int g = 2; g < 8; ++g) {
#pragma unroll
    for (int j = 0; j < 3; ++j) kv[g][j] = __builtin_amdgcn_raw_buffer_load_b128(rsK, vkv + j * 128, g * 3072, AUX_NT);
    SCHED_FENCE();
  }
#pragma unroll
  for (int g = 0; g < 8; ++g) {
#pragma unroll
    for (int j = 0; j < 3; ++j) vv[g][j] = __builtin_amdgcn_raw_buffer_load_b128(rsV, vkv + j * 128, g * 3072, AUX_NT);
    SCHED_FENCE();
  }
  STAMP(9);
  if (WE && lane == 0) __hip_atomic_fetch_add(&flg[2], 1u, __ATOMIC_RELAXED, __HIP_MEMORY_SCOPE_WORKGROUP);
  if (w == 2 || w == 3) {
    const int t2 = tid - 128;
    const unsigned tag = (epoch + 1u) | 0x80000000u;
    const unsigned gbase = WS_P0 + (unsigned)(h * 8 * 256 + 2 * t2) * 8u;
    v4u g[8];
#pragma unroll 1
    for (unsigned spins = 0; spins < SPIN_CAP; ++spins) {
      asm volatile("" ::: "memory");
#pragma unroll
      for (int i = 0; i < 8; ++i) g[i] = __builtin_amdgcn_raw_buffer_load_b128(rsWs, gbase, i * 2048, AUX_SC1);
      bool ok = true;
#pragma unroll
      for (int i = 0; i < 8; ++i) ok &= (g[i].y == tag) & (g[i].w == tag);
      if (__all(ok)) break;
      __builtin_amdgcn_s_sleep(1);
    }
    float t0 = 0.f, t1 = 0.f;
#pragma unroll
    for (int i = 0; i < 8; ++i) {
      t0 += asf(g[i].x);
      t1 += asf(g[i].z);
    }
    if (t2 >= 96) { t0 = fmaxf(t0, 0.f); t1 = fmaxf(t1, 0.f); }
    qs[t2] = pk2(t0, t1);
    __builtin_amdgcn_fence(__ATOMIC_RELEASE, "workgroup");
    if (lane == 0) __hip_atomic_store(&flg[w - 2], 1u, __ATOMIC_RELAXED, __HIP_MEMORY_SCOPE_WORKGROUP);
    STAMP(2);
  }
#pragma unroll 1
  for (unsigned spins = 0; spins < (SPIN_CAP << 6); ++spins) {
    const unsigned f0 = __hip_atomic_load(&flg[0], __ATOMIC_RELAXED, __HIP_MEMORY_SCOPE_WORKGROUP);
    const unsigned f1 = __hip_atomic_load(&flg[1], __ATOMIC_RELAXED, __HIP_MEMORY_SCOPE_WORKGROUP);
    if (f0 & f1) break;
    __builtin_amdgcn_s_sleep(1);
  }
  __builtin_amdgcn_fence(__ATOMIC_ACQUIRE, "workgroup");
  v4u qf[3];
#pragma unroll
  for (int j = 0; j < 3; ++j) qf[j] = *(const v4u*)(qs + 32 * j + 4 * lc);

  float sc[8];
#pragma unroll
  for (int g = 0; g < 8; ++g) {
    float a = 0.f;
#pragma unroll
    for (int j = 0; j < 3; ++j) {
      a = dot2(kv[g][j].x, qf[j].x, a);
      a = dot2(kv[g][j].y, qf[j].y, a);
      a = dot2(kv[g][j].z, qf[j].z, a);
      a = dot2(kv[g][j].w, qf[j].w, a);
    }
    a = sum8(a);
    sc[g] = rbf(rbf(a) * 0.07216878364870322f);
  }
  SCHED_FENCE();
  STAMP(3);
  unsigned pd[8];
  {
    float m = fmaxf(fmaxf(fmaxf(sc[0], sc[1]), fmaxf(sc[2], sc[3])), fmaxf(fmaxf(sc[4], sc[5]), fmaxf(sc[6], sc[7])));
    m = max_hi(m);
    float l = 0.f;
#pragma unroll
    for (int g = 0; g < 8; ++g) {
      const float p = __builtin_amdgcn_exp2f((sc[g] - m) * 1.4426950408889634f);
      l += p;
      pd[g] = pk2(p, p);
    }
    l = sum_hi(l);
    if (lane == 0) { mls[w] = m; mls[8 + w] = l; }
  }
  SCHED_FENCE();

  {
    f32x4 oacc[3] = {{0.f, 0.f, 0.f, 0.f}, {0.f, 0.f, 0.f, 0.f}, {0.f, 0.f, 0.f, 0.f}};
#pragma unroll
    for (int g = 0; g < 8; ++g) {
      const v4u bf = bfrag(pd[g], mask);
#pragma unroll
      for (int j = 0; j < 3; ++j) oacc[j] = mfma(bf, vv[g][j], oacc[j]);
      SCHED_FENCE();
    }
    float* const myred = red + w * RS + (rq >> 1) * 192 + 8 * (col & 7) + 4 * (rq & 1);
    if (dvalid) {
#pragma unroll
      for (int j = 0; j < 3; ++j) *(f32x4*)(myred + j * 64) = oacc[j];
    }
  }
  STAMP(10);
  const unsigned tag = (epoch + 1u) | 0x80000000u;
  CBAR();
  if (lane == 0) __hip_atomic_fetch_add(&flg[3], 1u, __ATOMIC_RELAXED, __HIP_MEMORY_SCOPE_WORKGROUP);
  CBAR();
  const int pw = col & 7, j8 = 2 * rq + (col >> 3);
  const float* const lsrc = red + pw * RS + 48 * (w & 3) + 6 * j8;
  const unsigned yz_off = col < 10 ? (unsigned)((s * 32 + h) * 640 + (4 * col + 2 * (rq & 1) + (rq >> 1)) * 16) : OOB;
  const unsigned vo3 = col < 10 ? (unsigned)(rq * 15360 + col * 16) : OOB;
  const unsigned oaddr = (unsigned)(size_t)((__attribute__((address_space(3))) unsigned*)osh) + (unsigned)(256 * (w & 3) + 32 * j8);
  unsigned mlo8[4], mhi8[4];
#pragma unroll
  for (int i = 0; i < 4; ++i) { mlo8[i] = col < 8 ? mask[i] : 0u; mhi8[i] = col < 8 ? 0u : mask[i]; }
  if (w >= 4) {
    unsigned po = pw < 3 ? WS_P1 + (unsigned)(wg * P1S + (24 * (w & 3) + 8 * pw + j8) * 16) : OOB;
    if (w == 4 && lane == 3) po = WS_ML + (unsigned)wg * 16u;
    const rsrc_t rsWo = mk(wo + (size_t)(192 * h + 48 * (w & 3)) * 640 + 80 * s, 47 * 1280 + 160);
#pragma unroll 1
    for (unsigned spins = 0; spins < (SPIN_CAP << 6); ++spins) {
      if (__hip_atomic_load(&flg[3], __ATOMIC_RELAXED, __HIP_MEMORY_SCOPE_WORKGROUP) >= 8u) break;
      __builtin_amdgcn_s_sleep(1);
    }
    CBAR();
    {
      f32x2 t[3];
#pragma unroll
      for (int i = 0; i < 3; ++i) t[i] = *(const f32x2*)(lsrc + 2 * i) + *(const f32x2*)(lsrc + 192 + 2 * i);
      const float m = mls[pw], l = mls[8 + pw];
      const float M = max8(m);
      const float f = __builtin_amdgcn_exp2f((m - M) * 1.4426950408889634f);
      const float L = sum8(f * l);
#pragma unroll
      for (int i = 0; i < 3; ++i) { t[i].x = sum8(f * t[i].x); t[i].y = sum8(f * t[i].y); }
      const float e0 = pw == 0 ? t[0].x : pw == 1 ? t[1].x : pw == 2 ? t[2].x : M;
      const float e1 = pw == 0 ? t[0].y : pw == 1 ? t[1].y : pw == 2 ? t[2].y : L;
      const v4u ga = {asu(e0), tag, asu(e1), tag};
      __builtin_amdgcn_raw_buffer_store_b128(ga, rsWs, po, 0, AUX_SC1);
    }
    CBAR();
    if (lane == 0) __hip_atomic_fetch_add(&flg[7], 1u, __ATOMIC_RELAXED, __HIP_MEMORY_SCOPE_WORKGROUP);
    CBAR();
    STAMP(12);
    SCHED_FENCE();
    if (WOSL) __builtin_amdgcn_s_sleep(WOSL);
    if (WE < 12) {
    v4u gv[12];
#pragma unroll
    for (int g = WE; g < 12; ++g) gv[g] = __builtin_amdgcn_raw_buffer_load_b128(rsWo, vo3, g * 1280, AUX_NT);
    SCHED_FENCE();
    STAMP(4);
    v4u oa, ob;
#pragma unroll 1
    for (unsigned spins = 0; spins < (SPIN_CAP << 6); ++spins) {
      asm volatile("ds_read_b128 %0, %2 offset:16\n ds_read_b128 %1, %2\n s_waitcnt lgkmcnt(0)"
                   : "=&v"(ob), "=&v"(oa) : "v"(oaddr) : "memory");
      if (__all(ob.z != 0u)) break;
      __builtin_amdgcn_s_sleep(1);
    }
    STAMP(13);
    const unsigned od[6] = {oa.x, oa.y, oa.z, oa.w, ob.x, ob.y};
    f32x4 ya = {0.f, 0.f, 0.f, 0.f}, yb = {0.f, 0.f, 0.f, 0.f};
#pragma unroll
    for (int i = WE; i < 12; ++i) {
      if (i & 1) yb = mfma(bfrag(od[i % 6], i < 6 ? mlo8 : mhi8), gv[i], yb);
      else ya = mfma(bfrag(od[i % 6], i < 6 ? mlo8 : mhi8), gv[i], ya);
    }
    Y_FINISH();
    }
  } else {
    if (w == 3) {
      const rsrc_t rsW2 = mk(w2 + (size_t)(48 * h) * 640 + 80 * s, 47 * 1280 + 160);
      v4u gz[12];
#pragma unroll
      for (int g = 0; g < 12; ++g) gz[g] = __builtin_amdgcn_raw_buffer_load_b128(rsW2, vo3, g * 1280, AUX_NT);
      const v2u* hb = (const v2u*)(qs + 96 + 6 * rq);
      const v2u h0 = hb[0], h1 = hb[1], h2 = hb[2];
      const unsigned hv[6] = {h0.x, h0.y, h1.x, h1.y, h2.x, h2.y};
      f32x4 za = {0.f, 0.f, 0.f, 0.f}, zb = {0.f, 0.f, 0.f, 0.f};
#pragma unroll
      for (int i = 0; i < 6; ++i) {
        za = mfma(bfrag(__builtin_amdgcn_perm(hv[i], hv[i], 0x01000100u), mask), gz[2 * i], za);
        zb = mfma(bfrag(__builtin_amdgcn_perm(hv[i], hv[i], 0x03020302u), mask), gz[2 * i + 1], zb);
      }
      za += zb;
      const v4u g = {asu(rq < 2 ? za[0] : za[2]), tag, asu(rq < 2 ? za[1] : za[3]), tag};
      __builtin_amdgcn_raw_buffer_store_b128(g, rsWs, yz_off + WS_Z, 0, AUX_SC1);
      asm volatile("s_waitcnt vmcnt(0)" ::: "memory");
      SCHED_FENCE();
      STAMP(14);
    }
    const int n0 = 80 * s + 4 * h + 2 * (lane >> 5);
    unsigned xin = 0u;
    if (w == 0) {
      xin = __builtin_amdgcn_raw_buffer_load_b32(rsX, (unsigned)n0 * 2u, 0, 0);
      asm volatile("" : "+v"(xin));
    }
    v4u gp[4 * NSET + 1], gv[12];
    if (WE) {
      const v4u srdWo = srd(wo + (size_t)(192 * h + 48 * w) * 640 + 80 * s, 47 * 1280 + 160);
#pragma unroll 1
      for (unsigned spins = 0; spins < (SPIN_CAP << 6); ++spins) {
        if (__hip_atomic_load(&flg[2], __ATOMIC_RELAXED, __HIP_MEMORY_SCOPE_WORKGROUP) >= GVCNT) break;
        __builtin_amdgcn_s_sleep(1);
      }
      CBAR();
      LOAD_WO();
      STAMP(4);
    }
    const unsigned go = pw == s ? OOB : WS_P1 + (unsigned)((h * 8 + pw) * P1S + (24 * w + j8) * 16);
    const unsigned g0 = DIRECT0 ? go : OOB;
    unsigned mo = pw == s ? OOB : WS_ML + (unsigned)(h * 8 + pw) * 16u;
    unsigned long long pm = ~(0x0101010101010101ull << s), pd = pm, sa;
    unsigned cnt = SPIN_CAP, sc, t0, t1, t2, t3;
#pragma unroll 1
    for (unsigned spins = 0; spins < (SPIN_CAP << 6); ++spins) {
      if (__hip_atomic_load(&flg[PG2 ? 7 : 3], __ATOMIC_RELAXED, __HIP_MEMORY_SCOPE_WORKGROUP) >= (PG2 ? PG2 : 8u)) break;
      __builtin_amdgcn_s_sleep(1);
    }
    CBAR();
    STAMP(15);
    P1_ISSUE0(gp);
    unsigned fv[8];
    {
      f32x2 t[3];
#pragma unroll
      for (int i = 0; i < 3; ++i) t[i] = *(const f32x2*)(lsrc + 2 * i) + *(const f32x2*)(lsrc + 192 + 2 * i);
      const float m = mls[pw], l = mls[8 + pw];
      const float M = max8(m);
      const float f = __builtin_amdgcn_exp2f((m - M) * 1.4426950408889634f);
      fv[6] = asu(M);
      fv[7] = asu(sum8(f * l));
#pragma unroll
      for (int i = 0; i < 3; ++i) { fv[2 * i] = asu(sum8(f * t[i].x)); fv[2 * i + 1] = asu(sum8(f * t[i].y)); }
    }
    P1_LOOP(gp);
    READY_WO();
    STAMP(5);
    {
      const float M = asf(fv[6]);
      const float Mg = max8(M);
      const float fr = __builtin_amdgcn_exp2f((M - Mg) * 1.4426950408889634f);
      const float rden = __builtin_amdgcn_rcpf(sum8(fr * asf(fv[7])));
      unsigned od[6];
#pragma unroll
      for (int i = 0; i < 6; ++i) {
        const float n = sum8(fr * asf(fv[i])) * rden;
        od[i] = pk2(n, n);
      }
      const v4u oa = {od[0], od[1], od[2], od[3]}, ob = {od[4], od[5], 1u, 1u};
      if (WE < 12 && pw == 0)
        asm volatile("ds_write_b128 %0, %1\n ds_write_b128 %0, %2 offset:16" :: "v"(oaddr), "v"(oa), "v"(ob) : "memory");
      if (WE) {
        f32x4 ya = {0.f, 0.f, 0.f, 0.f}, yb = {0.f, 0.f, 0.f, 0.f};
#pragma unroll
        for (int i = 0; i < WE; ++i) {
          if (i & 1) yb = mfma(bfrag(od[i % 6], i < 6 ? mlo8 : mhi8), gv[i], yb);
          else ya = mfma(bfrag(od[i % 6], i < 6 ? mlo8 : mhi8), gv[i], ya);
        }
        Y_FINISH();
      }
    }
    P1_DRAIN(gp);
    if (w == 0 && h < 20) {
      const unsigned gbase = (unsigned)((s * 32 + (lane & 31)) * 640 + (2 * h + (lane >> 5)) * 16);
#pragma unroll 1
      for (unsigned spins = 0; spins < (SPIN_CAP << 6); ++spins) {
        if (__hip_atomic_load(&flg[6], __ATOMIC_RELAXED, __HIP_MEMORY_SCOPE_WORKGROUP)) break;
        __builtin_amdgcn_s_sleep(1);
      }
      STAMP(13);
      unsigned y0 = 0u, y1 = 0u, go = gbase, cnt = SPIN_CAP, sc, t0;
      unsigned long long pend = ~0ull;
      v4u gq[5];
      P2_LOOP(gq);
      STAMP(8);
      v4u gzz = gq[4];
#pragma unroll 1
      for (unsigned spins = 0; spins < SPIN_CAP; ++spins) {
        if (__all((gzz.y == tag) & (gzz.w == tag))) break;
        CBAR();
        gzz = __builtin_amdgcn_raw_buffer_load_b128(rsWs, gbase, WS_Z, AUX_SC1);
        asm volatile("" : "+v"(gzz));
      }
      const float z0 = rbf(sum32(asf(gzz.x))), z1 = rbf(sum32(asf(gzz.z)));
      const float a0 = sum32(asf(y0)), a1 = sum32(asf(y1));
      if ((lane & 31) == 0) {
        const float r0 = rbf(rbf(bflo(xin) + rbf(a0)) + z0);
        const float r1 = rbf(rbf(bfhi(xin) + rbf(a1)) + z1);
        *(unsigned*)(out + n0) = pk2(r0, r1);
      }
      if (wg == 0 && lane == 0) __builtin_amdgcn_raw_buffer_store_b32(epoch + 1u, rsWs, 0, 0, AUX_SC1);
      P2_DRAIN(gq);
    }
  }
  STAMP(11);
  if (w == PW && true && stk_ >= st0_ && stk_ - st0_ < 20000ull) {
    const unsigned long long target = st0_ + 8000ull + 1ull * (stk_ - st0_);
#pragma unroll 1
    for (unsigned spins = 0; spins < (1u << 20); ++spins) {
      if (wall_clock64() >= target) break;
      __builtin_amdgcn_s_sleep(1);
    }
  }

}

}

extern "C" void kernel_launch(void* const* d_in, const int* in_sizes, int n_in, void* d_out, int out_size, void* d_ws,
                              size_t ws_size, hipStream_t stream) {
  (void)in_sizes; (void)n_in; (void)out_size;
  if (ws_size < WS_BYTES) return;
  fused_decode_kernel<<<dim3(256), dim3(512), 0, stream>>>(
      (const unsigned short*)d_in[0], (const unsigned short*)d_in[1], (const unsigned short*)d_in[2],
      (const unsigned short*)d_in[3], (const unsigned short*)d_in[4], (const unsigned short*)d_in[6],
      (const unsigned short*)d_in[7], (unsigned short*)d_out, (unsigned char*)d_ws);
}
```

```cpp
#include <hip/hip_runtime.h>
#include <hip/hip_bf16.h>

namespace {

typedef unsigned v4u __attribute__((ext_vector_type(4)));
typedef unsigned v2u __attribute__((ext_vector_type(2)));
typedef float f32x4 __attribute__((ext_vector_type(4)));
typedef float f32x2 __attribute__((ext_vector_type(2)));
typedef __bf16 bfx8 __attribute__((ext_vector_type(8)));
typedef __bf16 bfx2 __attribute__((ext_vector_type(2)));
typedef __amdgpu_buffer_rsrc_t rsrc_t;

constexpr int AUX_NT = 2;
constexpr int AUX_SC1 = 16;
constexpr int AUX_POLL = (int)(16u | 0x80000000u);
constexpr unsigned OOB = 0x80000000u;
constexpr unsigned SPIN_CAP = 1u << 16;

constexpr unsigned WS_P0 = 4096;
constexpr unsigned WS_P1 = WS_P0 + 256u * 256u * 8u;
constexpr unsigned WS_P2 = WS_P1 + 256u * 256u * 8u;
constexpr unsigned WS_BYTES = WS_P2 + 8u * 32u * 160u * 8u;

#define GVCNT 8

__device__ __forceinline__ rsrc_t mk(const void* p, unsigned bytes) {
  return __builtin_amdgcn_make_buffer_rsrc((void*)p, (short)0, (int)bytes, 0x00020000);
}
__device__ __forceinline__ float asf(unsigned u) { return __builtin_bit_cast(float, u); }
__device__ __forceinline__ unsigned asu(float f) { return __builtin_bit_cast(unsigned, f); }
__device__ __forceinline__ float rbf(float f) { return (float)(__bf16)f; }
__device__ __forceinline__ float bflo(unsigned u) { return __builtin_bit_cast(float, u << 16); }
__device__ __forceinline__ float bfhi(unsigned u) { return __builtin_bit_cast(float, u & 0xFFFF0000u); }
__device__ __forceinline__ unsigned pk2(float a, float b) {
  f32x2 v = {a, b};
  return __builtin_bit_cast(unsigned, __builtin_convertvector(v, bfx2));
}
__device__ __forceinline__ f32x4 mfma(v4u a, v4u b, f32x4 c) {
  return __builtin_amdgcn_mfma_f32_16x16x32_bf16(__builtin_bit_cast(bfx8, a), __builtin_bit_cast(bfx8, b), c, 0, 0, 0);
}
__device__ __forceinline__ float dot2(unsigned a, unsigned b, float c) {
  return __builtin_amdgcn_fdot2_f32_bf16(__builtin_bit_cast(bfx2, a), __builtin_bit_cast(bfx2, b), c, false);
}
template <int CTRL> __device__ __forceinline__ float dpp(float v) {
  return __builtin_bit_cast(float, __builtin_amdgcn_mov_dpp(__builtin_bit_cast(int, v), CTRL, 0xF, 0xF, true));
}
__device__ __forceinline__ float sum8(float v) {
  v += dpp<0xB1>(v);
  v += dpp<0x4E>(v);
  v += dpp<0x141>(v);
  return v;
}
__device__ __forceinline__ float sum_hi(float v) {
  v += dpp<0x128>(v);
  v += __shfl_xor(v, 16);
  v += __shfl_xor(v, 32);
  return v;
}
__device__ __forceinline__ float max_hi(float v) {
  v = fmaxf(v, dpp<0x128>(v));
  v = fmaxf(v, __shfl_xor(v, 16));
  v = fmaxf(v, __shfl_xor(v, 32));
  return v;
}
__device__ __forceinline__ v4u bfrag(unsigned dup, const unsigned (&mask)[4]) {
  v4u b;
  b.x = dup & mask[0];
  b.y = dup & mask[1];
  b.z = dup & mask[2];
  b.w = dup & mask[3];
  return b;
}
#define SCHED_FENCE() __builtin_amdgcn_sched_barrier(0)
constexpr unsigned WS_TS = 8u << 20;
#define STAMP(k) do { if ((k) == 0 && 1) { SCHED_FENCE(); const unsigned long long t_ = wall_clock64(); SCHED_FENCE(); if (blockIdx.x == 255 && threadIdx.x == 0) *(unsigned long long*)(ws + WS_TS) = t_; } } while (0)
__global__ void __launch_bounds__(64) ka_tiny(unsigned char* ws) {
  const unsigned long long t_ = wall_clock64();
  if (threadIdx.x == 0) *(unsigned long long*)(ws + WS_TS) = t_;
}
__global__ void __launch_bounds__(512, 2) ka_big(unsigned char* ws) {
  __shared__ float lds[4300];
  const unsigned long long t_ = wall_clock64();
  asm volatile("" ::: "v240");
  lds[threadIdx.x] = 1.f;
  __syncthreads();
  if (blockIdx.x == 255 && threadIdx.x == 0) *(unsigned long long*)(ws + WS_TS) = t_ + (lds[5] != 1.f);
}
__device__ __forceinline__ void waitts(const unsigned char* ws) {
  const unsigned long long t0 = *(const volatile unsigned long long*)(ws + WS_TS);
  const unsigned long long now = wall_clock64();
  if (now >= t0 && now - t0 < 15000ull) {
#pragma unroll 1
    for (unsigned spins = 0; spins < (1u << 20); ++spins) {
      if (wall_clock64() >= t0 + 15000ull) break;
      __builtin_amdgcn_s_sleep(1);
    }
  }
}
__global__ void __launch_bounds__(64) kc_tiny(unsigned char* ws) { waitts(ws); }
__global__ void __launch_bounds__(512, 2) kc_big(unsigned char* ws) {
  __shared__ float lds[4300];
  asm volatile("" ::: "v240");
  lds[threadIdx.x] = 1.f;
  __syncthreads();
  waitts(ws);
  if (lds[5] != 1.f) ws[WS_TS + 64] = 1;
}

__global__ void __launch_bounds__(512, 2)
fused_decode_kernel(const unsigned short* __restrict__ x, const unsigned short* __restrict__ kc,
                    const unsigned short* __restrict__ vc, const unsigned short* __restrict__ wq,
                    const unsigned short* __restrict__ wo, const unsigned short* __restrict__ w1,
                    const unsigned short* __restrict__ w2, unsigned short* __restrict__ out,
                    unsigned char* ws) {
  __shared__ __attribute__((aligned(16))) float red[16 * 192];
  __shared__ __attribute__((aligned(16))) float red3[4 * 256];
  __shared__ __attribute__((aligned(16))) unsigned qs[128];
  __shared__ __attribute__((aligned(16))) float mls[16];
  __shared__ unsigned flg[4];

  STAMP(0);
  const int tid = threadIdx.x;
  const int lane = tid & 63;
  const int w = __builtin_amdgcn_readfirstlane(tid >> 6);
  const int lg = lane >> 3, lc = lane & 7;
  const int col = lane & 15, rq = lane >> 4;
  const int b = blockIdx.x;
  const int h = 4 * (b & 7) + (b >> 6);
  const int s = (b >> 3) & 7;
  const int wg = h * 8 + s;

  const rsrc_t rsX = mk(x, 1280);
  const rsrc_t rsWs = mk(ws, WS_BYTES);
  const size_t kvoff = ((size_t)h * 32768 + 28672 + 512 * s + 64 * w) * 192;
  const rsrc_t rsK = mk(kc + kvoff, 64 * 384);
  const rsrc_t rsV = mk(vc + kvoff, 64 * 384);

  const unsigned epoch = __builtin_amdgcn_raw_buffer_load_b32(rsWs, 0, 0, AUX_SC1);
  if (tid < 4) flg[tid] = 0u;

  unsigned mask[4];
  {
    const unsigned mm = (lc & 1) ? 0xFFFF0000u : 0x0000FFFFu;
#pragma unroll
    for (int i = 0; i < 4; ++i) mask[i] = ((lc >> 1) == i) ? mm : 0u;
  }
  const bool dvalid = (rq >> 1) == (col >> 3);
  const unsigned vkv = (unsigned)(lg * 384 + lc * 16);
  const int t4 = tid - 256;
  const unsigned p1_off = (t4 >= 0 && t4 < 97) ? WS_P1 + (unsigned)(wg * 256 + 2 * t4) * 8u : OOB;
  const int r3 = 4 * (t4 >> 6) + (t4 & 3), c3 = (t4 >> 2) & 15;
  const unsigned p2_off =
      (t4 >= 0 && r3 < 10) ? WS_P2 + (unsigned)((s * 32 + h) * 160 + (8 * r3 + (c3 & 7)) * 2 + (c3 >> 3)) * 8u : OOB;
  v4u kv[8][3], vv[8][3];

  if (w < 2) {
    __builtin_amdgcn_s_setprio(3);
    const v4u xs0 = __builtin_amdgcn_raw_buffer_load_b128(rsX, 20 * lane, 0, 0);
    const unsigned xs1 = __builtin_amdgcn_raw_buffer_load_b32(rsX, 20 * lane + 16, 0, 0);
    const v4u xr0 = __builtin_amdgcn_raw_buffer_load_b128(rsX, 160 * s + 20 * lg, 0, 0);
    const unsigned xr1 = __builtin_amdgcn_raw_buffer_load_b32(rsX, 160 * s + 20 * lg + 16, 0, 0);
    const unsigned short* pa = wq + (size_t)(80 * s) * 6144 + 192 * h + 128 * w;
    const unsigned short* pb = w ? w1 + (size_t)(80 * s) * 1536 + 48 * h : pa + 64;
    const int sb = w ? 3072 : 12288;
    const rsrc_t rsA = mk(pa, 79 * 12288 + 128);
    const rsrc_t rsB = mk(pb, w ? 79 * 3072 + 96 : 79 * 12288 + 128);
    const unsigned voa = (unsigned)(lg * 122880 + lc * 16);
    const unsigned vob = (w && lc >= 6) ? OOB : (unsigned)(lg * 10 * sb + lc * 16);
    v4u wa[10], wb[10];
#pragma unroll
    for (int g = 0; g < 10; ++g) {
      wa[g] = __builtin_amdgcn_raw_buffer_load_b128(rsA, voa, g * 12288, AUX_NT);
      wb[g] = __builtin_amdgcn_raw_buffer_load_b128(rsB, vob, g * sb, AUX_NT);
      SCHED_FENCE();
    }
    const unsigned p0_off = dvalid ? WS_P0 + (unsigned)(wg * 256 + 128 * w + 64 * (rq >> 1) + 8 * (col & 7) + 4 * (rq & 1)) * 8u : OOB;
    SCHED_FENCE();
    __syncthreads();
    SCHED_FENCE();
    float mb, sd;
    {
      float e[10];
      e[0] = bflo(xs0.x); e[1] = bfhi(xs0.x); e[2] = bflo(xs0.y); e[3] = bfhi(xs0.y);
      e[4] = bflo(xs0.z); e[5] = bfhi(xs0.z); e[6] = bflo(xs0.w); e[7] = bfhi(xs0.w);
      e[8] = bflo(xs1);   e[9] = bfhi(xs1);
      float t = 0.f;
#pragma unroll
      for (int i = 0; i < 10; ++i) t += e[i];
      t = sum_hi(sum8(t));
      const float mean = t * (1.0f / 640.0f);
      float q2 = 0.f;
#pragma unroll
      for (int i = 0; i < 10; ++i) { const float d = e[i] - mean; q2 += d * d; }
      q2 = sum_hi(sum8(q2));
      const float var = rbf(q2 * (1.0f / 640.0f));
      mb = rbf(mean);
      sd = rbf(sqrtf(rbf(var + 1e-5f)));
    }
    const unsigned xr[5] = {xr0.x, xr0.y, xr0.z, xr0.w, xr1};
    const f32x4 zero = {0.f, 0.f, 0.f, 0.f};
    f32x4 a0 = zero, a1 = zero, b0 = zero, b1 = zero;
#pragma unroll
    for (int g = 0; g < 5; ++g) {
      const float n0 = rbf(bflo(xr[g]) - mb) / sd, n1 = rbf(bfhi(xr[g]) - mb) / sd;
      const v4u f0 = bfrag(pk2(n0, n0), mask), f1 = bfrag(pk2(n1, n1), mask);
      a0 = mfma(f0, wa[2 * g], a0);
      b0 = mfma(f0, wb[2 * g], b0);
      a1 = mfma(f1, wa[2 * g + 1], a1);
      b1 = mfma(f1, wb[2 * g + 1], b1);
    }
    a0 += a1;
    b0 += b1;
    const unsigned tag = (epoch + 1u) | 0x80000000u;
    {
      const bool p = rq >= 2;
      float r[4];
#pragma unroll
      for (int i = 0; i < 4; ++i) r[i] = (p ? b0[i] : a0[i]) + __shfl_xor(p ? a0[i] : b0[i], 40);
      const v4u g0 = {asu(r[0]), tag, asu(r[1]), tag}, g1 = {asu(r[2]), tag, asu(r[3]), tag};
      __builtin_amdgcn_raw_buffer_store_b128(g0, rsWs, p0_off, 0, AUX_SC1);
      __builtin_amdgcn_raw_buffer_store_b128(g1, rsWs, p0_off + 16, 0, AUX_SC1);
    }
    STAMP(1);
    __builtin_amdgcn_s_setprio(0);
    SCHED_FENCE();
#pragma unroll
    for (int g = 0; g < 2; ++g)
#pragma unroll
      for (int j = 0; j < 3; ++j) kv[g][j] = __builtin_amdgcn_raw_buffer_load_b128(rsK, vkv + j * 128, g * 3072, AUX_NT);
  } else {
#pragma unroll
    for (int g = 0; g < 2; ++g)
#pragma unroll
      for (int j = 0; j < 3; ++j) kv[g][j] = __builtin_amdgcn_raw_buffer_load_b128(rsK, vkv + j * 128, g * 3072, AUX_NT);
    SCHED_FENCE();
    __syncthreads();
  }
  SCHED_FENCE();
#pragma unroll
  for (int g = 2; g < 8; ++g) {
#pragma unroll
    for (int j = 0; j < 3; ++j) kv[g][j] = __builtin_amdgcn_raw_buffer_load_b128(rsK, vkv + j * 128, g * 3072, AUX_NT);
    SCHED_FENCE();
  }
#pragma unroll
  for (int g = 0; g < 8; ++g) {
#pragma unroll
    for (int j = 0; j < 3; ++j) vv[g][j] = __builtin_amdgcn_raw_buffer_load_b128(rsV, vkv + j * 128, g * 3072, AUX_NT);
    SCHED_FENCE();
  }
  STAMP(9);
  if (lane == 0) __hip_atomic_fetch_add(&flg[2], 1u, __ATOMIC_RELAXED, __HIP_MEMORY_SCOPE_WORKGROUP);
  if (w == 2 || w == 3) {
    const int t2 = tid - 128;
    const unsigned tag = (epoch + 1u) | 0x80000000u;
    const unsigned gbase = WS_P0 + (unsigned)(h * 8 * 256 + 2 * t2) * 8u;
    v4u g[8];
#pragma unroll 1
    for (unsigned spins = 0; spins < SPIN_CAP; ++spins) {
      asm volatile("" ::: "memory");
#pragma unroll
      for (int i = 0; i < 8; ++i) g[i] = __builtin_amdgcn_raw_buffer_load_b128(rsWs, gbase, i * 2048, AUX_SC1);
      bool ok = true;
#pragma unroll
      for (int i = 0; i < 8; ++i) ok &= (g[i].y == tag) & (g[i].w == tag);
      if (__all(ok)) break;
      __builtin_amdgcn_s_sleep(1);
    }
    float t0 = 0.f, t1 = 0.f;
#pragma unroll
    for (int i = 0; i < 8; ++i) {
      t0 += asf(g[i].x);
      t1 += asf(g[i].z);
    }
    if (t2 >= 96) { t0 = fmaxf(t0, 0.f); t1 = fmaxf(t1, 0.f); }
    qs[t2] = pk2(t0, t1);
    __builtin_amdgcn_fence(__ATOMIC_RELEASE, "workgroup");
    if (lane == 0) __hip_atomic_store(&flg[w - 2], 1u, __ATOMIC_RELAXED, __HIP_MEMORY_SCOPE_WORKGROUP);
    STAMP(2);
  }
#pragma unroll 1
  for (unsigned spins = 0; spins < (SPIN_CAP << 6); ++spins) {
    const unsigned f0 = __hip_atomic_load(&flg[0], __ATOMIC_RELAXED, __HIP_MEMORY_SCOPE_WORKGROUP);
    const unsigned f1 = __hip_atomic_load(&flg[1], __ATOMIC_RELAXED, __HIP_MEMORY_SCOPE_WORKGROUP);
    if (f0 & f1) break;
    __builtin_amdgcn_s_sleep(1);
  }
  __builtin_amdgcn_fence(__ATOMIC_ACQUIRE, "workgroup");
  v4u qf[3];
#pragma unroll
  for (int j = 0; j < 3; ++j) qf[j] = *(const v4u*)(qs + 32 * j + 4 * lc);

  float sc[8];
#pragma unroll
  for (int g = 0; g < 8; ++g) {
    float a = 0.f;
#pragma unroll
    for (int j = 0; j < 3; ++j) {
      a = dot2(kv[g][j].x, qf[j].x, a);
      a = dot2(kv[g][j].y, qf[j].y, a);
      a = dot2(kv[g][j].z, qf[j].z, a);
      a = dot2(kv[g][j].w, qf[j].w, a);
    }
    a = sum8(a);
    sc[g] = rbf(rbf(a) * 0.07216878364870322f);
  }
  SCHED_FENCE();
  STAMP(3);
  unsigned pd[8];
  {
    float m = fmaxf(fmaxf(fmaxf(sc[0], sc[1]), fmaxf(sc[2], sc[3])), fmaxf(fmaxf(sc[4], sc[5]), fmaxf(sc[6], sc[7])));
    m = max_hi(m);
    float l = 0.f;
#pragma unroll
    for (int g = 0; g < 8; ++g) {
      const float p = __builtin_amdgcn_exp2f((sc[g] - m) * 1.4426950408889634f);
      l += p;
      pd[g] = pk2(p, p);
    }
    l = sum_hi(l);
    if (lane == 0) { mls[w] = m; mls[8 + w] = l; }
  }
  SCHED_FENCE();

  {
    f32x4 oacc[3] = {{0.f, 0.f, 0.f, 0.f}, {0.f, 0.f, 0.f, 0.f}, {0.f, 0.f, 0.f, 0.f}};
#pragma unroll
    for (int g = 0; g < 8; ++g) {
      const v4u bf = bfrag(pd[g], mask);
#pragma unroll
      for (int j = 0; j < 3; ++j) oacc[j] = mfma(bf, vv[g][j], oacc[j]);
      SCHED_FENCE();
    }
    float* const myred = red + (w * 2 + (rq >> 1)) * 192 + 8 * (col & 7) + 4 * (rq & 1);
    if (dvalid) {
#pragma unroll
      for (int j = 0; j < 3; ++j) *(f32x4*)(myred + j * 64) = oacc[j];
    }
  }
  STAMP(10);
  const unsigned tag = (epoch + 1u) | 0x80000000u;
  if (w < 4) {
#pragma unroll 1
    for (unsigned spins = 0; spins < (SPIN_CAP << 6); ++spins) {
      if (__hip_atomic_load(&flg[2], __ATOMIC_RELAXED, __HIP_MEMORY_SCOPE_WORKGROUP) >= GVCNT) break;
      __builtin_amdgcn_s_sleep(1);
    }
    const rsrc_t rsWo = mk(wo + (size_t)(192 * h + 48 * w) * 640 + 80 * s, 47 * 1280 + 160);
    const rsrc_t rsW2 = mk(w2 + (size_t)(48 * h + 12 * w) * 640 + 80 * s, 11 * 1280 + 160);
    const unsigned vo3 = col < 10 ? (unsigned)(rq * 15360 + col * 16) : OOB;
    const unsigned vo4 = col < 10 ? (unsigned)(rq * 3840 + col * 16) : OOB;
    v4u gv[15];
#pragma unroll
    for (int g = 0; g < 3; ++g) gv[12 + g] = __builtin_amdgcn_raw_buffer_load_b128(rsW2, vo4, g * 1280, AUX_NT);
#pragma unroll
    for (int g = 0; g < 12; ++g) gv[g] = __builtin_amdgcn_raw_buffer_load_b128(rsWo, vo3, g * 1280, AUX_NT);
    SCHED_FENCE();
    STAMP(15);
    __syncthreads();
    STAMP(4);
    const unsigned gbase = WS_P1 + (unsigned)((h * 8 + (col & 7)) * 256 + 48 * w + 12 * rq + 6 * (col >> 3)) * 8u;
    const unsigned mlo = WS_P1 + (unsigned)((h * 8 + (col & 7)) * 256 + 192) * 8u;
    v4u g[3], gm;
#pragma unroll 1
    for (unsigned spins = 0; spins < SPIN_CAP; ++spins) {
      asm volatile("" ::: "memory");
#pragma unroll
      for (int i = 0; i < 3; ++i) g[i] = __builtin_amdgcn_raw_buffer_load_b128(rsWs, gbase + i * 16, 0, AUX_SC1);
      gm = __builtin_amdgcn_raw_buffer_load_b128(rsWs, mlo, 0, AUX_SC1);
      bool ok = (gm.y == tag) & (gm.w == tag);
#pragma unroll
      for (int i = 0; i < 3; ++i) ok &= (g[i].y == tag) & (g[i].w == tag);
      if (__all(ok)) break;
      __builtin_amdgcn_s_sleep(1);
    }
    STAMP(5);
    unsigned od[6];
    {
      const float M = asf(gm.x);
      float Mg = fmaxf(M, dpp<0xB1>(M));
      Mg = fmaxf(Mg, dpp<0x4E>(Mg));
      Mg = fmaxf(Mg, dpp<0x141>(Mg));
      const float f = __builtin_amdgcn_exp2f((M - Mg) * 1.4426950408889634f);
      const float den = sum8(f * asf(gm.z));
#pragma unroll
      for (int i = 0; i < 3; ++i) {
        const float n0 = sum8(f * asf(g[i].x)) / den, n1 = sum8(f * asf(g[i].z)) / den;
        od[2 * i] = pk2(n0, n0);
        od[2 * i + 1] = pk2(n1, n1);
      }
    }
    unsigned mlo8[4], mhi8[4];
#pragma unroll
    for (int i = 0; i < 4; ++i) { mlo8[i] = col < 8 ? mask[i] : 0u; mhi8[i] = col < 8 ? 0u : mask[i]; }
    const unsigned short* hb = (const unsigned short*)(qs + 96) + 12 * w + 3 * rq;
    f32x4 ya = {0.f, 0.f, 0.f, 0.f}, yb = {0.f, 0.f, 0.f, 0.f}, za = {0.f, 0.f, 0.f, 0.f};
#pragma unroll
    for (int i = 0; i < 3; ++i) za = mfma(gv[12 + i], bfrag((unsigned)hb[i] * 0x10001u, mhi8), za);
#pragma unroll
    for (int i = 0; i < 6; ++i) {
      ya = mfma(gv[i], bfrag(od[i], mlo8), ya);
      yb = mfma(gv[6 + i], bfrag(od[i], mhi8), yb);
    }
    f32x4 r;
#pragma unroll
    for (int i = 0; i < 4; ++i) r[i] = ya[i] + za[i] + dpp<0x128>(yb[i]);
    *(f32x4*)(red3 + w * 256 + lane * 4) = r;
    STAMP(6);
    __syncthreads();
    STAMP(7);
  } else {
    __syncthreads();
    if (w < 6) {
      float t0 = 0.f, t1 = 0.f;
      const f32x4 m0 = *(const f32x4*)mls, m1 = *(const f32x4*)(mls + 4);
      const f32x4 l0 = *(const f32x4*)(mls + 8), l1 = *(const f32x4*)(mls + 12);
      const float mw[8] = {m0.x, m0.y, m0.z, m0.w, m1.x, m1.y, m1.z, m1.w};
      const float lw[8] = {l0.x, l0.y, l0.z, l0.w, l1.x, l1.y, l1.z, l1.w};
      const float M = fmaxf(fmaxf(fmaxf(mw[0], mw[1]), fmaxf(mw[2], mw[3])), fmaxf(fmaxf(mw[4], mw[5]), fmaxf(mw[6], mw[7])));
      float L = 0.f;
      const int sl = t4 < 96 ? 2 * t4 : 0;
#pragma unroll
      for (int i = 0; i < 8; ++i) {
        const float f = __builtin_amdgcn_exp2f((mw[i] - M) * 1.4426950408889634f);
        const f32x2 va = *(const f32x2*)(red + (2 * i) * 192 + sl);
        const f32x2 vb = *(const f32x2*)(red + (2 * i + 1) * 192 + sl);
        t0 += f * (va.x + vb.x);
        t1 += f * (va.y + vb.y);
        L += f * lw[i];
      }
      if (t4 >= 96) { t0 = M; t1 = L; }
      const v4u g = {asu(t0), tag, asu(t1), tag};
      __builtin_amdgcn_raw_buffer_store_b128(g, rsWs, p1_off, 0, AUX_SC1);
      STAMP(12);
      STAMP(13);
    }
    __syncthreads();
    if (w < 7) {
      const float t = (red3[t4] + red3[256 + t4]) + (red3[512 + t4] + red3[768 + t4]);
      const v2u g = {asu(t), tag};
      __builtin_amdgcn_raw_buffer_store_b64(g, rsWs, p2_off, 0, AUX_SC1);
    } else if (h < 20) {
      const int n0 = 4 * h + 2 * (lane & 1);
      const unsigned xin = __builtin_amdgcn_raw_buffer_load_b32(rsX, (unsigned)(80 * s + n0) * 2u, 0, 0);
      const unsigned gbase = WS_P2 + (unsigned)((s * 32 + (lane >> 1)) * 160 + n0 * 2) * 8u;
      v4u g[2];
#pragma unroll 1
      for (unsigned spins = 0; spins < SPIN_CAP; ++spins) {
        asm volatile("" ::: "memory");
#pragma unroll
        for (int i = 0; i < 2; ++i) g[i] = __builtin_amdgcn_raw_buffer_load_b128(rsWs, gbase + i * 16, 0, AUX_SC1);
        const bool ok = (g[0].y == tag) & (g[0].w == tag) & (g[1].y == tag) & (g[1].w == tag);
        if (__all(ok)) break;
        __builtin_amdgcn_s_sleep(1);
      }
      STAMP(8);
      float v[4] = {asf(g[0].x), asf(g[0].z), asf(g[1].x), asf(g[1].z)};
#pragma unroll
      for (int i = 0; i < 4; ++i) {
        float t = v[i];
        t += dpp<0x4E>(t);
        t += __shfl_xor(t, 4);
        t = sum_hi(t);
        v[i] = t;
      }
      if (lane < 2) {
        const float r0 = rbf(rbf(bflo(xin) + rbf(v[0])) + rbf(v[1]));
        const float r1 = rbf(rbf(bfhi(xin) + rbf(v[2])) + rbf(v[3]));
        *(unsigned*)(out + 80 * s + n0) = pk2(r0, r1);
      }
      if (wg == 0 && lane == 0) __builtin_amdgcn_raw_buffer_store_b32(epoch + 1u, rsWs, 0, 0, AUX_SC1);
    }
  }

}

}

extern "C" void kernel_launch(void* const* d_in, const int* in_sizes, int n_in, void* d_out, int out_size, void* d_ws,
                              size_t ws_size, hipStream_t stream) {
  (void)in_sizes; (void)n_in; (void)out_size;
  if (ws_size < WS_BYTES) return;
  fused_decode_kernel<<<dim3(256), dim3(512), 0, stream>>>(
      (const unsigned short*)d_in[0], (const unsigned short*)d_in[1], (const unsigned short*)d_in[2],
      (const unsigned short*)d_in[3], (const unsigned short*)d_in[4], (const unsigned short*)d_in[6],
      (const unsigned short*)d_in[7], (unsigned short*)d_out, (unsigned char*)d_ws);
  kc_tiny<<<dim3(1), dim3(64), 0, stream>>>((unsigned char*)d_ws);
}
```

```cpp
#include <hip/hip_runtime.h>
#include <hip/hip_bf16.h>

namespace {

typedef unsigned v4u __attribute__((ext_vector_type(4)));
typedef unsigned v2u __attribute__((ext_vector_type(2)));
typedef float f32x4 __attribute__((ext_vector_type(4)));
typedef float f32x2 __attribute__((ext_vector_type(2)));
typedef __bf16 bfx8 __attribute__((ext_vector_type(8)));
typedef __bf16 bfx2 __attribute__((ext_vector_type(2)));
typedef __amdgpu_buffer_rsrc_t rsrc_t;

constexpr int AUX_NT = 2;
constexpr int AUX_SC1 = 16;
constexpr int AUX_POLL = (int)(16u | 0x80000000u);
constexpr unsigned OOB = 0x80000000u;
constexpr unsigned SPIN_CAP = 1u << 16;

constexpr unsigned WS_P0 = 4096;
constexpr unsigned WS_H = WS_P0 + 3072u * 8u;
constexpr unsigned P1S = 1664;
constexpr unsigned WS_P1 = WS_P0 + 122880u;
constexpr unsigned WS_ML = WS_P1 + 256u * P1S;
constexpr unsigned WS_Y = WS_ML + 4096u;
constexpr unsigned WS_Z = WS_Y + 8u * 32u * 640u;
constexpr unsigned WS_BYTES = WS_Z + 8u * 32u * 640u;
constexpr int RS = 392;

#define NQW 1
#define QNB 6
#define QB 40
#define QPRIO 0
#define QAUX AUX_NT
#define PG2 4
#define WE 3
#define GVCNT 8
#define WOSL 0
#define FEW 2
#define DIRECT0 1
#define NY 4
#define YFEW 64
#define YSL 8
#define NSET 1
#define PSL 0
#define STAG1 8
#define STAG2 6

__device__ __forceinline__ rsrc_t mk(const void* p, unsigned bytes) {
  return __builtin_amdgcn_make_buffer_rsrc((void*)p, (short)0, (int)bytes, 0x00020000);
}
__device__ __forceinline__ float asf(unsigned u) { return __builtin_bit_cast(float, u); }
__device__ __forceinline__ unsigned asu(float f) { return __builtin_bit_cast(unsigned, f); }
__device__ __forceinline__ float rbf(float f) { return (float)(__bf16)f; }
__device__ __forceinline__ float bflo(unsigned u) { return __builtin_bit_cast(float, u << 16); }
__device__ __forceinline__ float bfhi(unsigned u) { return __builtin_bit_cast(float, u & 0xFFFF0000u); }
__device__ __forceinline__ unsigned pk2(float a, float b) {
  f32x2 v = {a, b};
  return __builtin_bit_cast(unsigned, __builtin_convertvector(v, bfx2));
}
__device__ __forceinline__ f32x4 mfma(v4u a, v4u b, f32x4 c) {
  return __builtin_amdgcn_mfma_f32_16x16x32_bf16(__builtin_bit_cast(bfx8, a), __builtin_bit_cast(bfx8, b), c, 0, 0, 0);
}
typedef short s16x4 __attribute__((ext_vector_type(4)));
__device__ __forceinline__ f32x4 mfma4(v2u a, v2u b, f32x4 c) {
  return __builtin_amdgcn_mfma_f32_4x4x4bf16_1k(__builtin_bit_cast(s16x4, a), __builtin_bit_cast(s16x4, b), c, 0, 0, 0);
}
__device__ __forceinline__ float dot2(unsigned a, unsigned b, float c) {
  return __builtin_amdgcn_fdot2_f32_bf16(__builtin_bit_cast(bfx2, a), __builtin_bit_cast(bfx2, b), c, false);
}
template <int CTRL> __device__ __forceinline__ float dpp(float v) {
  return __builtin_bit_cast(float, __builtin_amdgcn_mov_dpp(__builtin_bit_cast(int, v), CTRL, 0xF, 0xF, true));
}
__device__ __forceinline__ float sum8(float v) {
  v += dpp<0xB1>(v);
  v += dpp<0x4E>(v);
  v += dpp<0x141>(v);
  return v;
}
__device__ __forceinline__ float max8(float v) {
  v = fmaxf(v, dpp<0xB1>(v));
  v = fmaxf(v, dpp<0x4E>(v));
  v = fmaxf(v, dpp<0x141>(v));
  return v;
}
__device__ __forceinline__ float sum_hi(float v) {
  v += dpp<0x128>(v);
  v += __shfl_xor(v, 16);
  v += __shfl_xor(v, 32);
  return v;
}
__device__ __forceinline__ float max_hi(float v) {
  v = fmaxf(v, dpp<0x128>(v));
  v = fmaxf(v, __shfl_xor(v, 16));
  v = fmaxf(v, __shfl_xor(v, 32));
  return v;
}
__device__ __forceinline__ v4u bfrag(unsigned dup, const unsigned (&mask)[4]) {
  v4u b;
  b.x = dup & mask[0];
  b.y = dup & mask[1];
  b.z = dup & mask[2];
  b.w = dup & mask[3];
  return b;
}
__device__ __forceinline__ float fold32(float a, float b) {
  const auto r = __builtin_amdgcn_permlane32_swap(asu(a), asu(b), false, false);
  return asf(r[0]) + asf(r[1]);
}
__device__ __forceinline__ float fold16(float a, float b) {
  const auto r = __builtin_amdgcn_permlane16_swap(asu(a), asu(b), false, false);
  return asf(r[0]) + asf(r[1]);
}
__device__ __forceinline__ float sum32(float v) {
  v = sum8(v);
  v += dpp<0x128>(v);
  const auto r = __builtin_amdgcn_permlane16_swap(asu(v), asu(v), false, false);
  return asf(r[0]) + asf(r[1]);
}
#define SCHED_FENCE() __builtin_amdgcn_sched_barrier(0)
#define CBAR() asm volatile("" ::: "memory")
#define STR_(x) #x
#define STR(x) STR_(x)
#define PSL_TXT ""
__device__ __forceinline__ v4u srd(const void* p, unsigned bytes) {
  const unsigned long long a = (unsigned long long)p;
  const v4u r = {(unsigned)a, (unsigned)(a >> 32) & 0xFFFFu, bytes, 0x00020000u};
  return r;
}
#define LWO0 if (WE > 0) asm volatile("s_nop 4\n buffer_load_dwordx4 v[160:163], %1, %2, %3 offen offset:0 nt" : "={v[160:163]}"(gv[0]) : "v"(vo3), "s"(srdWo), "s"(0) : "memory")
#define RWO0 if (WE > 0) asm volatile("" : "+{v[160:163]}"(gv[0]) :: "memory")
#define LWO1 if (WE > 1) asm volatile("s_nop 4\n buffer_load_dwordx4 v[164:167], %1, %2, %3 offen offset:1280 nt" : "={v[164:167]}"(gv[1]) : "v"(vo3), "s"(srdWo), "s"(0) : "memory")
#define RWO1 if (WE > 1) asm volatile("" : "+{v[164:167]}"(gv[1]) :: "memory")
#define LWO2 if (WE > 2) asm volatile("s_nop 4\n buffer_load_dwordx4 v[168:171], %1, %2, %3 offen offset:2560 nt" : "={v[168:171]}"(gv[2]) : "v"(vo3), "s"(srdWo), "s"(0) : "memory")
#define RWO2 if (WE > 2) asm volatile("" : "+{v[168:171]}"(gv[2]) :: "memory")
#define LWO3 if (WE > 3) asm volatile("s_nop 4\n buffer_load_dwordx4 v[172:175], %1, %2, %3 offen offset:3840 nt" : "={v[172:175]}"(gv[3]) : "v"(vo3), "s"(srdWo), "s"(0) : "memory")
#define RWO3 if (WE > 3) asm volatile("" : "+{v[172:175]}"(gv[3]) :: "memory")
#define LWO4 if (WE > 4) asm volatile("s_nop 4\n buffer_load_dwordx4 v[176:179], %1, %2, %3 offen offset:0 nt" : "={v[176:179]}"(gv[4]) : "v"(vo3), "s"(srdWo), "s"(5120) : "memory")
#define RWO4 if (WE > 4) asm volatile("" : "+{v[176:179]}"(gv[4]) :: "memory")
#define LWO5 if (WE > 5) asm volatile("s_nop 4\n buffer_load_dwordx4 v[180:183], %1, %2, %3 offen offset:1280 nt" : "={v[180:183]}"(gv[5]) : "v"(vo3), "s"(srdWo), "s"(5120) : "memory")
#define RWO5 if (WE > 5) asm volatile("" : "+{v[180:183]}"(gv[5]) :: "memory")
#define LWO6 if (WE > 6) asm volatile("s_nop 4\n buffer_load_dwordx4 v[184:187], %1, %2, %3 offen offset:2560 nt" : "={v[184:187]}"(gv[6]) : "v"(vo3), "s"(srdWo), "s"(5120) : "memory")
#define RWO6 if (WE > 6) asm volatile("" : "+{v[184:187]}"(gv[6]) :: "memory")
#define LWO7 if (WE > 7) asm volatile("s_nop 4\n buffer_load_dwordx4 v[188:191], %1, %2, %3 offen offset:3840 nt" : "={v[188:191]}"(gv[7]) : "v"(vo3), "s"(srdWo), "s"(5120) : "memory")
#define RWO7 if (WE > 7) asm volatile("" : "+{v[188:191]}"(gv[7]) :: "memory")
#define LWO8 if (WE > 8) asm volatile("s_nop 4\n buffer_load_dwordx4 v[192:195], %1, %2, %3 offen offset:0 nt" : "={v[192:195]}"(gv[8]) : "v"(vo3), "s"(srdWo), "s"(10240) : "memory")
#define RWO8 if (WE > 8) asm volatile("" : "+{v[192:195]}"(gv[8]) :: "memory")
#define LWO9 if (WE > 9) asm volatile("s_nop 4\n buffer_load_dwordx4 v[196:199], %1, %2, %3 offen offset:1280 nt" : "={v[196:199]}"(gv[9]) : "v"(vo3), "s"(srdWo), "s"(10240) : "memory")
#define RWO9 if (WE > 9) asm volatile("" : "+{v[196:199]}"(gv[9]) :: "memory")
#define LWO10 if (WE > 10) asm volatile("s_nop 4\n buffer_load_dwordx4 v[200:203], %1, %2, %3 offen offset:2560 nt" : "={v[200:203]}"(gv[10]) : "v"(vo3), "s"(srdWo), "s"(10240) : "memory")
#define RWO10 if (WE > 10) asm volatile("" : "+{v[200:203]}"(gv[10]) :: "memory")
#define LWO11 if (WE > 11) asm volatile("s_nop 4\n buffer_load_dwordx4 v[204:207], %1, %2, %3 offen offset:3840 nt" : "={v[204:207]}"(gv[11]) : "v"(vo3), "s"(srdWo), "s"(10240) : "memory")
#define RWO11 if (WE > 11) asm volatile("" : "+{v[204:207]}"(gv[11]) :: "memory")
#define LOAD_WO() do { LWO0; LWO1; LWO2; LWO3; LWO4; LWO5; LWO6; LWO7; LWO8; LWO9; LWO10; LWO11; } while (0)
#define READY_WO() do { RWO0; RWO1; RWO2; RWO3; RWO4; RWO5; RWO6; RWO7; RWO8; RWO9; RWO10; RWO11; } while (0)
#define P1_ISSUE0(G) asm volatile("s_nop 4\n" "buffer_load_dwordx4 v[220:223], %5, %6, 0 offen sc1\n" "buffer_load_dwordx4 v[208:211], %4, %6, 0 offen offset:0 sc1\n" "buffer_load_dwordx4 v[212:215], %4, %6, 0 offen offset:128 sc1\n" "buffer_load_dwordx4 v[216:219], %4, %6, 0 offen offset:256 sc1\n"  \
  : "={v[208:211]}"(G[0]), "={v[212:215]}"(G[1]), "={v[216:219]}"(G[2]), "={v[220:223]}"(G[3]) : "v"(g0), "v"(mo), "s"(srdWs) : "memory")
#define P1_ISSUE1(G) asm volatile("s_nop 4\n" "buffer_load_dwordx4 v[236:239], %5, %6, 0 offen sc1\n" "buffer_load_dwordx4 v[224:227], %4, %6, 0 offen offset:0 sc1\n" "buffer_load_dwordx4 v[228:231], %4, %6, 0 offen offset:128 sc1\n" "buffer_load_dwordx4 v[232:235], %4, %6, 0 offen offset:256 sc1\n"  \
  : "={v[224:227]}"(G[4]), "={v[228:231]}"(G[5]), "={v[232:235]}"(G[6]), "={v[236:239]}"(G[7]) : "v"(g0), "v"(mo), "s"(srdWs) : "memory")
#define P1_ISSUE2(G) asm volatile("s_nop 4\n" "buffer_load_dwordx4 v[252:255], %5, %6, 0 offen sc1\n" "buffer_load_dwordx4 v[240:243], %4, %6, 0 offen offset:0 sc1\n" "buffer_load_dwordx4 v[244:247], %4, %6, 0 offen offset:128 sc1\n" "buffer_load_dwordx4 v[248:251], %4, %6, 0 offen offset:256 sc1\n"  \
  : "={v[240:243]}"(G[8]), "={v[244:247]}"(G[9]), "={v[248:251]}"(G[10]), "={v[252:255]}"(G[11]) : "v"(g0), "v"(mo), "s"(srdWs) : "memory")
#define ZCNT 16
#define P1_LOOP(G) asm volatile( \
      ".Lp1l%=:\n" \
      "s_waitcnt vmcnt(0)\n" \
      "v_bitop3_b32 %[t0], v221, v223, %[tag] bitop3:0x7e\n" \
      "v_bitop3_b32 %[t1], v209, v211, %[tag] bitop3:0x7e\n" \
      "v_bitop3_b32 %[t2], v213, v215, %[tag] bitop3:0x7e\n" \
      "v_bitop3_b32 %[t3], v217, v219, %[tag] bitop3:0x7e\n" \
      "v_cmp_eq_u32 %[sa], 0, %[t0]\n" \
      "v_or3_b32 %[t1], %[t1], %[t2], %[t3]\n" \
      "v_cmp_eq_u32 vcc, 0, %[t1]\n" \
      "s_nop 0\n" \
      "v_cndmask_b32 %[f6], %[f6], v220, %[sa]\n" \
      "v_cndmask_b32 %[f7], %[f7], v222, %[sa]\n" \
      "v_cndmask_b32 %[mo], %[mo], %[oob], %[sa]\n" \
      "v_cndmask_b32 %[f0], %[f0], v208, vcc\n" \
      "v_cndmask_b32 %[f1], %[f1], v210, vcc\n" \
      "v_cndmask_b32 %[f2], %[f2], v212, vcc\n" \
      "v_cndmask_b32 %[f3], %[f3], v214, vcc\n" \
      "v_cndmask_b32 %[f4], %[f4], v216, vcc\n" \
      "v_cndmask_b32 %[f5], %[f5], v218, vcc\n" \
      "s_andn2_b64 %[pm], %[pm], %[sa]\n" \
      "s_andn2_b64 %[pd], %[pd], vcc\n" \
      "s_or_b64 %[sa], %[pm], %[pd]\n" \
      "s_cbranch_scc0 .Lp1x%=\n" \
      "s_bcnt1_i32_b64 %[sc], %[pd]\n" \
      "s_cmp_le_u32 %[sc], " STR(FEW) " * 8\n" \
      "s_cselect_b64 %[sa], 0, %[pm]\n" \
      "s_andn2_b64 %[sa], %[pd], %[sa]\n" \
      "v_cndmask_b32 %[t0], %[oob], %[go], %[sa]\n" \
      PSL_TXT \
      "buffer_load_dwordx4 v[220:223], %[mo], %[rs], 0 offen sc1\n" "buffer_load_dwordx4 v[208:211], %[t0], %[rs], 0 offen offset:0 sc1\n" "buffer_load_dwordx4 v[212:215], %[t0], %[rs], 0 offen offset:128 sc1\n" "buffer_load_dwordx4 v[216:219], %[t0], %[rs], 0 offen offset:256 sc1\n"  \
      "s_sub_u32 %[cnt], %[cnt], 1\n" "s_cbranch_scc0 .Lp1l%=\n" ".Lp1x%=:\n" "s_nop 1\n" \
      : "+{v[208:211]}"(G[0]), "+{v[212:215]}"(G[1]), "+{v[216:219]}"(G[2]), "+{v[220:223]}"(G[3]), \
        [f0] "+v"(fv[0]), [f1] "+v"(fv[1]), [f2] "+v"(fv[2]), [f3] "+v"(fv[3]), [f4] "+v"(fv[4]), [f5] "+v"(fv[5]), [f6] "+v"(fv[6]), [f7] "+v"(fv[7]), \
        [mo] "+v"(mo), [pm] "+s"(pm), [pd] "+s"(pd), [cnt] "+s"(cnt), [sa] "=&s"(sa), [sc] "=&s"(sc), \
        [t0] "=&v"(t0), [t1] "=&v"(t1), [t2] "=&v"(t2), [t3] "=&v"(t3) \
      : [go] "v"(go), [tag] "v"(tag), [oob] "v"(OOB), [rs] "s"(srdWs) : "vcc", "scc", "memory")
#define P1_DRAIN(G) asm volatile("s_waitcnt vmcnt(0)" :: "{v[208:211]}"(G[0]), "{v[212:215]}"(G[1]), "{v[216:219]}"(G[2]), "{v[220:223]}"(G[3]) : "memory")
#define P2_LOOP(G) asm volatile( \
      "s_nop 4\n" "buffer_load_dwordx4 v[224:227], %[go], %[rs], %[sz] offen sc1\n" \
      "buffer_load_dwordx4 v[208:211], %[go], %[rs], %[so] offen sc1\n" "s_sleep " STR(STAG2) "\n" \
      "buffer_load_dwordx4 v[212:215], %[go], %[rs], %[so] offen sc1\n" "s_sleep " STR(STAG2) "\n" \
      "buffer_load_dwordx4 v[216:219], %[go], %[rs], %[so] offen sc1\n" "s_sleep " STR(STAG2) "\n" \
      "buffer_load_dwordx4 v[220:223], %[go], %[rs], %[so] offen sc1\n" \
      ".Lp2l%=:\n" \
      "s_waitcnt vmcnt(3)\n" \
      "v_bitop3_b32 %[t0], v209, v211, %[tag] bitop3:0x7e\n" \
      "v_cmp_eq_u32 vcc, 0, %[t0]\n" \
      "s_nop 1\n" \
      "v_cndmask_b32 %[go], %[go], %[oob], vcc\n" \
      "v_cndmask_b32 %[y0], %[y0], v208, vcc\n" \
      "v_cndmask_b32 %[y1], %[y1], v210, vcc\n" \
      "s_andn2_b64 %[pend], %[pend], vcc\n" \
      "s_cbranch_scc0 .Lp2x%=\n" \
      "s_bcnt1_i32_b64 %[sc], %[pend]\n" \
      "s_cmp_le_u32 %[sc], " STR(YFEW) "\n" \
      "s_cbranch_scc1 .Lp2s0%=\n" \
      "s_sleep " STR(YSL) "\n" \
      ".Lp2s0%=:\n" \
      "buffer_load_dwordx4 v[208:211], %[go], %[rs], %[so] offen sc1\n" \
      "s_waitcnt vmcnt(3)\n" \
      "v_bitop3_b32 %[t0], v213, v215, %[tag] bitop3:0x7e\n" \
      "v_cmp_eq_u32 vcc, 0, %[t0]\n" \
      "s_nop 1\n" \
      "v_cndmask_b32 %[go], %[go], %[oob], vcc\n" \
      "v_cndmask_b32 %[y0], %[y0], v212, vcc\n" \
      "v_cndmask_b32 %[y1], %[y1], v214, vcc\n" \
      "s_andn2_b64 %[pend], %[pend], vcc\n" \
      "s_cbranch_scc0 .Lp2x%=\n" \
      "s_bcnt1_i32_b64 %[sc], %[pend]\n" \
      "s_cmp_le_u32 %[sc], " STR(YFEW) "\n" \
      "s_cbranch_scc1 .Lp2s1%=\n" \
      "s_sleep " STR(YSL) "\n" \
      ".Lp2s1%=:\n" \
      "buffer_load_dwordx4 v[212:215], %[go], %[rs], %[so] offen sc1\n" \
      "s_waitcnt vmcnt(3)\n" \
      "v_bitop3_b32 %[t0], v217, v219, %[tag] bitop3:0x7e\n" \
      "v_cmp_eq_u32 vcc, 0, %[t0]\n" \
      "s_nop 1\n" \
      "v_cndmask_b32 %[go], %[go], %[oob], vcc\n" \
      "v_cndmask_b32 %[y0], %[y0], v216, vcc\n" \
      "v_cndmask_b32 %[y1], %[y1], v218, vcc\n" \
      "s_andn2_b64 %[pend], %[pend], vcc\n" \
      "s_cbranch_scc0 .Lp2x%=\n" \
      "s_bcnt1_i32_b64 %[sc], %[pend]\n" \
      "s_cmp_le_u32 %[sc], " STR(YFEW) "\n" \
      "s_cbranch_scc1 .Lp2s2%=\n" \
      "s_sleep " STR(YSL) "\n" \
      ".Lp2s2%=:\n" \
      "buffer_load_dwordx4 v[216:219], %[go], %[rs], %[so] offen sc1\n" \
      "s_waitcnt vmcnt(3)\n" \
      "v_bitop3_b32 %[t0], v221, v223, %[tag] bitop3:0x7e\n" \
      "v_cmp_eq_u32 vcc, 0, %[t0]\n" \
      "s_nop 1\n" \
      "v_cndmask_b32 %[go], %[go], %[oob], vcc\n" \
      "v_cndmask_b32 %[y0], %[y0], v220, vcc\n" \
      "v_cndmask_b32 %[y1], %[y1], v222, vcc\n" \
      "s_andn2_b64 %[pend], %[pend], vcc\n" \
      "s_cbranch_scc0 .Lp2x%=\n" \
      "s_bcnt1_i32_b64 %[sc], %[pend]\n" \
      "s_cmp_le_u32 %[sc], " STR(YFEW) "\n" \
      "s_cbranch_scc1 .Lp2s3%=\n" \
      "s_sleep " STR(YSL) "\n" \
      ".Lp2s3%=:\n" \
      "buffer_load_dwordx4 v[220:223], %[go], %[rs], %[so] offen sc1\n" \
      "s_sub_u32 %[cnt], %[cnt], 1\n" "s_cbranch_scc0 .Lp2l%=\n" ".Lp2x%=:\n" "s_nop 1\n" \
      : "=&{v[208:211]}"(G[0]), "=&{v[212:215]}"(G[1]), "=&{v[216:219]}"(G[2]), "=&{v[220:223]}"(G[3]), "=&{v[224:227]}"(G[4]), \
        [y0] "+v"(y0), [y1] "+v"(y1), [go] "+v"(go), [pend] "+s"(pend), [cnt] "+s"(cnt), [sc] "=&s"(sc), [t0] "=&v"(t0) \
      : [tag] "v"(tag), [oob] "v"(OOB), [rs] "s"(srdWs), [so] "s"(WS_Y), [sz] "s"(WS_Z) : "vcc", "scc", "memory")
#define P2_DRAIN(G) asm volatile("s_waitcnt vmcnt(0)" :: "{v[208:211]}"(G[0]), "{v[212:215]}"(G[1]), "{v[216:219]}"(G[2]), "{v[220:223]}"(G[3]) : "memory")
#define STAMP(k)

#define Y_FINISH() do {                                                                                         \
    ya += yb;                                                                                                   \
    f32x2 r;                                                                                                    \
    r.x = fold32(ya[0], ya[2]);                                                                                 \
    r.y = fold32(ya[1], ya[3]);                                                                                 \
    *(f32x2*)(red3 + w * 128 + lane * 2) = r;                                                                   \
    STAMP(6);                                                                                                   \
    CBAR();                                                                                                     \
    unsigned old = 0u;                                                                                          \
    if (lane == 0) old = __hip_atomic_fetch_add(&flg[5], 1u, __ATOMIC_RELAXED, __HIP_MEMORY_SCOPE_WORKGROUP);   \
    old = __builtin_amdgcn_readfirstlane(old);                                                                  \
    CBAR();                                                                                                     \
    if (old == (WE > 0 && WE < 12 ? 7u : 3u)) {                                                                 \
      const float* rp = red3 + lane * 2;                                                                        \
      f32x2 t = {0.f, 0.f};                                                                                     \
      if (WE > 0) t += (*(const f32x2*)(rp) + *(const f32x2*)(rp + 128)) + (*(const f32x2*)(rp + 256) + *(const f32x2*)(rp + 384)); \
      if (WE < 12) t += (*(const f32x2*)(rp + 512) + *(const f32x2*)(rp + 640)) + (*(const f32x2*)(rp + 768) + *(const f32x2*)(rp + 896)); \
      const v4u g = {asu(t.x), tag, asu(t.y), tag};                                                             \
      __builtin_amdgcn_raw_buffer_store_b128(g, rsWs, yz_off + WS_Y, 0, AUX_SC1);                               \
      CBAR();                                                                                                   \
      if (lane == 0) __hip_atomic_store(&flg[6], 1u, __ATOMIC_RELAXED, __HIP_MEMORY_SCOPE_WORKGROUP);           \
      CBAR();                                                                                                   \
      STAMP(7);                                                                                                 \
    }                                                                                                           \
  } while (0)

__global__ void __launch_bounds__(512, 2)
fused_decode_kernel(const unsigned short* __restrict__ x, const unsigned short* __restrict__ kc,
                    const unsigned short* __restrict__ vc, const unsigned short* __restrict__ wq,
                    const unsigned short* __restrict__ wo, const unsigned short* __restrict__ w1,
                    const unsigned short* __restrict__ w2, unsigned short* __restrict__ out,
                    unsigned char* ws) {
  __shared__ __attribute__((aligned(16))) float red[8 * RS];
  __shared__ __attribute__((aligned(16))) float red3[8 * 128];
  __shared__ __attribute__((aligned(16))) unsigned qs[128];
  __shared__ __attribute__((aligned(16))) float mls[16];
  __shared__ __attribute__((aligned(16))) unsigned osh[4 * 64];
  __shared__ unsigned flg[8];

  STAMP(0);
  const int tid = threadIdx.x;
  const int lane = tid & 63;
  const int w = __builtin_amdgcn_readfirstlane(tid >> 6);
  const int lg = lane >> 3, lc = lane & 7;
  const int col = lane & 15, rq = lane >> 4;
  const int b = blockIdx.x;
  const int h = 4 * (b & 7) + (b >> 6);
  const int s = (b >> 3) & 7;
  const int wg = h * 8 + s;

  const rsrc_t rsX = mk(x, 1280);
  const rsrc_t rsWs = mk(ws, WS_BYTES);
  const v4u srdWs = srd(ws, WS_BYTES);
  const size_t kvoff = ((size_t)h * 32768 + 28672 + 512 * s + 64 * w) * 192;
  const rsrc_t rsK = mk(kc + kvoff, 64 * 384);
  const rsrc_t rsV = mk(vc + kvoff, 64 * 384);

  const unsigned epoch = __builtin_amdgcn_raw_buffer_load_b32(rsWs, 0, 0, AUX_SC1);
  if (tid < 8) flg[tid] = 0u;
  if (tid >= 256) osh[tid - 256] = 0u;

  unsigned mask[4];
  {
    const unsigned mm = (lc & 1) ? 0xFFFF0000u : 0x0000FFFFu;
#pragma unroll
    for (int i = 0; i < 4; ++i) mask[i] = ((lc >> 1) == i) ? mm : 0u;
  }
  const bool dvalid = (rq >> 1) == (col >> 3);
  const unsigned vkv = (unsigned)(lg * 384 + lc * 16);
  v4u kv[8][3], vv[8][3];

  const int jl = b >> 3, jp = (b >> 1) & 3;
  if (w < 2 && jl < 30) {
    __builtin_amdgcn_s_setprio(3);
    const int r = b & 1;
    const int n = r ? 10 - QNB : QNB;
    const int base = r ? 64 * QNB : 0;
    const v4u xs0 = __builtin_amdgcn_raw_buffer_load_b128(rsX, 20 * lane, 0, 0);
    const unsigned xs1 = __builtin_amdgcn_raw_buffer_load_b32(rsX, 20 * lane + 16, 0, 0);
    const unsigned xo = (unsigned)(2 * base + 4 * n * (4 * lg + (lane & 3)));
    const v4u xr0 = __builtin_amdgcn_raw_buffer_load_b128(rsX, xo, 0, 0);
    const v4u xr1 = __builtin_amdgcn_raw_buffer_load_b128(rsX, xo + 16, 0, 0);
    const unsigned short* pa = jl < 24 ? wq + (size_t)base * 6144 + 64 * (24 * jp + jl)
                                       : w1 + (size_t)base * 1536 + 64 * (6 * jp + jl - 24);
    const unsigned st = jl < 24 ? 12288u : 3072u;
    const rsrc_t rsA = mk(pa, (unsigned)(64 * n - 1) * st + 128u);
    const unsigned voa = (unsigned)(8 * n * lg + w) * st + (unsigned)lc * 16u;
    v4u wa[4 * QNB];
#pragma unroll
    for (int k = 0; k < 4 * (10 - QNB); ++k) {
      wa[k] = __builtin_amdgcn_raw_buffer_load_b128(rsA, voa, (n * (k & 3) + (k >> 2)) * 2 * st, QAUX);
      SCHED_FENCE();
    }
    if (!r) {
#pragma unroll
      for (int k = 4 * (10 - QNB); k < 4 * QNB; ++k) {
        wa[k] = __builtin_amdgcn_raw_buffer_load_b128(rsA, voa, (QNB * (k & 3) + (k >> 2)) * 2 * st, QAUX);
        SCHED_FENCE();
      }
    }
    const unsigned p0_off = (lane & 8) ? OOB
        : WS_P0 + (unsigned)(r * 61440) + (unsigned)((jl < 24 ? 32 * (24 * jp + jl) : 3072 + 32 * (6 * jp + jl - 24)) +
          4 * lc + 2 * (lane >> 5) + ((lane >> 4) & 1)) * 16u;
    SCHED_FENCE();
    __syncthreads();
    SCHED_FENCE();
    float mb, sd;
    {
      float e[10];
      e[0] = bflo(xs0.x); e[1] = bfhi(xs0.x); e[2] = bflo(xs0.y); e[3] = bfhi(xs0.y);
      e[4] = bflo(xs0.z); e[5] = bfhi(xs0.z); e[6] = bflo(xs0.w); e[7] = bfhi(xs0.w);
      e[8] = bflo(xs1);   e[9] = bfhi(xs1);
      float t = 0.f;
#pragma unroll
      for (int i = 0; i < 10; ++i) t += e[i];
      t = sum_hi(sum8(t));
      const float mean = t * (1.0f / 640.0f);
      float q2 = 0.f;
#pragma unroll
      for (int i = 0; i < 10; ++i) { const float d = e[i] - mean; q2 += d * d; }
      q2 = sum_hi(sum8(q2));
      const float var = rbf(q2 * (1.0f / 640.0f));
      mb = rbf(mean);
      sd = rbf(sqrtf(rbf(var + 1e-5f)));
    }
    const unsigned sh = 16u - 16u * w;
    const unsigned xr[8] = {xr0.x, xr0.y, xr0.z, xr0.w, xr1.x, xr1.y, xr1.z, xr1.w};
    const unsigned mm = (lane & 1) ? 0xFFFF0000u : 0x0000FFFFu;
    const unsigned m0 = (lane & 2) ? 0u : mm, m1 = (lane & 2) ? mm : 0u;
    const f32x4 zero = {0.f, 0.f, 0.f, 0.f};
    f32x4 lo[2] = {zero, zero}, hi[2] = {zero, zero};
#define QSTEP(i) do {                                                            \
      const float nv = rbf(asf((xr[i] << sh) & 0xFFFF0000u) - mb) / sd;          \
      const float xd = asf(pk2(nv, nv));                                         \
      const unsigned d[4] = {asu(dpp<0x00>(xd)), asu(dpp<0x55>(xd)), asu(dpp<0xAA>(xd)), asu(dpp<0xFF>(xd))}; \
      _Pragma("unroll")                                                          \
      for (int c = 0; c < 4; ++c) {                                              \
        const v2u a = {d[c] & m0, d[c] & m1};                                    \
        lo[c & 1] = mfma4(a, wa[4 * (i) + c].xy, lo[c & 1]);                     \
        hi[c & 1] = mfma4(a, wa[4 * (i) + c].zw, hi[c & 1]);                     \
      }                                                                          \
    } while (0)
#pragma unroll
    for (int i = 0; i < 10 - QNB; ++i) QSTEP(i);
    if (!r) {
#pragma unroll
      for (int i = 10 - QNB; i < QNB; ++i) QSTEP(i);
    }
    lo[0] += lo[1];
    hi[0] += hi[1];
    float u0 = fold16(fold32(lo[0][0], hi[0][0]), fold32(lo[0][2], hi[0][2]));
    float u1 = fold16(fold32(lo[0][1], hi[0][1]), fold32(lo[0][3], hi[0][3]));
    u0 += dpp<0x128>(u0);
    u1 += dpp<0x128>(u1);
    const unsigned tag = (epoch + 1u) | 0x80000000u;
    {
      const f32x2 uu = {u0, u1};
      *(f32x2*)(red3 + w * 128 + lane * 2) = uu;
      CBAR();
      unsigned old = 0u;
      if (lane == 0) old = __hip_atomic_fetch_add(&flg[4], 1u, __ATOMIC_RELAXED, __HIP_MEMORY_SCOPE_WORKGROUP);
      old = __builtin_amdgcn_readfirstlane(old);
      CBAR();
      if (old == 1u) {
        const f32x2 t = *(const f32x2*)(red3 + lane * 2) + *(const f32x2*)(red3 + 128 + lane * 2);
        const v4u g = {asu(t.x), tag, asu(t.y), tag};
        __builtin_amdgcn_raw_buffer_store_b128(g, rsWs, p0_off, 0, AUX_SC1);
      }
    }
    STAMP(1);
    __builtin_amdgcn_s_setprio(0);
    SCHED_FENCE();
#pragma unroll
    for (int g = 0; g < 2; ++g)
#pragma unroll
      for (int j = 0; j < 3; ++j) kv[g][j] = __builtin_amdgcn_raw_buffer_load_b128(rsK, vkv + j * 128, g * 3072, AUX_NT);
  } else {
#pragma unroll
    for (int g = 0; g < 2; ++g)
#pragma unroll
      for (int j = 0; j < 3; ++j) kv[g][j] = __builtin_amdgcn_raw_buffer_load_b128(rsK, vkv + j * 128, g * 3072, AUX_NT);
    SCHED_FENCE();
    __syncthreads();
  }
  SCHED_FENCE();
#pragma unroll
  for (int g = 2; g < 8; ++g) {
#pragma unroll
    for (int j = 0; j < 3; ++j) kv[g][j] = __builtin_amdgcn_raw_buffer_load_b128(rsK, vkv + j * 128, g * 3072, AUX_NT);
    SCHED_FENCE();
  }
#pragma unroll
  for (int g = 0; g < 8; ++g) {
#pragma unroll
    for (int j = 0; j < 3; ++j) vv[g][j] = __builtin_amdgcn_raw_buffer_load_b128(rsV, vkv + j * 128, g * 3072, AUX_NT);
    SCHED_FENCE();
  }
  STAMP(9);
  if (WE && lane == 0) __hip_atomic_fetch_add(&flg[2], 1u, __ATOMIC_RELAXED, __HIP_MEMORY_SCOPE_WORKGROUP);
  if (w == 2) {
    const unsigned tag = (epoch + 1u) | 0x80000000u;
    const unsigned ga = WS_P0 + (unsigned)(96 * h + lane) * 16u;
    const unsigned gb = lane < 32 ? ga + 1024u : lane < 56 ? WS_P0 + (unsigned)(3072 + 24 * h + lane - 32) * 16u : OOB;
    v4u g[4];
#pragma unroll 1
    for (unsigned spins = 0; spins < SPIN_CAP; ++spins) {
      asm volatile("" ::: "memory");
      g[0] = __builtin_amdgcn_raw_buffer_load_b128(rsWs, ga, 0, AUX_SC1);
      g[1] = __builtin_amdgcn_raw_buffer_load_b128(rsWs, ga, 61440, AUX_SC1);
      g[2] = __builtin_amdgcn_raw_buffer_load_b128(rsWs, gb, 0, AUX_SC1);
      g[3] = __builtin_amdgcn_raw_buffer_load_b128(rsWs, gb, 61440, AUX_SC1);
      const bool ok0 = (g[0].y == tag) & (g[0].w == tag) & (g[1].y == tag) & (g[1].w == tag);
      const bool ok1 = (g[2].y == tag) & (g[2].w == tag) & (g[3].y == tag) & (g[3].w == tag);
      if (__all(ok0 & (ok1 | (lane >= 56)))) break;
      __builtin_amdgcn_s_sleep(1);
    }
    qs[lane] = pk2(asf(g[0].x) + asf(g[1].x), asf(g[0].z) + asf(g[1].z));
    float t0 = asf(g[2].x) + asf(g[3].x), t1 = asf(g[2].z) + asf(g[3].z);
    if (lane >= 32) { t0 = fmaxf(t0, 0.f); t1 = fmaxf(t1, 0.f); }
    qs[64 + lane] = pk2(t0, t1);
    __builtin_amdgcn_fence(__ATOMIC_RELEASE, "workgroup");
    if (lane == 0) __hip_atomic_store(&flg[0], 1u, __ATOMIC_RELAXED, __HIP_MEMORY_SCOPE_WORKGROUP);
    STAMP(2);
  }
#pragma unroll 1
  for (unsigned spins = 0; spins < (SPIN_CAP << 6); ++spins) {
    if (__hip_atomic_load(&flg[0], __ATOMIC_RELAXED, __HIP_MEMORY_SCOPE_WORKGROUP)) break;
    __builtin_amdgcn_s_sleep(1);
  }
  __builtin_amdgcn_fence(__ATOMIC_ACQUIRE, "workgroup");
  v4u qf[3];
#pragma unroll
  for (int j = 0; j < 3; ++j) qf[j] = *(const v4u*)(qs + 32 * j + 4 * lc);

  float sc[8];
#pragma unroll
  for (int g = 0; g < 8; ++g) {
    float a = 0.f;
#pragma unroll
    for (int j = 0; j < 3; ++j) {
      a = dot2(kv[g][j].x, qf[j].x, a);
      a = dot2(kv[g][j].y, qf[j].y, a);
      a = dot2(kv[g][j].z, qf[j].z, a);
      a = dot2(kv[g][j].w, qf[j].w, a);
    }
    a = sum8(a);
    sc[g] = rbf(rbf(a) * 0.07216878364870322f);
  }
  SCHED_FENCE();
  STAMP(3);
  unsigned pd[8];
  {
    float m = fmaxf(fmaxf(fmaxf(sc[0], sc[1]), fmaxf(sc[2], sc[3])), fmaxf(fmaxf(sc[4], sc[5]), fmaxf(sc[6], sc[7])));
    m = max_hi(m);
    float l = 0.f;
#pragma unroll
    for (int g = 0; g < 8; ++g) {
      const float p = __builtin_amdgcn_exp2f((sc[g] - m) * 1.4426950408889634f);
      l += p;
      pd[g] = pk2(p, p);
    }
    l = sum_hi(l);
    if (lane == 0) { mls[w] = m; mls[8 + w] = l; }
  }
  SCHED_FENCE();

  {
    f32x4 oacc[3] = {{0.f, 0.f, 0.f, 0.f}, {0.f, 0.f, 0.f, 0.f}, {0.f, 0.f, 0.f, 0.f}};
#pragma unroll
    for (int g = 0; g < 8; ++g) {
      const v4u bf = bfrag(pd[g], mask);
#pragma unroll
      for (int j = 0; j < 3; ++j) oacc[j] = mfma(bf, vv[g][j], oacc[j]);
      SCHED_FENCE();
    }
    float* const myred = red + w * RS + (rq >> 1) * 192 + 8 * (col & 7) + 4 * (rq & 1);
    if (dvalid) {
#pragma unroll
      for (int j = 0; j < 3; ++j) *(f32x4*)(myred + j * 64) = oacc[j];
    }
  }
  STAMP(10);
  const unsigned tag = (epoch + 1u) | 0x80000000u;
  CBAR();
  if (lane == 0) __hip_atomic_fetch_add(&flg[3], 1u, __ATOMIC_RELAXED, __HIP_MEMORY_SCOPE_WORKGROUP);
  CBAR();
  const int pw = col & 7, j8 = 2 * rq + (col >> 3);
  const float* const lsrc = red + pw * RS + 48 * (w & 3) + 6 * j8;
  const unsigned yz_off = col < 10 ? (unsigned)((s * 32 + h) * 640 + (4 * col + 2 * (rq & 1) + (rq >> 1)) * 16) : OOB;
  const unsigned vo3 = col < 10 ? (unsigned)(rq * 15360 + col * 16) : OOB;
  const unsigned oaddr = (unsigned)(size_t)((__attribute__((address_space(3))) unsigned*)osh) + (unsigned)(256 * (w & 3) + 32 * j8);
  unsigned mlo8[4], mhi8[4];
#pragma unroll
  for (int i = 0; i < 4; ++i) { mlo8[i] = col < 8 ? mask[i] : 0u; mhi8[i] = col < 8 ? 0u : mask[i]; }
  if (w >= 4) {
    unsigned po = pw < 3 ? WS_P1 + (unsigned)(wg * P1S + (24 * (w & 3) + 8 * pw + j8) * 16) : OOB;
    if (w == 4 && lane == 3) po = WS_ML + (unsigned)wg * 16u;
    const rsrc_t rsWo = mk(wo + (size_t)(192 * h + 48 * (w & 3)) * 640 + 80 * s, 47 * 1280 + 160);
#pragma unroll 1
    for (unsigned spins = 0; spins < (SPIN_CAP << 6); ++spins) {
      if (__hip_atomic_load(&flg[3], __ATOMIC_RELAXED, __HIP_MEMORY_SCOPE_WORKGROUP) >= 8u) break;
      __builtin_amdgcn_s_sleep(1);
    }
    CBAR();
    {
      f32x2 t[3];
#pragma unroll
      for (int i = 0; i < 3; ++i) t[i] = *(const f32x2*)(lsrc + 2 * i) + *(const f32x2*)(lsrc + 192 + 2 * i);
      const float m = mls[pw], l = mls[8 + pw];
      const float M = max8(m);
      const float f = __builtin_amdgcn_exp2f((m - M) * 1.4426950408889634f);
      const float L = sum8(f * l);
#pragma unroll
      for (int i = 0; i < 3; ++i) { t[i].x = sum8(f * t[i].x); t[i].y = sum8(f * t[i].y); }
      const float e0 = pw == 0 ? t[0].x : pw == 1 ? t[1].x : pw == 2 ? t[2].x : M;
      const float e1 = pw == 0 ? t[0].y : pw == 1 ? t[1].y : pw == 2 ? t[2].y : L;
      const v4u ga = {asu(e0), tag, asu(e1), tag};
      __builtin_amdgcn_raw_buffer_store_b128(ga, rsWs, po, 0, AUX_SC1);
    }
    CBAR();
    if (lane == 0) __hip_atomic_fetch_add(&flg[7], 1u, __ATOMIC_RELAXED, __HIP_MEMORY_SCOPE_WORKGROUP);
    CBAR();
    STAMP(12);
    SCHED_FENCE();
    if (WOSL) __builtin_amdgcn_s_sleep(WOSL);
    if (WE < 12) {
    v4u gv[12];
#pragma unroll
    for (int g = WE; g < 12; ++g) gv[g] = __builtin_amdgcn_raw_buffer_load_b128(rsWo, vo3, g * 1280, AUX_NT);
    SCHED_FENCE();
    STAMP(4);
    v4u oa, ob;
#pragma unroll 1
    for (unsigned spins = 0; spins < (SPIN_CAP << 6); ++spins) {
      asm volatile("ds_read_b128 %0, %2 offset:16\n ds_read_b128 %1, %2\n s_waitcnt lgkmcnt(0)"
                   : "=&v"(ob), "=&v"(oa) : "v"(oaddr) : "memory");
      if (__all(ob.z != 0u)) break;
      __builtin_amdgcn_s_sleep(1);
    }
    STAMP(13);
    const unsigned od[6] = {oa.x, oa.y, oa.z, oa.w, ob.x, ob.y};
    f32x4 ya = {0.f, 0.f, 0.f, 0.f}, yb = {0.f, 0.f, 0.f, 0.f};
#pragma unroll
    for (int i = WE; i < 12; ++i) {
      if (i & 1) yb = mfma(bfrag(od[i % 6], i < 6 ? mlo8 : mhi8), gv[i], yb);
      else ya = mfma(bfrag(od[i % 6], i < 6 ? mlo8 : mhi8), gv[i], ya);
    }
    Y_FINISH();
    }
  } else {
    if (w == 3) {
      const rsrc_t rsW2 = mk(w2 + (size_t)(48 * h) * 640 + 80 * s, 47 * 1280 + 160);
      v4u gz[12];
#pragma unroll
      for (int g = 0; g < 12; ++g) gz[g] = __builtin_amdgcn_raw_buffer_load_b128(rsW2, vo3, g * 1280, AUX_NT);
      const v2u* hb = (const v2u*)(qs + 96 + 6 * rq);
      const v2u h0 = hb[0], h1 = hb[1], h2 = hb[2];
      const unsigned hv[6] = {h0.x, h0.y, h1.x, h1.y, h2.x, h2.y};
      f32x4 za = {0.f, 0.f, 0.f, 0.f}, zb = {0.f, 0.f, 0.f, 0.f};
#pragma unroll
      for (int i = 0; i < 6; ++i) {
        za = mfma(bfrag(__builtin_amdgcn_perm(hv[i], hv[i], 0x01000100u), mask), gz[2 * i], za);
        zb = mfma(bfrag(__builtin_amdgcn_perm(hv[i], hv[i], 0x03020302u), mask), gz[2 * i + 1], zb);
      }
      za += zb;
      const v4u g = {asu(rq < 2 ? za[0] : za[2]), tag, asu(rq < 2 ? za[1] : za[3]), tag};
      __builtin_amdgcn_raw_buffer_store_b128(g, rsWs, yz_off + WS_Z, 0, AUX_SC1);
      asm volatile("s_waitcnt vmcnt(0)" ::: "memory");
      SCHED_FENCE();
      STAMP(14);
    }
    const int n0 = 80 * s + 4 * h + 2 * (lane >> 5);
    unsigned xin = 0u;
    if (w == 0) {
      xin = __builtin_amdgcn_raw_buffer_load_b32(rsX, (unsigned)n0 * 2u, 0, 0);
      asm volatile("" : "+v"(xin));
    }
    v4u gp[4 * NSET + 1], gv[12];
    if (WE) {
      const v4u srdWo = srd(wo + (size_t)(192 * h + 48 * w) * 640 + 80 * s, 47 * 1280 + 160);
#pragma unroll 1
      for (unsigned spins = 0; spins < (SPIN_CAP << 6); ++spins) {
        if (__hip_atomic_load(&flg[2], __ATOMIC_RELAXED, __HIP_MEMORY_SCOPE_WORKGROUP) >= GVCNT) break;
        __builtin_amdgcn_s_sleep(1);
      }
      CBAR();
      LOAD_WO();
      STAMP(4);
    }
    const unsigned go = pw == s ? OOB : WS_P1 + (unsigned)((h * 8 + pw) * P1S + (24 * w + j8) * 16);
    const unsigned g0 = DIRECT0 ? go : OOB;
    unsigned mo = pw == s ? OOB : WS_ML + (unsigned)(h * 8 + pw) * 16u;
    unsigned long long pm = ~(0x0101010101010101ull << s), pd = pm, sa;
    unsigned cnt = SPIN_CAP, sc, t0, t1, t2, t3;
#pragma unroll 1
    for (unsigned spins = 0; spins < (SPIN_CAP << 6); ++spins) {
      if (__hip_atomic_load(&flg[PG2 ? 7 : 3], __ATOMIC_RELAXED, __HIP_MEMORY_SCOPE_WORKGROUP) >= (PG2 ? PG2 : 8u)) break;
      __builtin_amdgcn_s_sleep(1);
    }
    CBAR();
    STAMP(15);
    P1_ISSUE0(gp);
    unsigned fv[8];
    {
      f32x2 t[3];
#pragma unroll
      for (int i = 0; i < 3; ++i) t[i] = *(const f32x2*)(lsrc + 2 * i) + *(const f32x2*)(lsrc + 192 + 2 * i);
      const float m = mls[pw], l = mls[8 + pw];
      const float M = max8(m);
      const float f = __builtin_amdgcn_exp2f((m - M) * 1.4426950408889634f);
      fv[6] = asu(M);
      fv[7] = asu(sum8(f * l));
#pragma unroll
      for (int i = 0; i < 3; ++i) { fv[2 * i] = asu(sum8(f * t[i].x)); fv[2 * i + 1] = asu(sum8(f * t[i].y)); }
    }
    P1_LOOP(gp);
    READY_WO();
    STAMP(5);
    {
      const float M = asf(fv[6]);
      const float Mg = max8(M);
      const float fr = __builtin_amdgcn_exp2f((M - Mg) * 1.4426950408889634f);
      const float rden = __builtin_amdgcn_rcpf(sum8(fr * asf(fv[7])));
      unsigned od[6];
#pragma unroll
      for (int i = 0; i < 6; ++i) {
        const float n = sum8(fr * asf(fv[i])) * rden;
        od[i] = pk2(n, n);
      }
      const v4u oa = {od[0], od[1], od[2], od[3]}, ob = {od[4], od[5], 1u, 1u};
      if (WE < 12 && pw == 0)
        asm volatile("ds_write_b128 %0, %1\n ds_write_b128 %0, %2 offset:16" :: "v"(oaddr), "v"(oa), "v"(ob) : "memory");
      if (WE) {
        f32x4 ya = {0.f, 0.f, 0.f, 0.f}, yb = {0.f, 0.f, 0.f, 0.f};
#pragma unroll
        for (int i = 0; i < WE; ++i) {
          if (i & 1) yb = mfma(bfrag(od[i % 6], i < 6 ? mlo8 : mhi8), gv[i], yb);
          else ya = mfma(bfrag(od[i % 6], i < 6 ? mlo8 : mhi8), gv[i], ya);
        }
        Y_FINISH();
      }
    }
    P1_DRAIN(gp);
    if (w == 0 && h < 20) {
      const unsigned gbase = (unsigned)((s * 32 + (lane & 31)) * 640 + (2 * h + (lane >> 5)) * 16);
#pragma unroll 1
      for (unsigned spins = 0; spins < (SPIN_CAP << 6); ++spins) {
        if (__hip_atomic_load(&flg[6], __ATOMIC_RELAXED, __HIP_MEMORY_SCOPE_WORKGROUP)) break;
        __builtin_amdgcn_s_sleep(1);
      }
      STAMP(13);
      unsigned y0 = 0u, y1 = 0u, go = gbase, cnt = SPIN_CAP, sc, t0;
      unsigned long long pend = ~0ull;
      v4u gq[5];
      P2_LOOP(gq);
      STAMP(8);
      v4u gzz = gq[4];
#pragma unroll 1
      for (unsigned spins = 0; spins < SPIN_CAP; ++spins) {
        if (__all((gzz.y == tag) & (gzz.w == tag))) break;
        CBAR();
        gzz = __builtin_amdgcn_raw_buffer_load_b128(rsWs, gbase, WS_Z, AUX_SC1);
        asm volatile("" : "+v"(gzz));
      }
      const float z0 = rbf(sum32(asf(gzz.x))), z1 = rbf(sum32(asf(gzz.z)));
      const float a0 = sum32(asf(y0)), a1 = sum32(asf(y1));
      if ((lane & 31) == 0) {
        const float r0 = rbf(rbf(bflo(xin) + rbf(a0)) + z0);
        const float r1 = rbf(rbf(bfhi(xin) + rbf(a1)) + z1);
        *(unsigned*)(out + n0) = pk2(r0, r1);
      }
      if (wg == 0 && lane == 0) __builtin_amdgcn_raw_buffer_store_b32(epoch + 1u, rsWs, 0, 0, AUX_SC1);
      P2_DRAIN(gq);
    }
  }
}

}

extern "C" void kernel_launch(void* const* d_in, const int* in_sizes, int n_in, void* d_out, int out_size, void* d_ws,
                              size_t ws_size, hipStream_t stream) {
  (void)in_sizes; (void)n_in; (void)out_size;
  if (ws_size < WS_BYTES) return;
  fused_decode_kernel<<<dim3(256), dim3(512), 0, stream>>>(
      (const unsigned short*)d_in[0], (const unsigned short*)d_in[1], (const unsigned short*)d_in[2],
      (const unsigned short*)d_in[3], (const unsigned short*)d_in[4], (const unsigned short*)d_in[6],
      (const unsigned short*)d_in[7], (unsigned short*)d_out, (unsigned char*)d_ws);
}
```

```cpp
#include <hip/hip_runtime.h>
#include <hip/hip_bf16.h>

namespace {

typedef unsigned v4u __attribute__((ext_vector_type(4)));
typedef unsigned v2u __attribute__((ext_vector_type(2)));
typedef float f32x4 __attribute__((ext_vector_type(4)));
typedef float f32x2 __attribute__((ext_vector_type(2)));
typedef __bf16 bfx8 __attribute__((ext_vector_type(8)));
typedef __bf16 bfx2 __attribute__((ext_vector_type(2)));
typedef __amdgpu_buffer_rsrc_t rsrc_t;

constexpr int AUX_NT = 2;
constexpr int AUX_SC1 = 16;
constexpr int AUX_POLL = (int)(16u | 0x80000000u);
constexpr unsigned OOB = 0x80000000u;
constexpr unsigned SPIN_CAP = 1u << 16;

constexpr unsigned WS_P0 = 4096;
constexpr unsigned WS_H = WS_P0 + 3072u * 8u;
constexpr unsigned P1S = 1664;
constexpr unsigned WS_P1 = WS_P0 + 122880u;
constexpr unsigned WS_ML = WS_P1 + 256u * P1S;
constexpr unsigned WS_Y = WS_ML + 4096u;
constexpr unsigned WS_Z = WS_Y + 8u * 32u * 640u;
constexpr unsigned WS_BYTES = WS_Z + 8u * 32u * 640u;
constexpr int RS = 392;

#define NQW 1
#define QNB 6
#define QB 40
#define QPRIO 0
#define QAUX AUX_NT
#define PG2 4
#define WE 3
#define GVCNT 8
#define WOSL 0
#define FEW 2
#define DIRECT0 1
#define NY 4
#define YFEW 64
#define YSL 8
#define NSET 1
#define PSL 0
#define STAG1 8
#define STAG2 6

__device__ __forceinline__ rsrc_t mk(const void* p, unsigned bytes) {
  return __builtin_amdgcn_make_buffer_rsrc((void*)p, (short)0, (int)bytes, 0x00020000);
}
__device__ __forceinline__ float asf(unsigned u) { return __builtin_bit_cast(float, u); }
__device__ __forceinline__ unsigned asu(float f) { return __builtin_bit_cast(unsigned, f); }
__device__ __forceinline__ float rbf(float f) { return (float)(__bf16)f; }
__device__ __forceinline__ float bflo(unsigned u) { return __builtin_bit_cast(float, u << 16); }
__device__ __forceinline__ float bfhi(unsigned u) { return __builtin_bit_cast(float, u & 0xFFFF0000u); }
__device__ __forceinline__ unsigned pk2(float a, float b) {
  f32x2 v = {a, b};
  return __builtin_bit_cast(unsigned, __builtin_convertvector(v, bfx2));
}
__device__ __forceinline__ f32x4 mfma(v4u a, v4u b, f32x4 c) {
  return __builtin_amdgcn_mfma_f32_16x16x32_bf16(__builtin_bit_cast(bfx8, a), __builtin_bit_cast(bfx8, b), c, 0, 0, 0);
}
typedef short s16x4 __attribute__((ext_vector_type(4)));
__device__ __forceinline__ f32x4 mfma4(v2u a, v2u b, f32x4 c) {
  return __builtin_amdgcn_mfma_f32_4x4x4bf16_1k(__builtin_bit_cast(s16x4, a), __builtin_bit_cast(s16x4, b), c, 0, 0, 0);
}
__device__ __forceinline__ float dot2(unsigned a, unsigned b, float c) {
  return __builtin_amdgcn_fdot2_f32_bf16(__builtin_bit_cast(bfx2, a), __builtin_bit_cast(bfx2, b), c, false);
}
template <int CTRL> __device__ __forceinline__ float dpp(float v) {
  return __builtin_bit_cast(float, __builtin_amdgcn_mov_dpp(__builtin_bit_cast(int, v), CTRL, 0xF, 0xF, true));
}
__device__ __forceinline__ float sum8(float v) {
  v += dpp<0xB1>(v);
  v += dpp<0x4E>(v);
  v += dpp<0x141>(v);
  return v;
}
__device__ __forceinline__ float max8(float v) {
  v = fmaxf(v, dpp<0xB1>(v));
  v = fmaxf(v, dpp<0x4E>(v));
  v = fmaxf(v, dpp<0x141>(v));
  return v;
}
__device__ __forceinline__ float sum_hi(float v) {
  v += dpp<0x128>(v);
  v += __shfl_xor(v, 16);
  v += __shfl_xor(v, 32);
  return v;
}
__device__ __forceinline__ float max_hi(float v) {
  v = fmaxf(v, dpp<0x128>(v));
  v = fmaxf(v, __shfl_xor(v, 16));
  v = fmaxf(v, __shfl_xor(v, 32));
  return v;
}
__device__ __forceinline__ v4u bfrag(unsigned dup, const unsigned (&mask)[4]) {
  v4u b;
  b.x = dup & mask[0];
  b.y = dup & mask[1];
  b.z = dup & mask[2];
  b.w = dup & mask[3];
  return b;
}
__device__ __forceinline__ float fold32(float a, float b) {
  const auto r = __builtin_amdgcn_permlane32_swap(asu(a), asu(b), false, false);
  return asf(r[0]) + asf(r[1]);
}
__device__ __forceinline__ float fold16(float a, float b) {
  const auto r = __builtin_amdgcn_permlane16_swap(asu(a), asu(b), false, false);
  return asf(r[0]) + asf(r[1]);
}
__device__ __forceinline__ float sum32(float v) {
  v = sum8(v);
  v += dpp<0x128>(v);
  const auto r = __builtin_amdgcn_permlane16_swap(asu(v), asu(v), false, false);
  return asf(r[0]) + asf(r[1]);
}
#define SCHED_FENCE() __builtin_amdgcn_sched_barrier(0)
#define CBAR() asm volatile("" ::: "memory")
#define STR_(x) #x
#define STR(x) STR_(x)
#define PSL_TXT ""
__device__ __forceinline__ v4u srd(const void* p, unsigned bytes) {
  const unsigned long long a = (unsigned long long)p;
  const v4u r = {(unsigned)a, (unsigned)(a >> 32) & 0xFFFFu, bytes, 0x00020000u};
  return r;
}
#define LWO0 if (WE > 0) asm volatile("s_nop 4\n buffer_load_dwordx4 v[160:163], %1, %2, %3 offen offset:0 nt" : "={v[160:163]}"(gv[0]) : "v"(vo3), "s"(srdWo), "s"(0) : "memory")
#define RWO0 if (WE > 0) asm volatile("" : "+{v[160:163]}"(gv[0]) :: "memory")
#define LWO1 if (WE > 1) asm volatile("s_nop 4\n buffer_load_dwordx4 v[164:167], %1, %2, %3 offen offset:1280 nt" : "={v[164:167]}"(gv[1]) : "v"(vo3), "s"(srdWo), "s"(0) : "memory")
#define RWO1 if (WE > 1) asm volatile("" : "+{v[164:167]}"(gv[1]) :: "memory")
#define LWO2 if (WE > 2) asm volatile("s_nop 4\n buffer_load_dwordx4 v[168:171], %1, %2, %3 offen offset:2560 nt" : "={v[168:171]}"(gv[2]) : "v"(vo3), "s"(srdWo), "s"(0) : "memory")
#define RWO2 if (WE > 2) asm volatile("" : "+{v[168:171]}"(gv[2]) :: "memory")
#define LWO3 if (WE > 3) asm volatile("s_nop 4\n buffer_load_dwordx4 v[172:175], %1, %2, %3 offen offset:3840 nt" : "={v[172:175]}"(gv[3]) : "v"(vo3), "s"(srdWo), "s"(0) : "memory")
#define RWO3 if (WE > 3) asm volatile("" : "+{v[172:175]}"(gv[3]) :: "memory")
#define LWO4 if (WE > 4) asm volatile("s_nop 4\n buffer_load_dwordx4 v[176:179], %1, %2, %3 offen offset:0 nt" : "={v[176:179]}"(gv[4]) : "v"(vo3), "s"(srdWo), "s"(5120) : "memory")
#define RWO4 if (WE > 4) asm volatile("" : "+{v[176:179]}"(gv[4]) :: "memory")
#define LWO5 if (WE > 5) asm volatile("s_nop 4\n buffer_load_dwordx4 v[180:183], %1, %2, %3 offen offset:1280 nt" : "={v[180:183]}"(gv[5]) : "v"(vo3), "s"(srdWo), "s"(5120) : "memory")
#define RWO5 if (WE > 5) asm volatile("" : "+{v[180:183]}"(gv[5]) :: "memory")
#define LWO6 if (WE > 6) asm volatile("s_nop 4\n buffer_load_dwordx4 v[184:187], %1, %2, %3 offen offset:2560 nt" : "={v[184:187]}"(gv[6]) : "v"(vo3), "s"(srdWo), "s"(5120) : "memory")
#define RWO6 if (WE > 6) asm volatile("" : "+{v[184:187]}"(gv[6]) :: "memory")
#define LWO7 if (WE > 7) asm volatile("s_nop 4\n buffer_load_dwordx4 v[188:191], %1, %2, %3 offen offset:3840 nt" : "={v[188:191]}"(gv[7]) : "v"(vo3), "s"(srdWo), "s"(5120) : "memory")
#define RWO7 if (WE > 7) asm volatile("" : "+{v[188:191]}"(gv[7]) :: "memory")
#define LWO8 if (WE > 8) asm volatile("s_nop 4\n buffer_load_dwordx4 v[192:195], %1, %2, %3 offen offset:0 nt" : "={v[192:195]}"(gv[8]) : "v"(vo3), "s"(srdWo), "s"(10240) : "memory")
#define RWO8 if (WE > 8) asm volatile("" : "+{v[192:195]}"(gv[8]) :: "memory")
#define LWO9 if (WE > 9) asm volatile("s_nop 4\n buffer_load_dwordx4 v[196:199], %1, %2, %3 offen offset:1280 nt" : "={v[196:199]}"(gv[9]) : "v"(vo3), "s"(srdWo), "s"(10240) : "memory")
#define RWO9 if (WE > 9) asm volatile("" : "+{v[196:199]}"(gv[9]) :: "memory")
#define LWO10 if (WE > 10) asm volatile("s_nop 4\n buffer_load_dwordx4 v[200:203], %1, %2, %3 offen offset:2560 nt" : "={v[200:203]}"(gv[10]) : "v"(vo3), "s"(srdWo), "s"(10240) : "memory")
#define RWO10 if (WE > 10) asm volatile("" : "+{v[200:203]}"(gv[10]) :: "memory")
#define LWO11 if (WE > 11) asm volatile("s_nop 4\n buffer_load_dwordx4 v[204:207], %1, %2, %3 offen offset:3840 nt" : "={v[204:207]}"(gv[11]) : "v"(vo3), "s"(srdWo), "s"(10240) : "memory")
#define RWO11 if (WE > 11) asm volatile("" : "+{v[204:207]}"(gv[11]) :: "memory")
#define LOAD_WO() do { LWO0; LWO1; LWO2; LWO3; LWO4; LWO5; LWO6; LWO7; LWO8; LWO9; LWO10; LWO11; } while (0)
#define READY_WO() do { RWO0; RWO1; RWO2; RWO3; RWO4; RWO5; RWO6; RWO7; RWO8; RWO9; RWO10; RWO11; } while (0)
#define P1_ISSUE0(G) asm volatile("s_nop 4\n" "buffer_load_dwordx4 v[220:223], %5, %6, 0 offen sc1\n" "buffer_load_dwordx4 v[208:211], %4, %6, 0 offen offset:0 sc1\n" "buffer_load_dwordx4 v[212:215], %4, %6, 0 offen offset:128 sc1\n" "buffer_load_dwordx4 v[216:219], %4, %6, 0 offen offset:256 sc1\n"  \
  : "={v[208:211]}"(G[0]), "={v[212:215]}"(G[1]), "={v[216:219]}"(G[2]), "={v[220:223]}"(G[3]) : "v"(g0), "v"(mo), "s"(srdWs) : "memory")
#define P1_ISSUE1(G) asm volatile("s_nop 4\n" "buffer_load_dwordx4 v[236:239], %5, %6, 0 offen sc1\n" "buffer_load_dwordx4 v[224:227], %4, %6, 0 offen offset:0 sc1\n" "buffer_load_dwordx4 v[228:231], %4, %6, 0 offen offset:128 sc1\n" "buffer_load_dwordx4 v[232:235], %4, %6, 0 offen offset:256 sc1\n"  \
  : "={v[224:227]}"(G[4]), "={v[228:231]}"(G[5]), "={v[232:235]}"(G[6]), "={v[236:239]}"(G[7]) : "v"(g0), "v"(mo), "s"(srdWs) : "memory")
#define P1_ISSUE2(G) asm volatile("s_nop 4\n" "buffer_load_dwordx4 v[252:255], %5, %6, 0 offen sc1\n" "buffer_load_dwordx4 v[240:243], %4, %6, 0 offen offset:0 sc1\n" "buffer_load_dwordx4 v[244:247], %4, %6, 0 offen offset:128 sc1\n" "buffer_load_dwordx4 v[248:251], %4, %6, 0 offen offset:256 sc1\n"  \
  : "={v[240:243]}"(G[8]), "={v[244:247]}"(G[9]), "={v[248:251]}"(G[10]), "={v[252:255]}"(G[11]) : "v"(g0), "v"(mo), "s"(srdWs) : "memory")
#define ZCNT 16
#define P1_LOOP(G) asm volatile( \
      ".Lp1l%=:\n" \
      "s_waitcnt vmcnt(0)\n" \
      "v_bitop3_b32 %[t0], v221, v223, %[tag] bitop3:0x7e\n" \
      "v_bitop3_b32 %[t1], v209, v211, %[tag] bitop3:0x7e\n" \
      "v_bitop3_b32 %[t2], v213, v215, %[tag] bitop3:0x7e\n" \
      "v_bitop3_b32 %[t3], v217, v219, %[tag] bitop3:0x7e\n" \
      "v_cmp_eq_u32 %[sa], 0, %[t0]\n" \
      "v_or3_b32 %[t1], %[t1], %[t2], %[t3]\n" \
      "v_cmp_eq_u32 vcc, 0, %[t1]\n" \
      "s_nop 0\n" \
      "v_cndmask_b32 %[f6], %[f6], v220, %[sa]\n" \
      "v_cndmask_b32 %[f7], %[f7], v222, %[sa]\n" \
      "v_cndmask_b32 %[mo], %[mo], %[oob], %[sa]\n" \
      "v_cndmask_b32 %[f0], %[f0], v208, vcc\n" \
      "v_cndmask_b32 %[f1], %[f1], v210, vcc\n" \
      "v_cndmask_b32 %[f2], %[f2], v212, vcc\n" \
      "v_cndmask_b32 %[f3], %[f3], v214, vcc\n" \
      "v_cndmask_b32 %[f4], %[f4], v216, vcc\n" \
      "v_cndmask_b32 %[f5], %[f5], v218, vcc\n" \
      "s_andn2_b64 %[pm], %[pm], %[sa]\n" \
      "s_andn2_b64 %[pd], %[pd], vcc\n" \
      "s_or_b64 %[sa], %[pm], %[pd]\n" \
      "s_cbranch_scc0 .Lp1x%=\n" \
      "s_bcnt1_i32_b64 %[sc], %[pd]\n" \
      "s_cmp_le_u32 %[sc], " STR(FEW) " * 8\n" \
      "s_cselect_b64 %[sa], 0, %[pm]\n" \
      "s_andn2_b64 %[sa], %[pd], %[sa]\n" \
      "v_cndmask_b32 %[t0], %[oob], %[go], %[sa]\n" \
      PSL_TXT \
      "buffer_load_dwordx4 v[220:223], %[mo], %[rs], 0 offen sc1\n" "buffer_load_dwordx4 v[208:211], %[t0], %[rs], 0 offen offset:0 sc1\n" "buffer_load_dwordx4 v[212:215], %[t0], %[rs], 0 offen offset:128 sc1\n" "buffer_load_dwordx4 v[216:219], %[t0], %[rs], 0 offen offset:256 sc1\n"  \
      "s_sub_u32 %[cnt], %[cnt], 1\n" "s_cbranch_scc0 .Lp1l%=\n" ".Lp1x%=:\n" "s_nop 1\n" \
      : "+{v[208:211]}"(G[0]), "+{v[212:215]}"(G[1]), "+{v[216:219]}"(G[2]), "+{v[220:223]}"(G[3]), \
        [f0] "+v"(fv[0]), [f1] "+v"(fv[1]), [f2] "+v"(fv[2]), [f3] "+v"(fv[3]), [f4] "+v"(fv[4]), [f5] "+v"(fv[5]), [f6] "+v"(fv[6]), [f7] "+v"(fv[7]), \
        [mo] "+v"(mo), [pm] "+s"(pm), [pd] "+s"(pd), [cnt] "+s"(cnt), [sa] "=&s"(sa), [sc] "=&s"(sc), \
        [t0] "=&v"(t0), [t1] "=&v"(t1), [t2] "=&v"(t2), [t3] "=&v"(t3) \
      : [go] "v"(go), [tag] "v"(tag), [oob] "v"(OOB), [rs] "s"(srdWs) : "vcc", "scc", "memory")
#define P1_DRAIN(G) asm volatile("s_waitcnt vmcnt(0)" :: "{v[208:211]}"(G[0]), "{v[212:215]}"(G[1]), "{v[216:219]}"(G[2]), "{v[220:223]}"(G[3]) : "memory")
#define P2_LOOP(G) asm volatile( \
      "s_nop 4\n" "buffer_load_dwordx4 v[224:227], %[go], %[rs], %[sz] offen sc1\n" \
      "buffer_load_dwordx4 v[208:211], %[go], %[rs], %[so] offen sc1\n" "s_sleep " STR(STAG2) "\n" \
      "buffer_load_dwordx4 v[212:215], %[go], %[rs], %[so] offen sc1\n" "s_sleep " STR(STAG2) "\n" \
      "buffer_load_dwordx4 v[216:219], %[go], %[rs], %[so] offen sc1\n" "s_sleep " STR(STAG2) "\n" \
      "buffer_load_dwordx4 v[220:223], %[go], %[rs], %[so] offen sc1\n" \
      ".Lp2l%=:\n" \
      "s_waitcnt vmcnt(3)\n" \
      "v_bitop3_b32 %[t0], v209, v211, %[tag] bitop3:0x7e\n" \
      "v_cmp_eq_u32 vcc, 0, %[t0]\n" \
      "s_nop 1\n" \
      "v_cndmask_b32 %[go], %[go], %[oob], vcc\n" \
      "v_cndmask_b32 %[y0], %[y0], v208, vcc\n" \
      "v_cndmask_b32 %[y1], %[y1], v210, vcc\n" \
      "s_andn2_b64 %[pend], %[pend], vcc\n" \
      "s_cbranch_scc0 .Lp2x%=\n" \
      "s_bcnt1_i32_b64 %[sc], %[pend]\n" \
      "s_cmp_le_u32 %[sc], " STR(YFEW) "\n" \
      "s_cbranch_scc1 .Lp2s0%=\n" \
      "s_sleep " STR(YSL) "\n" \
      ".Lp2s0%=:\n" \
      "buffer_load_dwordx4 v[208:211], %[go], %[rs], %[so] offen sc1\n" \
      "s_waitcnt vmcnt(3)\n" \
      "v_bitop3_b32 %[t0], v213, v215, %[tag] bitop3:0x7e\n" \
      "v_cmp_eq_u32 vcc, 0, %[t0]\n" \
      "s_nop 1\n" \
      "v_cndmask_b32 %[go], %[go], %[oob], vcc\n" \
      "v_cndmask_b32 %[y0], %[y0], v212, vcc\n" \
      "v_cndmask_b32 %[y1], %[y1], v214, vcc\n" \
      "s_andn2_b64 %[pend], %[pend], vcc\n" \
      "s_cbranch_scc0 .Lp2x%=\n" \
      "s_bcnt1_i32_b64 %[sc], %[pend]\n" \
      "s_cmp_le_u32 %[sc], " STR(YFEW) "\n" \
      "s_cbranch_scc1 .Lp2s1%=\n" \
      "s_sleep " STR(YSL) "\n" \
      ".Lp2s1%=:\n" \
      "buffer_load_dwordx4 v[212:215], %[go], %[rs], %[so] offen sc1\n" \
      "s_waitcnt vmcnt(3)\n" \
      "v_bitop3_b32 %[t0], v217, v219, %[tag] bitop3:0x7e\n" \
      "v_cmp_eq_u32 vcc, 0, %[t0]\n" \
      "s_nop 1\n" \
      "v_cndmask_b32 %[go], %[go], %[oob], vcc\n" \
      "v_cndmask_b32 %[y0], %[y0], v216, vcc\n" \
      "v_cndmask_b32 %[y1], %[y1], v218, vcc\n" \
      "s_andn2_b64 %[pend], %[pend], vcc\n" \
      "s_cbranch_scc0 .Lp2x%=\n" \
      "s_bcnt1_i32_b64 %[sc], %[pend]\n" \
      "s_cmp_le_u32 %[sc], " STR(YFEW) "\n" \
      "s_cbranch_scc1 .Lp2s2%=\n" \
      "s_sleep " STR(YSL) "\n" \
      ".Lp2s2%=:\n" \
      "buffer_load_dwordx4 v[216:219], %[go], %[rs], %[so] offen sc1\n" \
      "s_waitcnt vmcnt(3)\n" \
      "v_bitop3_b32 %[t0], v221, v223, %[tag] bitop3:0x7e\n" \
      "v_cmp_eq_u32 vcc, 0, %[t0]\n" \
      "s_nop 1\n" \
      "v_cndmask_b32 %[go], %[go], %[oob], vcc\n" \
      "v_cndmask_b32 %[y0], %[y0], v220, vcc\n" \
      "v_cndmask_b32 %[y1], %[y1], v222, vcc\n" \
      "s_andn2_b64 %[pend], %[pend], vcc\n" \
      "s_cbranch_scc0 .Lp2x%=\n" \
      "s_bcnt1_i32_b64 %[sc], %[pend]\n" \
      "s_cmp_le_u32 %[sc], " STR(YFEW) "\n" \
      "s_cbranch_scc1 .Lp2s3%=\n" \
      "s_sleep " STR(YSL) "\n" \
      ".Lp2s3%=:\n" \
      "buffer_load_dwordx4 v[220:223], %[go], %[rs], %[so] offen sc1\n" \
      "s_sub_u32 %[cnt], %[cnt], 1\n" "s_cbranch_scc0 .Lp2l%=\n" ".Lp2x%=:\n" "s_nop 1\n" \
      : "=&{v[208:211]}"(G[0]), "=&{v[212:215]}"(G[1]), "=&{v[216:219]}"(G[2]), "=&{v[220:223]}"(G[3]), "=&{v[224:227]}"(G[4]), \
        [y0] "+v"(y0), [y1] "+v"(y1), [go] "+v"(go), [pend] "+s"(pend), [cnt] "+s"(cnt), [sc] "=&s"(sc), [t0] "=&v"(t0) \
      : [tag] "v"(tag), [oob] "v"(OOB), [rs] "s"(srdWs), [so] "s"(WS_Y), [sz] "s"(WS_Z) : "vcc", "scc", "memory")
#define P2_DRAIN(G) asm volatile("s_waitcnt vmcnt(0)" :: "{v[208:211]}"(G[0]), "{v[212:215]}"(G[1]), "{v[216:219]}"(G[2]), "{v[220:223]}"(G[3]) : "memory")
#define PK 0
#define PW 0
#define STAMP(k) do { if ((k) == 0 || (k) == PK) { SCHED_FENCE(); if ((k) == 0) st0_ = wall_clock64(); if ((k) == PK) stk_ = wall_clock64(); SCHED_FENCE(); } } while (0)

#define Y_FINISH() do {                                                                                         \
    ya += yb;                                                                                                   \
    f32x2 r;                                                                                                    \
    r.x = fold32(ya[0], ya[2]);                                                                                 \
    r.y = fold32(ya[1], ya[3]);                                                                                 \
    *(f32x2*)(red3 + w * 128 + lane * 2) = r;                                                                   \
    STAMP(6);                                                                                                   \
    CBAR();                                                                                                     \
    unsigned old = 0u;                                                                                          \
    if (lane == 0) old = __hip_atomic_fetch_add(&flg[5], 1u, __ATOMIC_RELAXED, __HIP_MEMORY_SCOPE_WORKGROUP);   \
    old = __builtin_amdgcn_readfirstlane(old);                                                                  \
    CBAR();                                                                                                     \
    if (old == (WE > 0 && WE < 12 ? 7u : 3u)) {                                                                 \
      const float* rp = red3 + lane * 2;                                                                        \
      f32x2 t = {0.f, 0.f};                                                                                     \
      if (WE > 0) t += (*(const f32x2*)(rp) + *(const f32x2*)(rp + 128)) + (*(const f32x2*)(rp + 256) + *(const f32x2*)(rp + 384)); \
      if (WE < 12) t += (*(const f32x2*)(rp + 512) + *(const f32x2*)(rp + 640)) + (*(const f32x2*)(rp + 768) + *(const f32x2*)(rp + 896)); \
      const v4u g = {asu(t.x), tag, asu(t.y), tag};                                                             \
      __builtin_amdgcn_raw_buffer_store_b128(g, rsWs, yz_off + WS_Y, 0, AUX_SC1);                               \
      CBAR();                                                                                                   \
      if (lane == 0) __hip_atomic_store(&flg[6], 1u, __ATOMIC_RELAXED, __HIP_MEMORY_SCOPE_WORKGROUP);           \
      CBAR();                                                                                                   \
      STAMP(7);                                                                                                 \
    }                                                                                                           \
  } while (0)

__global__ void __launch_bounds__(512, 2)
fused_decode_kernel(const unsigned short* __restrict__ x, const unsigned short* __restrict__ kc,
                    const unsigned short* __restrict__ vc, const unsigned short* __restrict__ wq,
                    const unsigned short* __restrict__ wo, const unsigned short* __restrict__ w1,
                    const unsigned short* __restrict__ w2, unsigned short* __restrict__ out,
                    unsigned char* ws) {
  __shared__ __attribute__((aligned(16))) float red[8 * RS];
  __shared__ __attribute__((aligned(16))) float red3[8 * 128];
  __shared__ __attribute__((aligned(16))) unsigned qs[128];
  __shared__ __attribute__((aligned(16))) float mls[16];
  __shared__ __attribute__((aligned(16))) unsigned osh[4 * 64];
  __shared__ unsigned flg[8];

  unsigned long long st0_ = 0, stk_ = 0;
  STAMP(0);
  const int tid = threadIdx.x;
  const int lane = tid & 63;
  const int w = __builtin_amdgcn_readfirstlane(tid >> 6);
  const int lg = lane >> 3, lc = lane & 7;
  const int col = lane & 15, rq = lane >> 4;
  const int b = blockIdx.x;
  const int h = 4 * (b & 7) + (b >> 6);
  const int s = (b >> 3) & 7;
  const int wg = h * 8 + s;

  const rsrc_t rsX = mk(x, 1280);
  const rsrc_t rsWs = mk(ws, WS_BYTES);
  const v4u srdWs = srd(ws, WS_BYTES);
  const size_t kvoff = ((size_t)h * 32768 + 28672 + 512 * s + 64 * w) * 192;
  const rsrc_t rsK = mk(kc + kvoff, 64 * 384);
  const rsrc_t rsV = mk(vc + kvoff, 64 * 384);

  const unsigned epoch = __builtin_amdgcn_raw_buffer_load_b32(rsWs, 0, 0, AUX_SC1);
  if (tid < 8) flg[tid] = 0u;
  if (tid >= 256) osh[tid - 256] = 0u;

  unsigned mask[4];
  {
    const unsigned mm = (lc & 1) ? 0xFFFF0000u : 0x0000FFFFu;
#pragma unroll
    for (int i = 0; i < 4; ++i) mask[i] = ((lc >> 1) == i) ? mm : 0u;
  }
  const bool dvalid = (rq >> 1) == (col >> 3);
  const unsigned vkv = (unsigned)(lg * 384 + lc * 16);
  v4u kv[8][3], vv[8][3];

  const int jl = b >> 3, jp = (b >> 1) & 3;
  if (w < 2 && jl < 30) {
    __builtin_amdgcn_s_setprio(3);
    const int r = b & 1;
    const int n = r ? 10 - QNB : QNB;
    const int base = r ? 64 * QNB : 0;
    const v4u xs0 = __builtin_amdgcn_raw_buffer_load_b128(rsX, 20 * lane, 0, 0);
    const unsigned xs1 = __builtin_amdgcn_raw_buffer_load_b32(rsX, 20 * lane + 16, 0, 0);
    const unsigned xo = (unsigned)(2 * base + 4 * n * (4 * lg + (lane & 3)));
    const v4u xr0 = __builtin_amdgcn_raw_buffer_load_b128(rsX, xo, 0, 0);
    const v4u xr1 = __builtin_amdgcn_raw_buffer_load_b128(rsX, xo + 16, 0, 0);
    const unsigned short* pa = jl < 24 ? wq + (size_t)base * 6144 + 64 * (24 * jp + jl)
                                       : w1 + (size_t)base * 1536 + 64 * (6 * jp + jl - 24);
    const unsigned st = jl < 24 ? 12288u : 3072u;
    const rsrc_t rsA = mk(pa, (unsigned)(64 * n - 1) * st + 128u);
    const unsigned voa = (unsigned)(8 * n * lg + w) * st + (unsigned)lc * 16u;
    v4u wa[4 * QNB];
#pragma unroll
    for (int k = 0; k < 4 * (10 - QNB); ++k) {
      wa[k] = __builtin_amdgcn_raw_buffer_load_b128(rsA, voa, (n * (k & 3) + (k >> 2)) * 2 * st, QAUX);
      SCHED_FENCE();
    }
    if (!r) {
#pragma unroll
      for (int k = 4 * (10 - QNB); k < 4 * QNB; ++k) {
        wa[k] = __builtin_amdgcn_raw_buffer_load_b128(rsA, voa, (QNB * (k & 3) + (k >> 2)) * 2 * st, QAUX);
        SCHED_FENCE();
      }
    }
    const unsigned p0_off = (lane & 8) ? OOB
        : WS_P0 + (unsigned)(r * 61440) + (unsigned)((jl < 24 ? 32 * (24 * jp + jl) : 3072 + 32 * (6 * jp + jl - 24)) +
          4 * lc + 2 * (lane >> 5) + ((lane >> 4) & 1)) * 16u;
    SCHED_FENCE();
    __syncthreads();
    SCHED_FENCE();
    float mb, sd;
    {
      float e[10];
      e[0] = bflo(xs0.x); e[1] = bfhi(xs0.x); e[2] = bflo(xs0.y); e[3] = bfhi(xs0.y);
      e[4] = bflo(xs0.z); e[5] = bfhi(xs0.z); e[6] = bflo(xs0.w); e[7] = bfhi(xs0.w);
      e[8] = bflo(xs1);   e[9] = bfhi(xs1);
      float t = 0.f;
#pragma unroll
      for (int i = 0; i < 10; ++i) t += e[i];
      t = sum_hi(sum8(t));
      const float mean = t * (1.0f / 640.0f);
      float q2 = 0.f;
#pragma unroll
      for (int i = 0; i < 10; ++i) { const float d = e[i] - mean; q2 += d * d; }
      q2 = sum_hi(sum8(q2));
      const float var = rbf(q2 * (1.0f / 640.0f));
      mb = rbf(mean);
      sd = rbf(sqrtf(rbf(var + 1e-5f)));
    }
    const unsigned sh = 16u - 16u * w;
    const unsigned xr[8] = {xr0.x, xr0.y, xr0.z, xr0.w, xr1.x, xr1.y, xr1.z, xr1.w};
    const unsigned mm = (lane & 1) ? 0xFFFF0000u : 0x0000FFFFu;
    const unsigned m0 = (lane & 2) ? 0u : mm, m1 = (lane & 2) ? mm : 0u;
    const f32x4 zero = {0.f, 0.f, 0.f, 0.f};
    f32x4 lo[2] = {zero, zero}, hi[2] = {zero, zero};
#define QSTEP(i) do {                                                            \
      const float nv = rbf(asf((xr[i] << sh) & 0xFFFF0000u) - mb) / sd;          \
      const float xd = asf(pk2(nv, nv));                                         \
      const unsigned d[4] = {asu(dpp<0x00>(xd)), asu(dpp<0x55>(xd)), asu(dpp<0xAA>(xd)), asu(dpp<0xFF>(xd))}; \
      _Pragma("unroll")                                                          \
      for (int c = 0; c < 4; ++c) {                                              \
        const v2u a = {d[c] & m0, d[c] & m1};                                    \
        lo[c & 1] = mfma4(a, wa[4 * (i) + c].xy, lo[c & 1]);                     \
        hi[c & 1] = mfma4(a, wa[4 * (i) + c].zw, hi[c & 1]);                     \
      }                                                                          \
    } while (0)
#pragma unroll
    for (int i = 0; i < 10 - QNB; ++i) QSTEP(i);
    if (!r) {
#pragma unroll
      for (int i = 10 - QNB; i < QNB; ++i) QSTEP(i);
    }
    lo[0] += lo[1];
    hi[0] += hi[1];
    float u0 = fold16(fold32(lo[0][0], hi[0][0]), fold32(lo[0][2], hi[0][2]));
    float u1 = fold16(fold32(lo[0][1], hi[0][1]), fold32(lo[0][3], hi[0][3]));
    u0 += dpp<0x128>(u0);
    u1 += dpp<0x128>(u1);
    const unsigned tag = (epoch + 1u) | 0x80000000u;
    {
      const f32x2 uu = {u0, u1};
      *(f32x2*)(red3 + w * 128 + lane * 2) = uu;
      CBAR();
      unsigned old = 0u;
      if (lane == 0) old = __hip_atomic_fetch_add(&flg[4], 1u, __ATOMIC_RELAXED, __HIP_MEMORY_SCOPE_WORKGROUP);
      old = __builtin_amdgcn_readfirstlane(old);
      CBAR();
      if (old == 1u) {
        const f32x2 t = *(const f32x2*)(red3 + lane * 2) + *(const f32x2*)(red3 + 128 + lane * 2);
        const v4u g = {asu(t.x), tag, asu(t.y), tag};
        __builtin_amdgcn_raw_buffer_store_b128(g, rsWs, p0_off, 0, AUX_SC1);
      }
    }
    STAMP(1);
    __builtin_amdgcn_s_setprio(0);
    SCHED_FENCE();
#pragma unroll
    for (int g = 0; g < 2; ++g)
#pragma unroll
      for (int j = 0; j < 3; ++j) kv[g][j] = __builtin_amdgcn_raw_buffer_load_b128(rsK, vkv + j * 128, g * 3072, AUX_NT);
  } else {
#pragma unroll
    for (int g = 0; g < 2; ++g)
#pragma unroll
      for (int j = 0; j < 3; ++j) kv[g][j] = __builtin_amdgcn_raw_buffer_load_b128(rsK, vkv + j * 128, g * 3072, AUX_NT);
    SCHED_FENCE();
    __syncthreads();
  }
  SCHED_FENCE();
#pragma unroll
  for (int g = 2; g < 8; ++g) {
#pragma unroll
    for (int j = 0; j < 3; ++j) kv[g][j] = __builtin_amdgcn_raw_buffer_load_b128(rsK, vkv + j * 128, g * 3072, AUX_NT);
    SCHED_FENCE();
  }
#pragma unroll
  for (int g = 0; g < 8; ++g) {
#pragma unroll
    for (int j = 0; j < 3; ++j) vv[g][j] = __builtin_amdgcn_raw_buffer_load_b128(rsV, vkv + j * 128, g * 3072, AUX_NT);
    SCHED_FENCE();
  }
  STAMP(9);
  if (WE && lane == 0) __hip_atomic_fetch_add(&flg[2], 1u, __ATOMIC_RELAXED, __HIP_MEMORY_SCOPE_WORKGROUP);
  if (w == 2) {
    const unsigned tag = (epoch + 1u) | 0x80000000u;
    const unsigned ga = WS_P0 + (unsigned)(96 * h + lane) * 16u;
    const unsigned gb = lane < 32 ? ga + 1024u : lane < 56 ? WS_P0 + (unsigned)(3072 + 24 * h + lane - 32) * 16u : OOB;
    v4u g[4];
#pragma unroll 1
    for (unsigned spins = 0; spins < SPIN_CAP; ++spins) {
      asm volatile("" ::: "memory");
      g[0] = __builtin_amdgcn_raw_buffer_load_b128(rsWs, ga, 0, AUX_SC1);
      g[1] = __builtin_amdgcn_raw_buffer_load_b128(rsWs, ga, 61440, AUX_SC1);
      g[2] = __builtin_amdgcn_raw_buffer_load_b128(rsWs, gb, 0, AUX_SC1);
      g[3] = __builtin_amdgcn_raw_buffer_load_b128(rsWs, gb, 61440, AUX_SC1);
      const bool ok0 = (g[0].y == tag) & (g[0].w == tag) & (g[1].y == tag) & (g[1].w == tag);
      const bool ok1 = (g[2].y == tag) & (g[2].w == tag) & (g[3].y == tag) & (g[3].w == tag);
      if (__all(ok0 & (ok1 | (lane >= 56)))) break;
      __builtin_amdgcn_s_sleep(1);
    }
    qs[lane] = pk2(asf(g[0].x) + asf(g[1].x), asf(g[0].z) + asf(g[1].z));
    float t0 = asf(g[2].x) + asf(g[3].x), t1 = asf(g[2].z) + asf(g[3].z);
    if (lane >= 32) { t0 = fmaxf(t0, 0.f); t1 = fmaxf(t1, 0.f); }
    qs[64 + lane] = pk2(t0, t1);
    __builtin_amdgcn_fence(__ATOMIC_RELEASE, "workgroup");
    if (lane == 0) __hip_atomic_store(&flg[0], 1u, __ATOMIC_RELAXED, __HIP_MEMORY_SCOPE_WORKGROUP);
    STAMP(2);
  }
#pragma unroll 1
  for (unsigned spins = 0; spins < (SPIN_CAP << 6); ++spins) {
    if (__hip_atomic_load(&flg[0], __ATOMIC_RELAXED, __HIP_MEMORY_SCOPE_WORKGROUP)) break;
    __builtin_amdgcn_s_sleep(1);
  }
  __builtin_amdgcn_fence(__ATOMIC_ACQUIRE, "workgroup");
  v4u qf[3];
#pragma unroll
  for (int j = 0; j < 3; ++j) qf[j] = *(const v4u*)(qs + 32 * j + 4 * lc);

  float sc[8];
#pragma unroll
  for (int g = 0; g < 8; ++g) {
    float a = 0.f;
#pragma unroll
    for (int j = 0; j < 3; ++j) {
      a = dot2(kv[g][j].x, qf[j].x, a);
      a = dot2(kv[g][j].y, qf[j].y, a);
      a = dot2(kv[g][j].z, qf[j].z, a);
      a = dot2(kv[g][j].w, qf[j].w, a);
    }
    a = sum8(a);
    sc[g] = rbf(rbf(a) * 0.07216878364870322f);
  }
  SCHED_FENCE();
  STAMP(3);
  unsigned pd[8];
  {
    float m = fmaxf(fmaxf(fmaxf(sc[0], sc[1]), fmaxf(sc[2], sc[3])), fmaxf(fmaxf(sc[4], sc[5]), fmaxf(sc[6], sc[7])));
    m = max_hi(m);
    float l = 0.f;
#pragma unroll
    for (int g = 0; g < 8; ++g) {
      const float p = __builtin_amdgcn_exp2f((sc[g] - m) * 1.4426950408889634f);
      l += p;
      pd[g] = pk2(p, p);
    }
    l = sum_hi(l);
    if (lane == 0) { mls[w] = m; mls[8 + w] = l; }
  }
  SCHED_FENCE();

  {
    f32x4 oacc[3] = {{0.f, 0.f, 0.f, 0.f}, {0.f, 0.f, 0.f, 0.f}, {0.f, 0.f, 0.f, 0.f}};
#pragma unroll
    for (int g = 0; g < 8; ++g) {
      const v4u bf = bfrag(pd[g], mask);
#pragma unroll
      for (int j = 0; j < 3; ++j) oacc[j] = mfma(bf, vv[g][j], oacc[j]);
      SCHED_FENCE();
    }
    float* const myred = red + w * RS + (rq >> 1) * 192 + 8 * (col & 7) + 4 * (rq & 1);
    if (dvalid) {
#pragma unroll
      for (int j = 0; j < 3; ++j) *(f32x4*)(myred + j * 64) = oacc[j];
    }
  }
  STAMP(10);
  const unsigned tag = (epoch + 1u) | 0x80000000u;
  CBAR();
  if (lane == 0) __hip_atomic_fetch_add(&flg[3], 1u, __ATOMIC_RELAXED, __HIP_MEMORY_SCOPE_WORKGROUP);
  CBAR();
  const int pw = col & 7, j8 = 2 * rq + (col >> 3);
  const float* const lsrc = red + pw * RS + 48 * (w & 3) + 6 * j8;
  const unsigned yz_off = col < 10 ? (unsigned)((s * 32 + h) * 640 + (4 * col + 2 * (rq & 1) + (rq >> 1)) * 16) : OOB;
  const unsigned vo3 = col < 10 ? (unsigned)(rq * 15360 + col * 16) : OOB;
  const unsigned oaddr = (unsigned)(size_t)((__attribute__((address_space(3))) unsigned*)osh) + (unsigned)(256 * (w & 3) + 32 * j8);
  unsigned mlo8[4], mhi8[4];
#pragma unroll
  for (int i = 0; i < 4; ++i) { mlo8[i] = col < 8 ? mask[i] : 0u; mhi8[i] = col < 8 ? 0u : mask[i]; }
  if (w >= 4) {
    unsigned po = pw < 3 ? WS_P1 + (unsigned)(wg * P1S + (24 * (w & 3) + 8 * pw + j8) * 16) : OOB;
    if (w == 4 && lane == 3) po = WS_ML + (unsigned)wg * 16u;
    const rsrc_t rsWo = mk(wo + (size_t)(192 * h + 48 * (w & 3)) * 640 + 80 * s, 47 * 1280 + 160);
#pragma unroll 1
    for (unsigned spins = 0; spins < (SPIN_CAP << 6); ++spins) {
      if (__hip_atomic_load(&flg[3], __ATOMIC_RELAXED, __HIP_MEMORY_SCOPE_WORKGROUP) >= 8u) break;
      __builtin_amdgcn_s_sleep(1);
    }
    CBAR();
    {
      f32x2 t[3];
#pragma unroll
      for (int i = 0; i < 3; ++i) t[i] = *(const f32x2*)(lsrc + 2 * i) + *(const f32x2*)(lsrc + 192 + 2 * i);
      const float m = mls[pw], l = mls[8 + pw];
      const float M = max8(m);
      const float f = __builtin_amdgcn_exp2f((m - M) * 1.4426950408889634f);
      const float L = sum8(f * l);
#pragma unroll
      for (int i = 0; i < 3; ++i) { t[i].x = sum8(f * t[i].x); t[i].y = sum8(f * t[i].y); }
      const float e0 = pw == 0 ? t[0].x : pw == 1 ? t[1].x : pw == 2 ? t[2].x : M;
      const float e1 = pw == 0 ? t[0].y : pw == 1 ? t[1].y : pw == 2 ? t[2].y : L;
      const v4u ga = {asu(e0), tag, asu(e1), tag};
      __builtin_amdgcn_raw_buffer_store_b128(ga, rsWs, po, 0, AUX_SC1);
    }
    CBAR();
    if (lane == 0) __hip_atomic_fetch_add(&flg[7], 1u, __ATOMIC_RELAXED, __HIP_MEMORY_SCOPE_WORKGROUP);
    CBAR();
    STAMP(12);
    SCHED_FENCE();
    if (WOSL) __builtin_amdgcn_s_sleep(WOSL);
    if (WE < 12) {
    v4u gv[12];
#pragma unroll
    for (int g = WE; g < 12; ++g) gv[g] = __builtin_amdgcn_raw_buffer_load_b128(rsWo, vo3, g * 1280, AUX_NT);
    SCHED_FENCE();
    STAMP(4);
    v4u oa, ob;
#pragma unroll 1
    for (unsigned spins = 0; spins < (SPIN_CAP << 6); ++spins) {
      asm volatile("ds_read_b128 %0, %2 offset:16\n ds_read_b128 %1, %2\n s_waitcnt lgkmcnt(0)"
                   : "=&v"(ob), "=&v"(oa) : "v"(oaddr) : "memory");
      if (__all(ob.z != 0u)) break;
      __builtin_amdgcn_s_sleep(1);
    }
    STAMP(13);
    const unsigned od[6] = {oa.x, oa.y, oa.z, oa.w, ob.x, ob.y};
    f32x4 ya = {0.f, 0.f, 0.f, 0.f}, yb = {0.f, 0.f, 0.f, 0.f};
#pragma unroll
    for (int i = WE; i < 12; ++i) {
      if (i & 1) yb = mfma(bfrag(od[i % 6], i < 6 ? mlo8 : mhi8), gv[i], yb);
      else ya = mfma(bfrag(od[i % 6], i < 6 ? mlo8 : mhi8), gv[i], ya);
    }
    Y_FINISH();
    }
  } else {
    if (w == 3) {
      const rsrc_t rsW2 = mk(w2 + (size_t)(48 * h) * 640 + 80 * s, 47 * 1280 + 160);
      v4u gz[12];
#pragma unroll
      for (int g = 0; g < 12; ++g) gz[g] = __builtin_amdgcn_raw_buffer_load_b128(rsW2, vo3, g * 1280, AUX_NT);
      const v2u* hb = (const v2u*)(qs + 96 + 6 * rq);
      const v2u h0 = hb[0], h1 = hb[1], h2 = hb[2];
      const unsigned hv[6] = {h0.x, h0.y, h1.x, h1.y, h2.x, h2.y};
      f32x4 za = {0.f, 0.f, 0.f, 0.f}, zb = {0.f, 0.f, 0.f, 0.f};
#pragma unroll
      for (int i = 0; i < 6; ++i) {
        za = mfma(bfrag(__builtin_amdgcn_perm(hv[i], hv[i], 0x01000100u), mask), gz[2 * i], za);
        zb = mfma(bfrag(__builtin_amdgcn_perm(hv[i], hv[i], 0x03020302u), mask), gz[2 * i + 1], zb);
      }
      za += zb;
      const v4u g = {asu(rq < 2 ? za[0] : za[2]), tag, asu(rq < 2 ? za[1] : za[3]), tag};
      __builtin_amdgcn_raw_buffer_store_b128(g, rsWs, yz_off + WS_Z, 0, AUX_SC1);
      asm volatile("s_waitcnt vmcnt(0)" ::: "memory");
      SCHED_FENCE();
      STAMP(14);
    }
    const int n0 = 80 * s + 4 * h + 2 * (lane >> 5);
    unsigned xin = 0u;
    if (w == 0) {
      xin = __builtin_amdgcn_raw_buffer_load_b32(rsX, (unsigned)n0 * 2u, 0, 0);
      asm volatile("" : "+v"(xin));
    }
    v4u gp[4 * NSET + 1], gv[12];
    if (WE) {
      const v4u srdWo = srd(wo + (size_t)(192 * h + 48 * w) * 640 + 80 * s, 47 * 1280 + 160);
#pragma unroll 1
      for (unsigned spins = 0; spins < (SPIN_CAP << 6); ++spins) {
        if (__hip_atomic_load(&flg[2], __ATOMIC_RELAXED, __HIP_MEMORY_SCOPE_WORKGROUP) >= GVCNT) break;
        __builtin_amdgcn_s_sleep(1);
      }
      CBAR();
      LOAD_WO();
      STAMP(4);
    }
    const unsigned go = pw == s ? OOB : WS_P1 + (unsigned)((h * 8 + pw) * P1S + (24 * w + j8) * 16);
    const unsigned g0 = DIRECT0 ? go : OOB;
    unsigned mo = pw == s ? OOB : WS_ML + (unsigned)(h * 8 + pw) * 16u;
    unsigned long long pm = ~(0x0101010101010101ull << s), pd = pm, sa;
    unsigned cnt = SPIN_CAP, sc, t0, t1, t2, t3;
#pragma unroll 1
    for (unsigned spins = 0; spins < (SPIN_CAP << 6); ++spins) {
      if (__hip_atomic_load(&flg[PG2 ? 7 : 3], __ATOMIC_RELAXED, __HIP_MEMORY_SCOPE_WORKGROUP) >= (PG2 ? PG2 : 8u)) break;
      __builtin_amdgcn_s_sleep(1);
    }
    CBAR();
    STAMP(15);
    P1_ISSUE0(gp);
    unsigned fv[8];
    {
      f32x2 t[3];
#pragma unroll
      for (int i = 0; i < 3; ++i) t[i] = *(const f32x2*)(lsrc + 2 * i) + *(const f32x2*)(lsrc + 192 + 2 * i);
      const float m = mls[pw], l = mls[8 + pw];
      const float M = max8(m);
      const float f = __builtin_amdgcn_exp2f((m - M) * 1.4426950408889634f);
      fv[6] = asu(M);
      fv[7] = asu(sum8(f * l));
#pragma unroll
      for (int i = 0; i < 3; ++i) { fv[2 * i] = asu(sum8(f * t[i].x)); fv[2 * i + 1] = asu(sum8(f * t[i].y)); }
    }
    P1_LOOP(gp);
    READY_WO();
    STAMP(5);
    {
      const float M = asf(fv[6]);
      const float Mg = max8(M);
      const float fr = __builtin_amdgcn_exp2f((M - Mg) * 1.4426950408889634f);
      const float rden = __builtin_amdgcn_rcpf(sum8(fr * asf(fv[7])));
      unsigned od[6];
#pragma unroll
      for (int i = 0; i < 6; ++i) {
        const float n = sum8(fr * asf(fv[i])) * rden;
        od[i] = pk2(n, n);
      }
      const v4u oa = {od[0], od[1], od[2], od[3]}, ob = {od[4], od[5], 1u, 1u};
      if (WE < 12 && pw == 0)
        asm volatile("ds_write_b128 %0, %1\n ds_write_b128 %0, %2 offset:16" :: "v"(oaddr), "v"(oa), "v"(ob) : "memory");
      if (WE) {
        f32x4 ya = {0.f, 0.f, 0.f, 0.f}, yb = {0.f, 0.f, 0.f, 0.f};
#pragma unroll
        for (int i = 0; i < WE; ++i) {
          if (i & 1) yb = mfma(bfrag(od[i % 6], i < 6 ? mlo8 : mhi8), gv[i], yb);
          else ya = mfma(bfrag(od[i % 6], i < 6 ? mlo8 : mhi8), gv[i], ya);
        }
        Y_FINISH();
      }
    }
    P1_DRAIN(gp);
    if (w == 0 && h < 20) {
      const unsigned gbase = (unsigned)((s * 32 + (lane & 31)) * 640 + (2 * h + (lane >> 5)) * 16);
#pragma unroll 1
      for (unsigned spins = 0; spins < (SPIN_CAP << 6); ++spins) {
        if (__hip_atomic_load(&flg[6], __ATOMIC_RELAXED, __HIP_MEMORY_SCOPE_WORKGROUP)) break;
        __builtin_amdgcn_s_sleep(1);
      }
      STAMP(13);
      unsigned y0 = 0u, y1 = 0u, go = gbase, cnt = SPIN_CAP, sc, t0;
      unsigned long long pend = ~0ull;
      v4u gq[5];
      P2_LOOP(gq);
      STAMP(8);
      v4u gzz = gq[4];
#pragma unroll 1
      for (unsigned spins = 0; spins < SPIN_CAP; ++spins) {
        if (__all((gzz.y == tag) & (gzz.w == tag))) break;
        CBAR();
        gzz = __builtin_amdgcn_raw_buffer_load_b128(rsWs, gbase, WS_Z, AUX_SC1);
        asm volatile("" : "+v"(gzz));
      }
      const float z0 = rbf(sum32(asf(gzz.x))), z1 = rbf(sum32(asf(gzz.z)));
      const float a0 = sum32(asf(y0)), a1 = sum32(asf(y1));
      if ((lane & 31) == 0) {
        const float r0 = rbf(rbf(bflo(xin) + rbf(a0)) + z0);
        const float r1 = rbf(rbf(bfhi(xin) + rbf(a1)) + z1);
        *(unsigned*)(out + n0) = pk2(r0, r1);
      }
      if (wg == 0 && lane == 0) __builtin_amdgcn_raw_buffer_store_b32(epoch + 1u, rsWs, 0, 0, AUX_SC1);
      P2_DRAIN(gq);
    }
  }
  STAMP(11);
  if (w == PW && true && stk_ >= st0_ && stk_ - st0_ < 20000ull) {
    const unsigned long long target = st0_ + 8000ull + 1ull * (stk_ - st0_);
#pragma unroll 1
    for (unsigned spins = 0; spins < (1u << 20); ++spins) {
      if (wall_clock64() >= target) break;
      __builtin_amdgcn_s_sleep(1);
    }
  }

}

}

extern "C" void kernel_launch(void* const* d_in, const int* in_sizes, int n_in, void* d_out, int out_size, void* d_ws,
                              size_t ws_size, hipStream_t stream) {
  (void)in_sizes; (void)n_in; (void)out_size;
  if (ws_size < WS_BYTES) return;
  fused_decode_kernel<<<dim3(256), dim3(512), 0, stream>>>(
      (const unsigned short*)d_in[0], (const unsigned short*)d_in[1], (const unsigned short*)d_in[2],
      (const unsigned short*)d_in[3], (const unsigned short*)d_in[4], (const unsigned short*)d_in[6],
      (const unsigned short*)d_in[7], (unsigned short*)d_out, (unsigned char*)d_ws);
}
```

```cpp
#include <hip/hip_runtime.h>
#include <hip/hip_bf16.h>

namespace {

typedef unsigned v4u __attribute__((ext_vector_type(4)));
typedef unsigned v2u __attribute__((ext_vector_type(2)));
typedef float f32x4 __attribute__((ext_vector_type(4)));
typedef float f32x2 __attribute__((ext_vector_type(2)));
typedef __bf16 bfx8 __attribute__((ext_vector_type(8)));
typedef __bf16 bfx2 __attribute__((ext_vector_type(2)));
typedef __amdgpu_buffer_rsrc_t rsrc_t;

constexpr int AUX_NT = 2;
constexpr int AUX_SC1 = 16;
constexpr int AUX_POLL = (int)(16u | 0x80000000u);
constexpr unsigned OOB = 0x80000000u;
constexpr unsigned SPIN_CAP = 1u << 16;

constexpr unsigned WS_P0 = 4096;
constexpr unsigned WS_P1 = WS_P0 + 256u * 256u * 8u;
constexpr unsigned WS_P2 = WS_P1 + 256u * 256u * 8u;
constexpr unsigned WS_BYTES = WS_P2 + 8u * 32u * 160u * 8u;

#define GVCNT 8

__device__ __forceinline__ rsrc_t mk(const void* p, unsigned bytes) {
  return __builtin_amdgcn_make_buffer_rsrc((void*)p, (short)0, (int)bytes, 0x00020000);
}
__device__ __forceinline__ float asf(unsigned u) { return __builtin_bit_cast(float, u); }
__device__ __forceinline__ unsigned asu(float f) { return __builtin_bit_cast(unsigned, f); }
__device__ __forceinline__ float rbf(float f) { return (float)(__bf16)f; }
__device__ __forceinline__ float bflo(unsigned u) { return __builtin_bit_cast(float, u << 16); }
__device__ __forceinline__ float bfhi(unsigned u) { return __builtin_bit_cast(float, u & 0xFFFF0000u); }
__device__ __forceinline__ unsigned pk2(float a, float b) {
  f32x2 v = {a, b};
  return __builtin_bit_cast(unsigned, __builtin_convertvector(v, bfx2));
}
__device__ __forceinline__ f32x4 mfma(v4u a, v4u b, f32x4 c) {
  return __builtin_amdgcn_mfma_f32_16x16x32_bf16(__builtin_bit_cast(bfx8, a), __builtin_bit_cast(bfx8, b), c, 0, 0, 0);
}
__device__ __forceinline__ float dot2(unsigned a, unsigned b, float c) {
  return __builtin_amdgcn_fdot2_f32_bf16(__builtin_bit_cast(bfx2, a), __builtin_bit_cast(bfx2, b), c, false);
}
template <int CTRL> __device__ __forceinline__ float dpp(float v) {
  return __builtin_bit_cast(float, __builtin_amdgcn_mov_dpp(__builtin_bit_cast(int, v), CTRL, 0xF, 0xF, true));
}
__device__ __forceinline__ float sum8(float v) {
  v += dpp<0xB1>(v);
  v += dpp<0x4E>(v);
  v += dpp<0x141>(v);
  return v;
}
__device__ __forceinline__ float sum_hi(float v) {
  v += dpp<0x128>(v);
  v += __shfl_xor(v, 16);
  v += __shfl_xor(v, 32);
  return v;
}
__device__ __forceinline__ float max_hi(float v) {
  v = fmaxf(v, dpp<0x128>(v));
  v = fmaxf(v, __shfl_xor(v, 16));
  v = fmaxf(v, __shfl_xor(v, 32));
  return v;
}
__device__ __forceinline__ v4u bfrag(unsigned dup, const unsigned (&mask)[4]) {
  v4u b;
  b.x = dup & mask[0];
  b.y = dup & mask[1];
  b.z = dup & mask[2];
  b.w = dup & mask[3];
  return b;
}
#define SCHED_FENCE() __builtin_amdgcn_sched_barrier(0)
constexpr unsigned WS_TS = 8u << 20;
#define STAMP(k) do { if ((k) == 0 && 0) { SCHED_FENCE(); const unsigned long long t_ = wall_clock64(); SCHED_FENCE(); if (blockIdx.x == 255 && threadIdx.x == 0) *(unsigned long long*)(ws + WS_TS) = t_; } } while (0)
__global__ void __launch_bounds__(64) ka_tiny(unsigned char* ws) {
  const unsigned long long t_ = wall_clock64();
  if (threadIdx.x == 0) *(unsigned long long*)(ws + WS_TS) = t_;
}
__global__ void __launch_bounds__(512, 2) ka_big(unsigned char* ws) {
  __shared__ float lds[4300];
  const unsigned long long t_ = wall_clock64();
  asm volatile("" ::: "v240");
  lds[threadIdx.x] = 1.f;
  __syncthreads();
  if (blockIdx.x == 255 && threadIdx.x == 0) *(unsigned long long*)(ws + WS_TS) = t_ + (lds[5] != 1.f);
}
__device__ __forceinline__ void waitts(const unsigned char* ws) {
  const unsigned long long t0 = *(const volatile unsigned long long*)(ws + WS_TS);
  const unsigned long long now = wall_clock64();
  if (now >= t0 && now - t0 < 15000ull) {
#pragma unroll 1
    for (unsigned spins = 0; spins < (1u << 20); ++spins) {
      if (wall_clock64() >= t0 + 15000ull) break;
      __builtin_amdgcn_s_sleep(1);
    }
  }
}
__global__ void __launch_bounds__(64) kc_tiny(unsigned char* ws) { waitts(ws); }
__global__ void __launch_bounds__(512, 2) kc_big(unsigned char* ws) {
  __shared__ float lds[4300];
  asm volatile("" ::: "v240");
  lds[threadIdx.x] = 1.f;
  __syncthreads();
  waitts(ws);
  if (lds[5] != 1.f) ws[WS_TS + 64] = 1;
}

__global__ void __launch_bounds__(512, 2)
fused_decode_kernel(const unsigned short* __restrict__ x, const unsigned short* __restrict__ kc,
                    const unsigned short* __restrict__ vc, const unsigned short* __restrict__ wq,
                    const unsigned short* __restrict__ wo, const unsigned short* __restrict__ w1,
                    const unsigned short* __restrict__ w2, unsigned short* __restrict__ out,
                    unsigned char* ws) {
  __shared__ __attribute__((aligned(16))) float red[16 * 192];
  __shared__ __attribute__((aligned(16))) float red3[4 * 256];
  __shared__ __attribute__((aligned(16))) unsigned qs[128];
  __shared__ __attribute__((aligned(16))) float mls[16];
  __shared__ unsigned flg[4];

  STAMP(0);
  const int tid = threadIdx.x;
  const int lane = tid & 63;
  const int w = __builtin_amdgcn_readfirstlane(tid >> 6);
  const int lg = lane >> 3, lc = lane & 7;
  const int col = lane & 15, rq = lane >> 4;
  const int b = blockIdx.x;
  const int h = 4 * (b & 7) + (b >> 6);
  const int s = (b >> 3) & 7;
  const int wg = h * 8 + s;

  const rsrc_t rsX = mk(x, 1280);
  const rsrc_t rsWs = mk(ws, WS_BYTES);
  const size_t kvoff = ((size_t)h * 32768 + 28672 + 512 * s + 64 * w) * 192;
  const rsrc_t rsK = mk(kc + kvoff, 64 * 384);
  const rsrc_t rsV = mk(vc + kvoff, 64 * 384);

  const unsigned epoch = __builtin_amdgcn_raw_buffer_load_b32(rsWs, 0, 0, AUX_SC1);
  if (tid < 4) flg[tid] = 0u;

  unsigned mask[4];
  {
    const unsigned mm = (lc & 1) ? 0xFFFF0000u : 0x0000FFFFu;
#pragma unroll
    for (int i = 0; i < 4; ++i) mask[i] = ((lc >> 1) == i) ? mm : 0u;
  }
  const bool dvalid = (rq >> 1) == (col >> 3);
  const unsigned vkv = (unsigned)(lg * 384 + lc * 16);
  const int t4 = tid - 256;
  const unsigned p1_off = (t4 >= 0 && t4 < 97) ? WS_P1 + (unsigned)(wg * 256 + 2 * t4) * 8u : OOB;
  const int r3 = 4 * (t4 >> 6) + (t4 & 3), c3 = (t4 >> 2) & 15;
  const unsigned p2_off =
      (t4 >= 0 && r3 < 10) ? WS_P2 + (unsigned)((s * 32 + h) * 160 + (8 * r3 + (c3 & 7)) * 2 + (c3 >> 3)) * 8u : OOB;
  v4u kv[8][3], vv[8][3];

  if (w < 2) {
    __builtin_amdgcn_s_setprio(3);
    const v4u xs0 = __builtin_amdgcn_raw_buffer_load_b128(rsX, 20 * lane, 0, 0);
    const unsigned xs1 = __builtin_amdgcn_raw_buffer_load_b32(rsX, 20 * lane + 16, 0, 0);
    const v4u xr0 = __builtin_amdgcn_raw_buffer_load_b128(rsX, 160 * s + 20 * lg, 0, 0);
    const unsigned xr1 = __builtin_amdgcn_raw_buffer_load_b32(rsX, 160 * s + 20 * lg + 16, 0, 0);
    const unsigned short* pa = wq + (size_t)(80 * s) * 6144 + 192 * h + 128 * w;
    const unsigned short* pb = w ? w1 + (size_t)(80 * s) * 1536 + 48 * h : pa + 64;
    const int sb = w ? 3072 : 12288;
    const rsrc_t rsA = mk(pa, 79 * 12288 + 128);
    const rsrc_t rsB = mk(pb, w ? 79 * 3072 + 96 : 79 * 12288 + 128);
    const unsigned voa = (unsigned)(lg * 122880 + lc * 16);
    const unsigned vob = (w && lc >= 6) ? OOB : (unsigned)(lg * 10 * sb + lc * 16);
    v4u wa[10], wb[10];
#pragma unroll
    for (int g = 0; g < 10; ++g) {
      wa[g] = __builtin_amdgcn_raw_buffer_load_b128(rsA, voa, g * 12288, AUX_NT);
      wb[g] = __builtin_amdgcn_raw_buffer_load_b128(rsB, vob, g * sb, AUX_NT);
      SCHED_FENCE();
    }
    const unsigned p0_off = dvalid ? WS_P0 + (unsigned)(wg * 256 + 128 * w + 64 * (rq >> 1) + 8 * (col & 7) + 4 * (rq & 1)) * 8u : OOB;
    SCHED_FENCE();
    __syncthreads();
    SCHED_FENCE();
    float mb, sd;
    {
      float e[10];
      e[0] = bflo(xs0.x); e[1] = bfhi(xs0.x); e[2] = bflo(xs0.y); e[3] = bfhi(xs0.y);
      e[4] = bflo(xs0.z); e[5] = bfhi(xs0.z); e[6] = bflo(xs0.w); e[7] = bfhi(xs0.w);
      e[8] = bflo(xs1);   e[9] = bfhi(xs1);
      float t = 0.f;
#pragma unroll
      for (int i = 0; i < 10; ++i) t += e[i];
      t = sum_hi(sum8(t));
      const float mean = t * (1.0f / 640.0f);
      float q2 = 0.f;
#pragma unroll
      for (int i = 0; i < 10; ++i) { const float d = e[i] - mean; q2 += d * d; }
      q2 = sum_hi(sum8(q2));
      const float var = rbf(q2 * (1.0f / 640.0f));
      mb = rbf(mean);
      sd = rbf(sqrtf(rbf(var + 1e-5f)));
    }
    const unsigned xr[5] = {xr0.x, xr0.y, xr0.z, xr0.w, xr1};
    const f32x4 zero = {0.f, 0.f, 0.f, 0.f};
    f32x4 a0 = zero, a1 = zero, b0 = zero, b1 = zero;
#pragma unroll
    for (int g = 0; g < 5; ++g) {
      const float n0 = rbf(bflo(xr[g]) - mb) / sd, n1 = rbf(bfhi(xr[g]) - mb) / sd;
      const v4u f0 = bfrag(pk2(n0, n0), mask), f1 = bfrag(pk2(n1, n1), mask);
      a0 = mfma(f0, wa[2 * g], a0);
      b0 = mfma(f0, wb[2 * g], b0);
      a1 = mfma(f1, wa[2 * g + 1], a1);
      b1 = mfma(f1, wb[2 * g + 1], b1);
    }
    a0 += a1;
    b0 += b1;
    const unsigned tag = (epoch + 1u) | 0x80000000u;
    {
      const bool p = rq >= 2;
      float r[4];
#pragma unroll
      for (int i = 0; i < 4; ++i) r[i] = (p ? b0[i] : a0[i]) + __shfl_xor(p ? a0[i] : b0[i], 40);
      const v4u g0 = {asu(r[0]), tag, asu(r[1]), tag}, g1 = {asu(r[2]), tag, asu(r[3]), tag};
      __builtin_amdgcn_raw_buffer_store_b128(g0, rsWs, p0_off, 0, AUX_SC1);
      __builtin_amdgcn_raw_buffer_store_b128(g1, rsWs, p0_off + 16, 0, AUX_SC1);
    }
    STAMP(1);
    __builtin_amdgcn_s_setprio(0);
    SCHED_FENCE();
#pragma unroll
    for (int g = 0; g < 2; ++g)
#pragma unroll
      for (int j = 0; j < 3; ++j) kv[g][j] = __builtin_amdgcn_raw_buffer_load_b128(rsK, vkv + j * 128, g * 3072, AUX_NT);
  } else {
#pragma unroll
    for (int g = 0; g < 2; ++g)
#pragma unroll
      for (int j = 0; j < 3; ++j) kv[g][j] = __builtin_amdgcn_raw_buffer_load_b128(rsK, vkv + j * 128, g * 3072, AUX_NT);
    SCHED_FENCE();
    __syncthreads();
  }
  SCHED_FENCE();
#pragma unroll
  for (int g = 2; g < 8; ++g) {
#pragma unroll
    for (int j = 0; j < 3; ++j) kv[g][j] = __builtin_amdgcn_raw_buffer_load_b128(rsK, vkv + j * 128, g * 3072, AUX_NT);
    SCHED_FENCE();
  }
#pragma unroll
  for (int g = 0; g < 8; ++g) {
#pragma unroll
    for (int j = 0; j < 3; ++j) vv[g][j] = __builtin_amdgcn_raw_buffer_load_b128(rsV, vkv + j * 128, g * 3072, AUX_NT);
    SCHED_FENCE();
  }
  STAMP(9);
  if (lane == 0) __hip_atomic_fetch_add(&flg[2], 1u, __ATOMIC_RELAXED, __HIP_MEMORY_SCOPE_WORKGROUP);
  if (w == 2 || w == 3) {
    const int t2 = tid - 128;
    const unsigned tag = (epoch + 1u) | 0x80000000u;
    const unsigned gbase = WS_P0 + (unsigned)(h * 8 * 256 + 2 * t2) * 8u;
    v4u g[8];
#pragma unroll 1
    for (unsigned spins = 0; spins < SPIN_CAP; ++spins) {
      asm volatile("" ::: "memory");
#pragma unroll
      for (int i = 0; i < 8; ++i) g[i] = __builtin_amdgcn_raw_buffer_load_b128(rsWs, gbase, i * 2048, AUX_SC1);
      bool ok = true;
#pragma unroll
      for (int i = 0; i < 8; ++i) ok &= (g[i].y == tag) & (g[i].w == tag);
      if (__all(ok)) break;
      __builtin_amdgcn_s_sleep(1);
    }
    float t0 = 0.f, t1 = 0.f;
#pragma unroll
    for (int i = 0; i < 8; ++i) {
      t0 += asf(g[i].x);
      t1 += asf(g[i].z);
    }
    if (t2 >= 96) { t0 = fmaxf(t0, 0.f); t1 = fmaxf(t1, 0.f); }
    qs[t2] = pk2(t0, t1);
    __builtin_amdgcn_fence(__ATOMIC_RELEASE, "workgroup");
    if (lane == 0) __hip_atomic_store(&flg[w - 2], 1u, __ATOMIC_RELAXED, __HIP_MEMORY_SCOPE_WORKGROUP);
    STAMP(2);
  }
#pragma unroll 1
  for (unsigned spins = 0; spins < (SPIN_CAP << 6); ++spins) {
    const unsigned f0 = __hip_atomic_load(&flg[0], __ATOMIC_RELAXED, __HIP_MEMORY_SCOPE_WORKGROUP);
    const unsigned f1 = __hip_atomic_load(&flg[1], __ATOMIC_RELAXED, __HIP_MEMORY_SCOPE_WORKGROUP);
    if (f0 & f1) break;
    __builtin_amdgcn_s_sleep(1);
  }
  __builtin_amdgcn_fence(__ATOMIC_ACQUIRE, "workgroup");
  v4u qf[3];
#pragma unroll
  for (int j = 0; j < 3; ++j) qf[j] = *(const v4u*)(qs + 32 * j + 4 * lc);

  float sc[8];
#pragma unroll
  for (int g = 0; g < 8; ++g) {
    float a = 0.f;
#pragma unroll
    for (int j = 0; j < 3; ++j) {
      a = dot2(kv[g][j].x, qf[j].x, a);
      a = dot2(kv[g][j].y, qf[j].y, a);
      a = dot2(kv[g][j].z, qf[j].z, a);
      a = dot2(kv[g][j].w, qf[j].w, a);
    }
    a = sum8(a);
    sc[g] = rbf(rbf(a) * 0.07216878364870322f);
  }
  SCHED_FENCE();
  STAMP(3);
  unsigned pd[8];
  {
    float m = fmaxf(fmaxf(fmaxf(sc[0], sc[1]), fmaxf(sc[2], sc[3])), fmaxf(fmaxf(sc[4], sc[5]), fmaxf(sc[6], sc[7])));
    m = max_hi(m);
    float l = 0.f;
#pragma unroll
    for (int g = 0; g < 8; ++g) {
      const float p = __builtin_amdgcn_exp2f((sc[g] - m) * 1.4426950408889634f);
      l += p;
      pd[g] = pk2(p, p);
    }
    l = sum_hi(l);
    if (lane == 0) { mls[w] = m; mls[8 + w] = l; }
  }
  SCHED_FENCE();

  {
    f32x4 oacc[3] = {{0.f, 0.f, 0.f, 0.f}, {0.f, 0.f, 0.f, 0.f}, {0.f, 0.f, 0.f, 0.f}};
#pragma unroll
    for (int g = 0; g < 8; ++g) {
      const v4u bf = bfrag(pd[g], mask);
#pragma unroll
      for (int j = 0; j < 3; ++j) oacc[j] = mfma(bf, vv[g][j], oacc[j]);
      SCHED_FENCE();
    }
    float* const myred = red + (w * 2 + (rq >> 1)) * 192 + 8 * (col & 7) + 4 * (rq & 1);
    if (dvalid) {
#pragma unroll
      for (int j = 0; j < 3; ++j) *(f32x4*)(myred + j * 64) = oacc[j];
    }
  }
  STAMP(10);
  const unsigned tag = (epoch + 1u) | 0x80000000u;
  if (w < 4) {
#pragma unroll 1
    for (unsigned spins = 0; spins < (SPIN_CAP << 6); ++spins) {
      if (__hip_atomic_load(&flg[2], __ATOMIC_RELAXED, __HIP_MEMORY_SCOPE_WORKGROUP) >= GVCNT) break;
      __builtin_amdgcn_s_sleep(1);
    }
    const rsrc_t rsWo = mk(wo + (size_t)(192 * h + 48 * w) * 640 + 80 * s, 47 * 1280 + 160);
    const rsrc_t rsW2 = mk(w2 + (size_t)(48 * h + 12 * w) * 640 + 80 * s, 11 * 1280 + 160);
    const unsigned vo3 = col < 10 ? (unsigned)(rq * 15360 + col * 16) : OOB;
    const unsigned vo4 = col < 10 ? (unsigned)(rq * 3840 + col * 16) : OOB;
    v4u gv[15];
#pragma unroll
    for (int g = 0; g < 3; ++g) gv[12 + g] = __builtin_amdgcn_raw_buffer_load_b128(rsW2, vo4, g * 1280, AUX_NT);
#pragma unroll
    for (int g = 0; g < 12; ++g) gv[g] = __builtin_amdgcn_raw_buffer_load_b128(rsWo, vo3, g * 1280, AUX_NT);
    SCHED_FENCE();
    STAMP(15);
    __syncthreads();
    STAMP(4);
    const unsigned gbase = WS_P1 + (unsigned)((h * 8 + (col & 7)) * 256 + 48 * w + 12 * rq + 6 * (col >> 3)) * 8u;
    const unsigned mlo = WS_P1 + (unsigned)((h * 8 + (col & 7)) * 256 + 192) * 8u;
    v4u g[3], gm;
#pragma unroll 1
    for (unsigned spins = 0; spins < SPIN_CAP; ++spins) {
      asm volatile("" ::: "memory");
#pragma unroll
      for (int i = 0; i < 3; ++i) g[i] = __builtin_amdgcn_raw_buffer_load_b128(rsWs, gbase + i * 16, 0, AUX_SC1);
      gm = __builtin_amdgcn_raw_buffer_load_b128(rsWs, mlo, 0, AUX_SC1);
      bool ok = (gm.y == tag) & (gm.w == tag);
#pragma unroll
      for (int i = 0; i < 3; ++i) ok &= (g[i].y == tag) & (g[i].w == tag);
      if (__all(ok)) break;
      __builtin_amdgcn_s_sleep(1);
    }
    STAMP(5);
    unsigned od[6];
    {
      const float M = asf(gm.x);
      float Mg = fmaxf(M, dpp<0xB1>(M));
      Mg = fmaxf(Mg, dpp<0x4E>(Mg));
      Mg = fmaxf(Mg, dpp<0x141>(Mg));
      const float f = __builtin_amdgcn_exp2f((M - Mg) * 1.4426950408889634f);
      const float den = sum8(f * asf(gm.z));
#pragma unroll
      for (int i = 0; i < 3; ++i) {
        const float n0 = sum8(f * asf(g[i].x)) / den, n1 = sum8(f * asf(g[i].z)) / den;
        od[2 * i] = pk2(n0, n0);
        od[2 * i + 1] = pk2(n1, n1);
      }
    }
    unsigned mlo8[4], mhi8[4];
#pragma unroll
    for (int i = 0; i < 4; ++i) { mlo8[i] = col < 8 ? mask[i] : 0u; mhi8[i] = col < 8 ? 0u : mask[i]; }
    const unsigned short* hb = (const unsigned short*)(qs + 96) + 12 * w + 3 * rq;
    f32x4 ya = {0.f, 0.f, 0.f, 0.f}, yb = {0.f, 0.f, 0.f, 0.f}, za = {0.f, 0.f, 0.f, 0.f};
#pragma unroll
    for (int i = 0; i < 3; ++i) za = mfma(gv[12 + i], bfrag((unsigned)hb[i] * 0x10001u, mhi8), za);
#pragma unroll
    for (int i = 0; i < 6; ++i) {
      ya = mfma(gv[i], bfrag(od[i], mlo8), ya);
      yb = mfma(gv[6 + i], bfrag(od[i], mhi8), yb);
    }
    f32x4 r;
#pragma unroll
    for (int i = 0; i < 4; ++i) r[i] = ya[i] + za[i] + dpp<0x128>(yb[i]);
    *(f32x4*)(red3 + w * 256 + lane * 4) = r;
    STAMP(6);
    __syncthreads();
    STAMP(7);
  } else {
    __syncthreads();
    if (w < 6) {
      float t0 = 0.f, t1 = 0.f;
      const f32x4 m0 = *(const f32x4*)mls, m1 = *(const f32x4*)(mls + 4);
      const f32x4 l0 = *(const f32x4*)(mls + 8), l1 = *(const f32x4*)(mls + 12);
      const float mw[8] = {m0.x, m0.y, m0.z, m0.w, m1.x, m1.y, m1.z, m1.w};
      const float lw[8] = {l0.x, l0.y, l0.z, l0.w, l1.x, l1.y, l1.z, l1.w};
      const float M = fmaxf(fmaxf(fmaxf(mw[0], mw[1]), fmaxf(mw[2], mw[3])), fmaxf(fmaxf(mw[4], mw[5]), fmaxf(mw[6], mw[7])));
      float L = 0.f;
      const int sl = t4 < 96 ? 2 * t4 : 0;
#pragma unroll
      for (int i = 0; i < 8; ++i) {
        const float f = __builtin_amdgcn_exp2f((mw[i] - M) * 1.4426950408889634f);
        const f32x2 va = *(const f32x2*)(red + (2 * i) * 192 + sl);
        const f32x2 vb = *(const f32x2*)(red + (2 * i + 1) * 192 + sl);
        t0 += f * (va.x + vb.x);
        t1 += f * (va.y + vb.y);
        L += f * lw[i];
      }
      if (t4 >= 96) { t0 = M; t1 = L; }
      const v4u g = {asu(t0), tag, asu(t1), tag};
      __builtin_amdgcn_raw_buffer_store_b128(g, rsWs, p1_off, 0, AUX_SC1);
      STAMP(12);
      STAMP(13);
    }
    __syncthreads();
    if (w < 7) {
      const float t = (red3[t4] + red3[256 + t4]) + (red3[512 + t4] + red3[768 + t4]);
      const v2u g = {asu(t), tag};
      __builtin_amdgcn_raw_buffer_store_b64(g, rsWs, p2_off, 0, AUX_SC1);
    } else if (h < 20) {
      const int n0 = 4 * h + 2 * (lane & 1);
      const unsigned xin = __builtin_amdgcn_raw_buffer_load_b32(rsX, (unsigned)(80 * s + n0) * 2u, 0, 0);
      const unsigned gbase = WS_P2 + (unsigned)((s * 32 + (lane >> 1)) * 160 + n0 * 2) * 8u;
      v4u g[2];
#pragma unroll 1
      for (unsigned spins = 0; spins < SPIN_CAP; ++spins) {
        asm volatile("" ::: "memory");
#pragma unroll
        for (int i = 0; i < 2; ++i) g[i] = __builtin_amdgcn_raw_buffer_load_b128(rsWs, gbase + i * 16, 0, AUX_SC1);
        const bool ok = (g[0].y == tag) & (g[0].w == tag) & (g[1].y == tag) & (g[1].w == tag);
        if (__all(ok)) break;
        __builtin_amdgcn_s_sleep(1);
      }
      STAMP(8);
      float v[4] = {asf(g[0].x), asf(g[0].z), asf(g[1].x), asf(g[1].z)};
#pragma unroll
      for (int i = 0; i < 4; ++i) {
        float t = v[i];
        t += dpp<0x4E>(t);
        t += __shfl_xor(t, 4);
        t = sum_hi(t);
        v[i] = t;
      }
      if (lane < 2) {
        const float r0 = rbf(rbf(bflo(xin) + rbf(v[0])) + rbf(v[1]));
        const float r1 = rbf(rbf(bfhi(xin) + rbf(v[2])) + rbf(v[3]));
        *(unsigned*)(out + 80 * s + n0) = pk2(r0, r1);
      }
      if (wg == 0 && lane == 0) __builtin_amdgcn_raw_buffer_store_b32(epoch + 1u, rsWs, 0, 0, AUX_SC1);
    }
  }

}

}

extern "C" void kernel_launch(void* const* d_in, const int* in_sizes, int n_in, void* d_out, int out_size, void* d_ws,
                              size_t ws_size, hipStream_t stream) {
  (void)in_sizes; (void)n_in; (void)out_size;
  if (ws_size < WS_BYTES) return;
  ka_big<<<dim3(256), dim3(512), 0, stream>>>((unsigned char*)d_ws);
  fused_decode_kernel<<<dim3(256), dim3(512), 0, stream>>>(
      (const unsigned short*)d_in[0], (const unsigned short*)d_in[1], (const unsigned short*)d_in[2],
      (const unsigned short*)d_in[3], (const unsigned short*)d_in[4], (const unsigned short*)d_in[6],
      (const unsigned short*)d_in[7], (unsigned short*)d_out, (unsigned char*)d_ws);
  kc_tiny<<<dim3(1), dim3(64), 0, stream>>>((unsigned char*)d_ws);
}
```

```cpp
#define MAP 2
#define RSH 4
#define GT 0
#define ZW 7
#define MLW 0
#define GPL 100000
#define FEW1 0
#define ES4 0
#define ES5 0
#define ES6 0
#define ES7 0
#define KPM 0xF0
#define GL 1550
#define GWE 1625
#define GZ 1650
#include <hip/hip_runtime.h>
#include <hip/hip_bf16.h>

namespace {

typedef unsigned v4u __attribute__((ext_vector_type(4)));
typedef unsigned v2u __attribute__((ext_vector_type(2)));
typedef float f32x4 __attribute__((ext_vector_type(4)));
typedef float f32x2 __attribute__((ext_vector_type(2)));
typedef __bf16 bfx8 __attribute__((ext_vector_type(8)));
typedef __bf16 bfx2 __attribute__((ext_vector_type(2)));
typedef __amdgpu_buffer_rsrc_t rsrc_t;

constexpr int AUX_NT = 2;
constexpr int AUX_SC1 = 16;
constexpr int AUX_POLL = (int)(16u | 0x80000000u);
constexpr unsigned OOB = 0x80000000u;
constexpr unsigned SPIN_CAP = 1u << 16;

constexpr unsigned WS_T = 2048;
constexpr unsigned WS_P0 = 4096;
constexpr unsigned WS_H = WS_P0 + 3072u * 8u;
constexpr unsigned P1S = 1664;
constexpr unsigned WS_P1 = WS_P0 + 122880u;
constexpr unsigned WS_ML = WS_P1 + 256u * P1S;
constexpr unsigned WS_Y = WS_ML + 4096u;
constexpr unsigned WS_Z = WS_Y + 8u * 32u * 640u;
constexpr unsigned WS_BYTES = WS_Z + 8u * 32u * 640u;
constexpr int RS = 392;

#define FEW 8
#define PS2 0
#define QNB 6
#define GLG 6
#define WEA 0
#define GP2 0
#define PSL 0
#define MLS 8
#define PRE 0
#define QAUX AUX_NT
#define NY 4
#define YFEW 64
#define YSL 8
#define STAG2 6
#define KPR 0
#define QE 1
#define NOPT ""
#define STAG1 12
#define PST 40
#define GA 100
#define PPR 3
constexpr int NLG = 8 - GLG, NL = 4 * NLG;

__device__ __forceinline__ rsrc_t mk(const void* p, unsigned bytes) {
  return __builtin_amdgcn_make_buffer_rsrc((void*)p, (short)0, (int)bytes, 0x00020000);
}
__device__ __forceinline__ void gate(unsigned t0, unsigned dt) {
#pragma unroll 1
  for (unsigned spins = 0; spins < 4096u; ++spins) {
    if ((int)((unsigned)wall_clock64() - t0) >= (int)dt) break;
    __builtin_amdgcn_s_sleep(2);
  }
}
__device__ __forceinline__ float asf(unsigned u) { return __builtin_bit_cast(float, u); }
__device__ __forceinline__ unsigned asu(float f) { return __builtin_bit_cast(unsigned, f); }
__device__ __forceinline__ float rbf(float f) { return (float)(__bf16)f; }
__device__ __forceinline__ float bflo(unsigned u) { return __builtin_bit_cast(float, u << 16); }
__device__ __forceinline__ float bfhi(unsigned u) { return __builtin_bit_cast(float, u & 0xFFFF0000u); }
__device__ __forceinline__ unsigned pk2(float a, float b) {
  f32x2 v = {a, b};
  return __builtin_bit_cast(unsigned, __builtin_convertvector(v, bfx2));
}
__device__ __forceinline__ f32x4 mfma(v4u a, v4u b, f32x4 c) {
  return __builtin_amdgcn_mfma_f32_16x16x32_bf16(__builtin_bit_cast(bfx8, a), __builtin_bit_cast(bfx8, b), c, 0, 0, 0);
}
typedef short s16x4 __attribute__((ext_vector_type(4)));
__device__ __forceinline__ f32x4 mfma4(v2u a, v2u b, f32x4 c) {
  return __builtin_amdgcn_mfma_f32_4x4x4bf16_1k(__builtin_bit_cast(s16x4, a), __builtin_bit_cast(s16x4, b), c, 0, 0, 0);
}
__device__ __forceinline__ float dot2(unsigned a, unsigned b, float c) {
  return __builtin_amdgcn_fdot2_f32_bf16(__builtin_bit_cast(bfx2, a), __builtin_bit_cast(bfx2, b), c, false);
}
template <int CTRL> __device__ __forceinline__ float dpp(float v) {
  return __builtin_bit_cast(float, __builtin_amdgcn_mov_dpp(__builtin_bit_cast(int, v), CTRL, 0xF, 0xF, true));
}
__device__ __forceinline__ float sum8(float v) {
  v += dpp<0xB1>(v);
  v += dpp<0x4E>(v);
  v += dpp<0x141>(v);
  return v;
}
__device__ __forceinline__ float max8(float v) {
  v = fmaxf(v, dpp<0xB1>(v));
  v = fmaxf(v, dpp<0x4E>(v));
  v = fmaxf(v, dpp<0x141>(v));
  return v;
}
__device__ __forceinline__ float sum_hi(float v) {
  v += dpp<0x128>(v);
  const auto r = __builtin_amdgcn_permlane16_swap(asu(v), asu(v), false, false);
  v = asf(r[0]) + asf(r[1]);
  const auto t = __builtin_amdgcn_permlane32_swap(asu(v), asu(v), false, false);
  return asf(t[0]) + asf(t[1]);
}
__device__ __forceinline__ float max_hi(float v) {
  v = fmaxf(v, dpp<0x128>(v));
  const auto r = __builtin_amdgcn_permlane16_swap(asu(v), asu(v), false, false);
  v = fmaxf(asf(r[0]), asf(r[1]));
  const auto t = __builtin_amdgcn_permlane32_swap(asu(v), asu(v), false, false);
  return fmaxf(asf(t[0]), asf(t[1]));
}
__device__ __forceinline__ v4u bfrag(unsigned dup, const unsigned (&mask)[4]) {
  v4u b;
  b.x = dup & mask[0];
  b.y = dup & mask[1];
  b.z = dup & mask[2];
  b.w = dup & mask[3];
  return b;
}
__device__ __forceinline__ float fold32(float a, float b) {
  const auto r = __builtin_amdgcn_permlane32_swap(asu(a), asu(b), false, false);
  return asf(r[0]) + asf(r[1]);
}
__device__ __forceinline__ float fold16(float a, float b) {
  const auto r = __builtin_amdgcn_permlane16_swap(asu(a), asu(b), false, false);
  return asf(r[0]) + asf(r[1]);
}
__device__ __forceinline__ float sum32(float v) {
  v = sum8(v);
  v += dpp<0x128>(v);
  const auto r = __builtin_amdgcn_permlane16_swap(asu(v), asu(v), false, false);
  return asf(r[0]) + asf(r[1]);
}
#define SCHED_FENCE() __builtin_amdgcn_sched_barrier(0)
#define CBAR() asm volatile("" ::: "memory")
#define STR_(x) #x
#define STR(x) STR_(x)
__device__ __forceinline__ v4u srd(const void* p, unsigned bytes) {
  const unsigned long long a = (unsigned long long)p;
  const v4u r = {(unsigned)a, (unsigned)(a >> 32) & 0xFFFFu, bytes, 0x00020000u};
  return r;
}
#define LWO0 asm volatile("s_nop 4\n buffer_load_dwordx4 v[160:163], %1, %2, %3 offen offset:0 nt" : "={v[160:163]}"(gv[0]) : "v"(vo3), "s"(srdWo), "s"(0) : "memory")
#define RWO0 asm volatile("" : "+{v[160:163]}"(gv[0]) :: "memory")
#define LWO1 asm volatile("s_nop 4\n buffer_load_dwordx4 v[164:167], %1, %2, %3 offen offset:1280 nt" : "={v[164:167]}"(gv[1]) : "v"(vo3), "s"(srdWo), "s"(0) : "memory")
#define RWO1 asm volatile("" : "+{v[164:167]}"(gv[1]) :: "memory")
#define LWO2 asm volatile("s_nop 4\n buffer_load_dwordx4 v[168:171], %1, %2, %3 offen offset:0 nt" : "={v[168:171]}"(gv[2]) : "v"(vo3), "s"(srdWo), "s"(20480) : "memory")
#define RWO2 asm volatile("" : "+{v[168:171]}"(gv[2]) :: "memory")
#define LWO3 asm volatile("s_nop 4\n buffer_load_dwordx4 v[172:175], %1, %2, %3 offen offset:1280 nt" : "={v[172:175]}"(gv[3]) : "v"(vo3), "s"(srdWo), "s"(20480) : "memory")
#define RWO3 asm volatile("" : "+{v[172:175]}"(gv[3]) :: "memory")
#define LWO4 asm volatile("s_nop 4\n buffer_load_dwordx4 v[176:179], %1, %2, %3 offen offset:0 nt" : "={v[176:179]}"(gv[4]) : "v"(vo3), "s"(srdWo), "s"(40960) : "memory")
#define RWO4 asm volatile("" : "+{v[176:179]}"(gv[4]) :: "memory")
#define LWO5 asm volatile("s_nop 4\n buffer_load_dwordx4 v[180:183], %1, %2, %3 offen offset:1280 nt" : "={v[180:183]}"(gv[5]) : "v"(vo3), "s"(srdWo), "s"(40960) : "memory")
#define RWO5 asm volatile("" : "+{v[180:183]}"(gv[5]) :: "memory")
#define LWO6 asm volatile("s_nop 4\n buffer_load_dwordx4 v[184:187], %1, %2, %3 offen offset:2560 nt" : "={v[184:187]}"(gv[6]) : "v"(vo3), "s"(srdWo), "s"(0) : "memory")
#define RWO6 asm volatile("" : "+{v[184:187]}"(gv[6]) :: "memory")
#define LWO7 asm volatile("s_nop 4\n buffer_load_dwordx4 v[188:191], %1, %2, %3 offen offset:3840 nt" : "={v[188:191]}"(gv[7]) : "v"(vo3), "s"(srdWo), "s"(0) : "memory")
#define RWO7 asm volatile("" : "+{v[188:191]}"(gv[7]) :: "memory")
#define LWO8 asm volatile("s_nop 4\n buffer_load_dwordx4 v[192:195], %1, %2, %3 offen offset:2560 nt" : "={v[192:195]}"(gv[8]) : "v"(vo3), "s"(srdWo), "s"(20480) : "memory")
#define RWO8 asm volatile("" : "+{v[192:195]}"(gv[8]) :: "memory")
#define LWO9 asm volatile("s_nop 4\n buffer_load_dwordx4 v[196:199], %1, %2, %3 offen offset:3840 nt" : "={v[196:199]}"(gv[9]) : "v"(vo3), "s"(srdWo), "s"(20480) : "memory")
#define RWO9 asm volatile("" : "+{v[196:199]}"(gv[9]) :: "memory")
#define LWO10 asm volatile("s_nop 4\n buffer_load_dwordx4 v[200:203], %1, %2, %3 offen offset:2560 nt" : "={v[200:203]}"(gv[10]) : "v"(vo3), "s"(srdWo), "s"(40960) : "memory")
#define RWO10 asm volatile("" : "+{v[200:203]}"(gv[10]) :: "memory")
#define LWO11 asm volatile("s_nop 4\n buffer_load_dwordx4 v[204:207], %1, %2, %3 offen offset:3840 nt" : "={v[204:207]}"(gv[11]) : "v"(vo3), "s"(srdWo), "s"(40960) : "memory")
#define RWO11 asm volatile("" : "+{v[204:207]}"(gv[11]) :: "memory")
#define LOAD_WO(A, B) do { if (A <= 0 && 0 < B) LWO0; if (A <= 1 && 1 < B) LWO1; if (A <= 2 && 2 < B) LWO2; if (A <= 3 && 3 < B) LWO3; if (A <= 4 && 4 < B) LWO4; if (A <= 5 && 5 < B) LWO5; if (A <= 6 && 6 < B) LWO6; if (A <= 7 && 7 < B) LWO7; if (A <= 8 && 8 < B) LWO8; if (A <= 9 && 9 < B) LWO9; if (A <= 10 && 10 < B) LWO10; if (A <= 11 && 11 < B) LWO11; } while (0)
#define READY_WO() do { RWO0; RWO1; RWO2; RWO3; RWO4; RWO5; RWO6; RWO7; RWO8; RWO9; RWO10; RWO11; } while (0)
#define P1_LA(GO, GM, RS) "buffer_load_dwordx4 v[220:223], " GM ", " RS ", 0 offen sc1\n" "buffer_load_dwordx4 v[208:211], " GO ", " RS ", 0 offen offset:0 sc1\n" "buffer_load_dwordx4 v[212:215], " GO ", " RS ", 0 offen offset:128 sc1\n" "buffer_load_dwordx4 v[216:219], " GO ", " RS ", 0 offen offset:256 sc1\n"
#define P1_LB(GO, GM, RS) "buffer_load_dwordx4 v[240:243], " GM ", " RS ", 0 offen sc1\n" "buffer_load_dwordx4 v[228:231], " GO ", " RS ", 0 offen offset:0 sc1\n" "buffer_load_dwordx4 v[232:235], " GO ", " RS ", 0 offen offset:128 sc1\n" "buffer_load_dwordx4 v[236:239], " GO ", " RS ", 0 offen offset:256 sc1\n"
#define P1_CA \
      "v_bitop3_b32 %[t1], v209, v211, %[tag] bitop3:0x7e\n" \
      "v_bitop3_b32 %[t2], v213, v215, %[tag] bitop3:0x7e\n" \
      "v_bitop3_b32 %[t3], v217, v219, %[tag] bitop3:0x7e\n" \
      "v_or3_b32 %[t1], %[t1], %[t2], %[t3]\n" \
      "v_bitop3_b32 %[t2], v221, v223, %[tag] bitop3:0x7e\n" \
      "v_or_b32 %[t1], %[t1], %[t2]\n" \
      "v_cmp_eq_u32 vcc, 0, %[t1]\n" \
      "s_nop 1\n" \
      "v_cndmask_b32 %[go], %[go], %[oob], vcc\n" \
      "v_cndmask_b32 %[gm], %[gm], %[oob], vcc\n" \
      "v_cndmask_b32 %[f6], %[f6], v220, vcc\n" \
      "v_cndmask_b32 %[f7], %[f7], v222, vcc\n" \
      "v_cndmask_b32 %[f0], %[f0], v208, vcc\n" \
      "v_cndmask_b32 %[f1], %[f1], v210, vcc\n" \
      "v_cndmask_b32 %[f2], %[f2], v212, vcc\n" \
      "v_cndmask_b32 %[f3], %[f3], v214, vcc\n" \
      "v_cndmask_b32 %[f4], %[f4], v216, vcc\n" \
      "v_cndmask_b32 %[f5], %[f5], v218, vcc\n" \
      "s_andn2_b64 %[pd], %[pd], vcc\n" \
      "s_cbranch_scc0 .Lp1x%=\n"
#define P1_CB \
      "v_bitop3_b32 %[t1], v229, v231, %[tag] bitop3:0x7e\n" \
      "v_bitop3_b32 %[t2], v233, v235, %[tag] bitop3:0x7e\n" \
      "v_bitop3_b32 %[t3], v237, v239, %[tag] bitop3:0x7e\n" \
      "v_or3_b32 %[t1], %[t1], %[t2], %[t3]\n" \
      "v_bitop3_b32 %[t2], v241, v243, %[tag] bitop3:0x7e\n" \
      "v_or_b32 %[t1], %[t1], %[t2]\n" \
      "v_cmp_eq_u32 vcc, 0, %[t1]\n" \
      "s_nop 1\n" \
      "v_cndmask_b32 %[go], %[go], %[oob], vcc\n" \
      "v_cndmask_b32 %[gm], %[gm], %[oob], vcc\n" \
      "v_cndmask_b32 %[f6], %[f6], v240, vcc\n" \
      "v_cndmask_b32 %[f7], %[f7], v242, vcc\n" \
      "v_cndmask_b32 %[f0], %[f0], v228, vcc\n" \
      "v_cndmask_b32 %[f1], %[f1], v230, vcc\n" \
      "v_cndmask_b32 %[f2], %[f2], v232, vcc\n" \
      "v_cndmask_b32 %[f3], %[f3], v234, vcc\n" \
      "v_cndmask_b32 %[f4], %[f4], v236, vcc\n" \
      "v_cndmask_b32 %[f5], %[f5], v238, vcc\n" \
      "s_andn2_b64 %[pd], %[pd], vcc\n" \
      "s_cbranch_scc0 .Lp1x%=\n"
#define P1_ISSUE(G) asm volatile("s_nop 4\n" P1_LA("%4", "%5", "%6") \
  : "={v[208:211]}"(G[0]), "={v[212:215]}"(G[1]), "={v[216:219]}"(G[2]), "={v[220:223]}"(G[3]) : "v"(go), "v"(gm), "s"(srdWs) : "memory")
#define P1_LOOP(G, H) asm volatile( \
      ".Lp1l%=:\n" \
      "s_waitcnt vmcnt(0)\n" \
      P1_CA \
      P1_LA("%[go]", "%[gm]", "%[rs]") \
      "s_sub_u32 %[cnt], %[cnt], 1\n" "s_cbranch_scc1 .Lp1x%=\n" \
      "s_bcnt1_i32_b64 %[sc], %[pd]\n" "s_cmp_gt_u32 %[sc], " STR(FEW1) "\n" "s_cbranch_scc1 .Lp1l%=\n" \
      "s_sleep " STR(STAG1) "\n" \
      ".Lp1c%=:\n" \
      P1_LB("%[go]", "%[gm]", "%[rs]") \
      "s_waitcnt vmcnt(4)\n" \
      P1_CA \
      P1_LA("%[go]", "%[gm]", "%[rs]") \
      "s_waitcnt vmcnt(4)\n" \
      P1_CB \
      "s_sub_u32 %[cnt], %[cnt], 1\n" "s_cbranch_scc0 .Lp1c%=\n" ".Lp1x%=:\n" "s_nop 1\n" \
      : "+{v[208:211]}"(G[0]), "+{v[212:215]}"(G[1]), "+{v[216:219]}"(G[2]), "+{v[220:223]}"(G[3]), \
        "=&{v[228:231]}"(H[0]), "=&{v[232:235]}"(H[1]), "=&{v[236:239]}"(H[2]), "=&{v[240:243]}"(H[3]), \
        [f0] "+v"(fv[0]), [f1] "+v"(fv[1]), [f2] "+v"(fv[2]), [f3] "+v"(fv[3]), [f4] "+v"(fv[4]), [f5] "+v"(fv[5]), \
        [f6] "+v"(fv[6]), [f7] "+v"(fv[7]), [sc] "=&s"(sc), \
        [go] "+v"(go), [gm] "+v"(gm), [pd] "+s"(pd), [cnt] "+s"(cnt), [t1] "=&v"(t1), [t2] "=&v"(t2), [t3] "=&v"(t3) \
      : [tag] "v"(tag), [oob] "v"(OOB), [rs] "s"(srdWs) : "vcc", "scc", "memory")
#define P1_DRAIN(G, H) asm volatile("s_waitcnt vmcnt(0)" :: "{v[208:211]}"(G[0]), "{v[212:215]}"(G[1]), "{v[216:219]}"(G[2]), "{v[220:223]}"(G[3]), \
        "{v[228:231]}"(H[0]), "{v[232:235]}"(H[1]), "{v[236:239]}"(H[2]), "{v[240:243]}"(H[3]) : "memory")
#define P2_LOOP(G) asm volatile( \
      "s_nop 4\n" "buffer_load_dwordx4 v[224:227], %[go], %[rs], %[sz] offen sc1\n" \
      "buffer_load_dwordx4 v[208:211], %[go], %[rs], %[so] offen sc1\n" "s_sleep " STR(STAG2) "\n" \
      "buffer_load_dwordx4 v[212:215], %[go], %[rs], %[so] offen sc1\n" "s_sleep " STR(STAG2) "\n" \
      "buffer_load_dwordx4 v[216:219], %[go], %[rs], %[so] offen sc1\n" "s_sleep " STR(STAG2) "\n" \
      "buffer_load_dwordx4 v[220:223], %[go], %[rs], %[so] offen sc1\n" \
      ".Lp2l%=:\n" \
      "s_waitcnt vmcnt(3)\n" \
      "v_bitop3_b32 %[t0], v209, v211, %[tag] bitop3:0x7e\n" \
      "v_cmp_eq_u32 vcc, 0, %[t0]\n" \
      "s_nop 1\n" \
      "v_cndmask_b32 %[go], %[go], %[oob], vcc\n" \
      "v_cndmask_b32 %[y0], %[y0], v208, vcc\n" \
      "v_cndmask_b32 %[y1], %[y1], v210, vcc\n" \
      "s_andn2_b64 %[pend], %[pend], vcc\n" \
      "s_cbranch_scc0 .Lp2x%=\n" \
      "s_bcnt1_i32_b64 %[sc], %[pend]\n" \
      "s_cmp_le_u32 %[sc], " STR(YFEW) "\n" \
      "s_cbranch_scc1 .Lp2s0%=\n" \
      "s_sleep " STR(YSL) "\n" \
      ".Lp2s0%=:\n" \
      "buffer_load_dwordx4 v[208:211], %[go], %[rs], %[so] offen sc1\n" \
      "s_waitcnt vmcnt(3)\n" \
      "v_bitop3_b32 %[t0], v213, v215, %[tag] bitop3:0x7e\n" \
      "v_cmp_eq_u32 vcc, 0, %[t0]\n" \
      "s_nop 1\n" \
      "v_cndmask_b32 %[go], %[go], %[oob], vcc\n" \
      "v_cndmask_b32 %[y0], %[y0], v212, vcc\n" \
      "v_cndmask_b32 %[y1], %[y1], v214, vcc\n" \
      "s_andn2_b64 %[pend], %[pend], vcc\n" \
      "s_cbranch_scc0 .Lp2x%=\n" \
      "s_bcnt1_i32_b64 %[sc], %[pend]\n" \
      "s_cmp_le_u32 %[sc], " STR(YFEW) "\n" \
      "s_cbranch_scc1 .Lp2s1%=\n" \
      "s_sleep " STR(YSL) "\n" \
      ".Lp2s1%=:\n" \
      "buffer_load_dwordx4 v[212:215], %[go], %[rs], %[so] offen sc1\n" \
      "s_waitcnt vmcnt(3)\n" \
      "v_bitop3_b32 %[t0], v217, v219, %[tag] bitop3:0x7e\n" \
      "v_cmp_eq_u32 vcc, 0, %[t0]\n" \
      "s_nop 1\n" \
      "v_cndmask_b32 %[go], %[go], %[oob], vcc\n" \
      "v_cndmask_b32 %[y0], %[y0], v216, vcc\n" \
      "v_cndmask_b32 %[y1], %[y1], v218, vcc\n" \
      "s_andn2_b64 %[pend], %[pend], vcc\n" \
      "s_cbranch_scc0 .Lp2x%=\n" \
      "s_bcnt1_i32_b64 %[sc], %[pend]\n" \
      "s_cmp_le_u32 %[sc], " STR(YFEW) "\n" \
      "s_cbranch_scc1 .Lp2s2%=\n" \
      "s_sleep " STR(YSL) "\n" \
      ".Lp2s2%=:\n" \
      "buffer_load_dwordx4 v[216:219], %[go], %[rs], %[so] offen sc1\n" \
      "s_waitcnt vmcnt(3)\n" \
      "v_bitop3_b32 %[t0], v221, v223, %[tag] bitop3:0x7e\n" \
      "v_cmp_eq_u32 vcc, 0, %[t0]\n" \
      "s_nop 1\n" \
      "v_cndmask_b32 %[go], %[go], %[oob], vcc\n" \
      "v_cndmask_b32 %[y0], %[y0], v220, vcc\n" \
      "v_cndmask_b32 %[y1], %[y1], v222, vcc\n" \
      "s_andn2_b64 %[pend], %[pend], vcc\n" \
      "s_cbranch_scc0 .Lp2x%=\n" \
      "s_bcnt1_i32_b64 %[sc], %[pend]\n" \
      "s_cmp_le_u32 %[sc], " STR(YFEW) "\n" \
      "s_cbranch_scc1 .Lp2s3%=\n" \
      "s_sleep " STR(YSL) "\n" \
      ".Lp2s3%=:\n" \
      "buffer_load_dwordx4 v[220:223], %[go], %[rs], %[so] offen sc1\n" \
      "s_sub_u32 %[cnt], %[cnt], 1\n" "s_cbranch_scc0 .Lp2l%=\n" ".Lp2x%=:\n" "s_nop 1\n" \
      : "+{v[208:211]}"(G[0]), "+{v[212:215]}"(G[1]), "+{v[216:219]}"(G[2]), "+{v[220:223]}"(G[3]), "=&{v[224:227]}"(G[4]), \
        [y0] "+v"(y0), [y1] "+v"(y1), [go] "+v"(go), [pend] "+s"(pend), [cnt] "+s"(cnt), [sc] "=&s"(sc), [t0] "=&v"(t0) \
      : [tag] "v"(tag), [oob] "v"(OOB), [rs] "s"(srdWs), [so] "s"(WS_Y), [sz] "s"(WS_Z) : "vcc", "scc", "memory")
#define P2_DRAIN(G) asm volatile("s_waitcnt vmcnt(0)" :: "{v[208:211]}"(G[0]), "{v[212:215]}"(G[1]), "{v[216:219]}"(G[2]), "{v[220:223]}"(G[3]) : "memory")
#define STAMP(k)

#define ALD(dst, vo, rs, so, imm) asm volatile(NOPT "buffer_load_dwordx4 %0, %1, %2, %3 offen offset:" STR(imm) " nt ; KVL" \
    : "=v"(dst) : "v"(vo), "s"(rs), "s"(so) : "memory")
#define ALD3(dst, rs, g) do { ALD(dst[0], vkv, rs, (g) * 3072, 0); ALD(dst[1], vkv, rs, (g) * 3072, 128); ALD(dst[2], vkv, rs, (g) * 3072, 256); } while (0)
#define ALDV(dst, rs, g) asm volatile(NOPT "buffer_load_dwordx4 %0, %3, %4, %5 offen nt ; KVL\n" NOPT "buffer_load_dwordx4 %1, %3, %4, %5 offen offset:128 nt ; KVL\n" \
    NOPT "buffer_load_dwordx4 %2, %3, %4, %5 offen offset:256 nt ; KVL" :: "v"(dst[0]), "v"(dst[1]), "v"(dst[2]), "v"(vkv), "s"(rs), "s"((g) * 3072) : "memory")
#define ALDX(dst, vo, rs, so) asm volatile(NOPT "buffer_load_dwordx4 %0, %1, %2, %3 offen nt ; KVL" \
    :: "v"(dst), "v"(vo), "s"(rs), "s"(so) : "memory")
#define AW1(n, d) asm volatile("s_waitcnt vmcnt(%1) ; KVW" : "+v"(d) : "n"(n) : "memory")
#define AW2(n, a, b) asm volatile("s_waitcnt vmcnt(%2) ; KVW" : "+v"(a), "+v"(b) : "n"(n) : "memory")
#define WAITF(i, n, sl) do {                                                                              \
    _Pragma("unroll 1")                                                                                   \
    for (unsigned spins = 0; spins < (SPIN_CAP << 6); ++spins) {                                          \
      if (__hip_atomic_load(&flg[i], __ATOMIC_RELAXED, __HIP_MEMORY_SCOPE_WORKGROUP) >= (n)) break;       \
      __builtin_amdgcn_s_sleep(sl);                                                                       \
    }                                                                                                     \
    CBAR();                                                                                               \
  } while (0)
#define COUNTF(i) do {                                                                                    \
    CBAR();                                                                                               \
    if (lane == 0) __hip_atomic_fetch_add(&flg[i], 1u, __ATOMIC_RELAXED, __HIP_MEMORY_SCOPE_WORKGROUP);   \
    CBAR();                                                                                               \
  } while (0)
#define PV(G0, G1, NEW) do {                                                                              \
    _Pragma("unroll")                                                                                     \
    for (int g = G0; g < G1; ++g) {                                                                       \
      v4u bf = bfrag(pd[g], mask);                                                                        \
      AW1((NEW) + 3 * (G1 - 1 - g), bf);                                                                  \
      _Pragma("unroll")                                                                                   \
      for (int j = 0; j < 3; ++j) oacc[j] = mfma(bf, vv[g][j], oacc[j]);                                  \
      SCHED_FENCE();                                                                                      \
    }                                                                                                     \
  } while (0)
#define PV_OUT() do {                                                                                     \
    if (dvalid) {                                                                                         \
      _Pragma("unroll")                                                                                   \
      for (int j = 0; j < 3; ++j) *(f32x4*)(red + w * RS + dpos + j * 64) = oacc[j];                      \
    }                                                                                                     \
    STAMP(10);                                                                                            \
    COUNTF(3);                                                                                            \
  } while (0)

__global__ void __launch_bounds__(512, 2)
fused_decode_kernel(const unsigned short* __restrict__ x, const unsigned short* __restrict__ kc,
                    const unsigned short* __restrict__ vc, const unsigned short* __restrict__ wq,
                    const unsigned short* __restrict__ wo, const unsigned short* __restrict__ w1,
                    const unsigned short* __restrict__ w2, unsigned short* __restrict__ out,
                    unsigned char* ws) {
  __shared__ __attribute__((aligned(16))) float red[8 * RS];
  __shared__ __attribute__((aligned(16))) float red3[8 * 128];
  __shared__ __attribute__((aligned(16))) unsigned qs[128];
  __shared__ __attribute__((aligned(16))) float mls[16];
  __shared__ __attribute__((aligned(16))) float lsc[8 * NL];
  __shared__ __attribute__((aligned(16))) float own[192];
  __shared__ float fac[8];
  __shared__ unsigned misc[2];
  __shared__ unsigned flg[12];

  STAMP(0);
  const unsigned tz = (unsigned)wall_clock64();
  const int tid = threadIdx.x;
  const int lane = tid & 63;
  const int w = __builtin_amdgcn_readfirstlane(tid >> 6);
  const int lg = lane >> 3, lc = lane & 7;
  const int col = lane & 15, rq = lane >> 4;
  const int b = blockIdx.x;
  const int h = MAP == 0 ? 4 * (b & 7) + (b >> 6) : MAP == 1 ? b >> 3 : MAP == 2 ? 8 * ((b >> 1) & 3) + (b >> 5) : 16 * ((b >> 2) & 1) + (b >> 4);
  const int s = MAP == 0 ? (b >> 3) & 7 : MAP == 1 ? b & 7 : MAP == 2 ? 4 * (b & 1) + ((b >> 3) & 3) : 2 * (b & 3) + ((b >> 3) & 1);
  const int sr = (s + (RSH < 8 ? RSH : 1 + h % 7)) & 7;
  const int wg = h * 8 + s;

  const rsrc_t rsX = mk(x, 1280);
  const rsrc_t rsWs = mk(ws, WS_BYTES);
  const v4u srdWs = srd(ws, WS_BYTES);
  const size_t kvoff = ((size_t)h * 32768 + 28672 + 512 * s + 64 * w) * 192;
  const v4u rsK = srd(kc + kvoff, 64 * 384);
  const v4u rsV = srd(vc + kvoff, 64 * 384);

  unsigned epoch;
  asm volatile("s_nop 4\n buffer_load_dword %0, off, %1, 0 sc1 ; KVL" : "=v"(epoch) : "s"(srdWs) : "memory");
  if (tid < 12) flg[tid] = 0u;

  unsigned mask[4];
  {
    const unsigned mm = (lc & 1) ? 0xFFFF0000u : 0x0000FFFFu;
#pragma unroll
    for (int i = 0; i < 4; ++i) mask[i] = ((lc >> 1) == i) ? mm : 0u;
  }
  const bool dvalid = (rq >> 1) == (col >> 3);
  const unsigned vkv = (unsigned)(lg * 384 + lc * 16);
  v4u kv[8][3], vv[GLG][3];

  const int jl = b >> 3, jp = (b >> 1) & 3;
  if (w < 2 && jl < 30) {
    __builtin_amdgcn_s_setprio(3);
    const int r = b & 1;
    const int n = r ? 10 - QNB : QNB;
    const int base = r ? 64 * QNB : 0;
    const v4u xs0 = __builtin_amdgcn_raw_buffer_load_b128(rsX, 20 * lane, 0, 0);
    const unsigned xs1 = __builtin_amdgcn_raw_buffer_load_b32(rsX, 20 * lane + 16, 0, 0);
    const unsigned xo = (unsigned)(2 * base + 4 * n * (4 * lg + (lane & 3)));
    const v4u xr0 = __builtin_amdgcn_raw_buffer_load_b128(rsX, xo, 0, 0);
    const v4u xr1 = __builtin_amdgcn_raw_buffer_load_b128(rsX, xo + 16, 0, 0);
    const unsigned short* pa = jl < 24 ? wq + (size_t)base * 6144 + 64 * (24 * jp + jl)
                                       : w1 + (size_t)base * 1536 + 64 * (6 * jp + jl - 24);
    const unsigned st = jl < 24 ? 12288u : 3072u;
    const rsrc_t rsA = mk(pa, (unsigned)(64 * n - 1) * st + 128u);
    const unsigned voa = (unsigned)(8 * n * lg + w) * st + (unsigned)lc * 16u;
    v4u wa[4 * QNB];
#pragma unroll
    for (int k = 0; k < 4 * (10 - QNB); ++k) {
      wa[k] = __builtin_amdgcn_raw_buffer_load_b128(rsA, voa, (n * (k & 3) + (k >> 2)) * 2 * st, QAUX);
      SCHED_FENCE();
    }
    if (!r) {
#pragma unroll
      for (int k = 4 * (10 - QNB); k < 4 * QNB; ++k) {
        wa[k] = __builtin_amdgcn_raw_buffer_load_b128(rsA, voa, (QNB * (k & 3) + (k >> 2)) * 2 * st, QAUX);
        SCHED_FENCE();
      }
    }
    const unsigned p0_off = (lane & 8) ? OOB
        : WS_P0 + (unsigned)(r * 61440) + (unsigned)((jl < 24 ? 32 * (24 * jp + jl) : 3072 + 32 * (6 * jp + jl - 24)) +
          4 * lc + 2 * (lane >> 5) + ((lane >> 4) & 1)) * 16u;
    SCHED_FENCE();
    __syncthreads();
    SCHED_FENCE();
    float mb, sd;
    {
      float e[10];
      e[0] = bflo(xs0.x); e[1] = bfhi(xs0.x); e[2] = bflo(xs0.y); e[3] = bfhi(xs0.y);
      e[4] = bflo(xs0.z); e[5] = bfhi(xs0.z); e[6] = bflo(xs0.w); e[7] = bfhi(xs0.w);
      e[8] = bflo(xs1);   e[9] = bfhi(xs1);
      float t = 0.f;
#pragma unroll
      for (int i = 0; i < 10; ++i) t += e[i];
      t = sum_hi(sum8(t));
      const float mean = t * (1.0f / 640.0f);
      float q2 = 0.f;
#pragma unroll
      for (int i = 0; i < 10; ++i) { const float d = e[i] - mean; q2 += d * d; }
      q2 = sum_hi(sum8(q2));
      const float var = rbf(q2 * (1.0f / 640.0f));
      mb = rbf(mean);
      sd = rbf(sqrtf(rbf(var + 1e-5f)));
    }
    const unsigned sh = 16u - 16u * w;
    const unsigned xr[8] = {xr0.x, xr0.y, xr0.z, xr0.w, xr1.x, xr1.y, xr1.z, xr1.w};
    const unsigned mm = (lane & 1) ? 0xFFFF0000u : 0x0000FFFFu;
    const unsigned m0 = (lane & 2) ? 0u : mm, m1 = (lane & 2) ? mm : 0u;
    const f32x4 zero = {0.f, 0.f, 0.f, 0.f};
    f32x4 lo[2] = {zero, zero}, hi[2] = {zero, zero};
#define QSTEP(i) do {                                                            \
      const float nv = rbf(asf((xr[i] << sh) & 0xFFFF0000u) - mb) / sd;          \
      const float xd = asf(pk2(nv, nv));                                         \
      const unsigned d[4] = {asu(dpp<0x00>(xd)), asu(dpp<0x55>(xd)), asu(dpp<0xAA>(xd)), asu(dpp<0xFF>(xd))}; \
      _Pragma("unroll")                                                          \
      for (int c = 0; c < 4; ++c) {                                              \
        const v2u a = {d[c] & m0, d[c] & m1};                                    \
        lo[c & 1] = mfma4(a, wa[4 * (i) + c].xy, lo[c & 1]);                     \
        hi[c & 1] = mfma4(a, wa[4 * (i) + c].zw, hi[c & 1]);                     \
      }                                                                          \
    } while (0)
#pragma unroll
    for (int i = 0; i < 10 - QNB; ++i) QSTEP(i);
    if (!r) {
#pragma unroll
      for (int i = 10 - QNB; i < QNB; ++i) QSTEP(i);
    }
    lo[0] += lo[1];
    hi[0] += hi[1];
    float u0 = fold16(fold32(lo[0][0], hi[0][0]), fold32(lo[0][2], hi[0][2]));
    float u1 = fold16(fold32(lo[0][1], hi[0][1]), fold32(lo[0][3], hi[0][3]));
    u0 += dpp<0x128>(u0);
    u1 += dpp<0x128>(u1);
    asm volatile("; KVW" : "+v"(epoch), "+v"(u0), "+v"(u1));
    const unsigned tag = (epoch + 1u) | 0x80000000u;
    {
      const f32x2 uu = {u0, u1};
      *(f32x2*)(red3 + w * 128 + lane * 2) = uu;
      CBAR();
      unsigned old = 0u;
      if (lane == 0) old = __hip_atomic_fetch_add(&flg[4], 1u, __ATOMIC_RELAXED, __HIP_MEMORY_SCOPE_WORKGROUP);
      old = __builtin_amdgcn_readfirstlane(old);
      CBAR();
      if (old == 1u) {
        const f32x2 t = *(const f32x2*)(red3 + lane * 2) + *(const f32x2*)(red3 + 128 + lane * 2);
        const v4u g = {asu(t.x), tag, asu(t.y), tag};
        __builtin_amdgcn_raw_buffer_store_b128(g, rsWs, p0_off, 0, AUX_SC1);
      }
    }
    STAMP(1);
    __builtin_amdgcn_s_setprio(KPR);
    SCHED_FENCE();
    ALD3(kv[0], rsK, 0);
    ALD3(kv[1], rsK, 1);
  } else {
    if ((KPM >> w) & 1) __builtin_amdgcn_s_setprio(1);
    ALD3(kv[0], rsK, 0);
    ALD3(kv[1], rsK, 1);
    SCHED_FENCE();
    __syncthreads();
  }
  const bool pub = w >= 4;
  const unsigned short* pl = pub ? vc + ((size_t)h * 32768 + 28672 + 512 * s + 256) * 192 + 64 * (w - 4) : vc + kvoff;
  unsigned nl = pub ? 255u * 384u + 128u : 64u * 384u;
  int so[NL];
#pragma unroll
  for (int t = 0; t < NL; ++t) {
    so[t] = pub ? (64 * (t / NLG) + 8 * (GLG + t % NLG)) * 384 : (GLG + t / 3) * 3072 + (t % 3) * 128;
    asm volatile("" : "+s"(so[t]));
  }
  asm volatile("" : "+s"(pl), "+s"(nl));
  v4u rsL = srd(pl, nl);
  asm volatile("" : "+s"(rsL));
  SCHED_FENCE();
#pragma unroll
  for (int g = 2; g < 8; ++g) ALD3(kv[g], rsK, g);
  SCHED_FENCE();
  auto qgather = [&](unsigned tag) __attribute__((always_inline)) {
    const unsigned ga = WS_P0 + (unsigned)(96 * h + lane) * 16u;
    const unsigned gb = lane < 32 ? ga + 1024u : lane < 56 ? WS_P0 + (unsigned)(3072 + 24 * h + lane - 32) * 16u : OOB;
    v4u g[4];
#pragma unroll 1
    for (unsigned spins = 0; spins < SPIN_CAP; ++spins) {
      asm volatile("" ::: "memory");
      g[0] = __builtin_amdgcn_raw_buffer_load_b128(rsWs, ga, 0, AUX_SC1);
      g[1] = __builtin_amdgcn_raw_buffer_load_b128(rsWs, ga, 61440, AUX_SC1);
      g[2] = __builtin_amdgcn_raw_buffer_load_b128(rsWs, gb, 0, AUX_SC1);
      g[3] = __builtin_amdgcn_raw_buffer_load_b128(rsWs, gb, 61440, AUX_SC1);
      const bool ok0 = (g[0].y == tag) & (g[0].w == tag) & (g[1].y == tag) & (g[1].w == tag);
      const bool ok1 = (g[2].y == tag) & (g[2].w == tag) & (g[3].y == tag) & (g[3].w == tag);
      if (__all(ok0 & (ok1 | (lane >= 56)))) break;
      __builtin_amdgcn_s_sleep(1);
    }
    qs[lane] = pk2(asf(g[0].x) + asf(g[1].x), asf(g[0].z) + asf(g[1].z));
    float t0 = asf(g[2].x) + asf(g[3].x), t1 = asf(g[2].z) + asf(g[3].z);
    if (lane >= 32) { t0 = fmaxf(t0, 0.f); t1 = fmaxf(t1, 0.f); }
    qs[64 + lane] = pk2(t0, t1);
    COUNTF(0);
    STAMP(2);
  };
  __builtin_amdgcn_s_setprio(0);
  if (QE && w == 2) {
    asm volatile("s_waitcnt vmcnt(24) ; KVW" : "+v"(epoch) :: "memory");
    qgather((epoch + 1u) | 0x80000000u);
  }
#pragma unroll
  for (int g = 0; g < GLG; ++g) asm volatile("" : "=v"(vv[g][0]), "=v"(vv[g][1]), "=v"(vv[g][2]));
  const int es = w < 4 ? GLG : w == 4 ? ES4 : w == 5 ? ES5 : w == 6 ? ES6 : ES7;
#pragma unroll
  for (int g = 0; g < GLG; ++g)
    if (g < es) ALDV(vv[g], rsV, g);
  v4u vx[NL];
#pragma unroll
  for (int t = 0; t < NL; ++t) asm volatile("" : "=v"(vx[t]));
  if (w < 4) {
#pragma unroll
    for (int t = 0; t < 3 * NLG; ++t) ALDX(vx[t], vkv, rsL, so[t]);
    COUNTF(2);
  }
  STAMP(4);
  asm volatile("s_waitcnt vmcnt(24) ; KVW" : "+v"(epoch) :: "memory");
  const unsigned tag = (epoch + 1u) | 0x80000000u;
  unsigned pd[8];
#define SCORES(LATE) do {                                                                                 \
    WAITF(0, 1u, 1);                                                                                      \
    v4u qf[3];                                                                                            \
    _Pragma("unroll")                                                                                     \
    for (int j = 0; j < 3; ++j) qf[j] = *(const v4u*)(qs + 32 * j + 4 * lc);                              \
    float sc[8];                                                                                          \
    _Pragma("unroll")                                                                                     \
    for (int g = 0; g < 8; ++g) {                                                                         \
      float a = kz;                                                                                       \
      _Pragma("unroll")                                                                                   \
      for (int j = 0; j < 3; ++j) {                                                                       \
        a = dot2(kv[g][j].x, qf[j].x, a);                                                                 \
        a = dot2(kv[g][j].y, qf[j].y, a);                                                                 \
        a = dot2(kv[g][j].z, qf[j].z, a);                                                                 \
        a = dot2(kv[g][j].w, qf[j].w, a);                                                                 \
      }                                                                                                   \
      a = sum8(a);                                                                                        \
      sc[g] = rbf(rbf(a) * 0.07216878364870322f);                                                         \
    }                                                                                                     \
    SCHED_FENCE();                                                                                        \
    if (w >= 4 && lc == 0) {                                                                              \
      _Pragma("unroll")                                                                                   \
      for (int g = GLG; g < 8; ++g) lsc[lg * NL + (w - 4) * NLG + g - GLG] = sc[g];                       \
    }                                                                                                     \
    float m = fmaxf(fmaxf(fmaxf(sc[0], sc[1]), fmaxf(sc[2], sc[3])), fmaxf(fmaxf(sc[4], sc[5]), fmaxf(sc[6], sc[7]))); \
    m = max_hi(m);                                                                                        \
    float l = 0.f;                                                                                        \
    _Pragma("unroll")                                                                                     \
    for (int g = 0; g < 8; ++g) {                                                                         \
      const float p = __builtin_amdgcn_exp2f((sc[g] - m) * 1.4426950408889634f);                          \
      l += p;                                                                                             \
      pd[g] = pk2(p, p);                                                                                  \
    }                                                                                                     \
    l = sum_hi(l);                                                                                        \
    if (lane == 0) { mls[w] = m; mls[8 + w] = l; }                                                        \
    COUNTF(8);                                                                                            \
    STAMP(3);                                                                                             \
    SCHED_FENCE();                                                                                        \
  } while (0)
  f32x4 oacc[3] = {{0.f, 0.f, 0.f, 0.f}, {0.f, 0.f, 0.f, 0.f}, {0.f, 0.f, 0.f, 0.f}};
  const int dpos = (rq >> 1) * 192 + 8 * (col & 7) + 4 * (rq & 1);
  const unsigned yz_off = col < 10 ? (unsigned)((s * 32 + h) * 640 + (4 * col + 2 * (rq & 1) + (rq >> 1)) * 16) : OOB;
  auto mljob = [&]() __attribute__((always_inline)) {
    const unsigned gm = lane < 8 ? WS_P1 + (unsigned)((h * 8 + lane) * P1S + 1536) : OOB;
    v4u g;
#pragma unroll 1
    for (unsigned spins = 0; spins < SPIN_CAP; ++spins) {
      CBAR();
      g = __builtin_amdgcn_raw_buffer_load_b128(rsWs, gm, 0, AUX_SC1);
      if (__all(((g.y == tag) & (g.w == tag)) | (lane >= 8))) break;
      __builtin_amdgcn_s_sleep(MLS);
    }
    const float Mh = max8(asf(g.x));
    const float eh = __builtin_amdgcn_exp2f((asf(g.x) - Mh) * 1.4426950408889634f);
    const float f = eh * __builtin_amdgcn_rcpf(sum8(eh * asf(g.z)));
    if (lane < 8) fac[lane] = f;
    COUNTF(1);
    STAMP(2);
  };
  auto zjob = [&](unsigned tg) __attribute__((always_inline)) {
    const rsrc_t rsW2 = mk(w2 + (size_t)(48 * h) * 640 + 80 * s, 47 * 1280 + 160);
    const unsigned voz = col < 10 ? (unsigned)(rq * 15360 + col * 16) : OOB;
    if (GZ) gate(tg, GZ);
    SCHED_FENCE();
    v4u gz[12];
#pragma unroll
    for (int g = 0; g < 12; ++g) gz[g] = __builtin_amdgcn_raw_buffer_load_b128(rsW2, voz, g * 1280, AUX_NT);
    if (MLW == 1 && ZW == 7) { SCHED_FENCE(); mljob(); SCHED_FENCE(); }
    const v2u* hb = (const v2u*)(qs + 96 + 6 * rq);
    const v2u h0 = hb[0], h1 = hb[1], h2 = hb[2];
    const unsigned hv[6] = {h0.x, h0.y, h1.x, h1.y, h2.x, h2.y};
    f32x4 za = {0.f, 0.f, 0.f, 0.f}, zb = {0.f, 0.f, 0.f, 0.f};
#pragma unroll
    for (int i = 0; i < 6; ++i) {
      za = mfma(bfrag(__builtin_amdgcn_perm(hv[i], hv[i], 0x01000100u), mask), gz[2 * i], za);
      zb = mfma(bfrag(__builtin_amdgcn_perm(hv[i], hv[i], 0x03020302u), mask), gz[2 * i + 1], zb);
    }
    za += zb;
    const v4u g = {asu(rq < 2 ? za[0] : za[2]), tag, asu(rq < 2 ? za[1] : za[3]), tag};
    __builtin_amdgcn_raw_buffer_store_b128(g, rsWs, yz_off + WS_Z, 0, AUX_SC1);
    asm volatile("s_waitcnt vmcnt(0)" ::: "memory");
    SCHED_FENCE();
    STAMP(14);
    };
  if (!QE && w == 2) qgather(tag);
  float kz = 0.f;
  if (w < 4) AW1(24, kz);
  else if (w == 4) AW1(3 * ES4, kz);
  else if (w == 5) AW1(3 * ES5, kz);
  else if (w == 6) AW1(3 * ES6, kz);
  else AW1(3 * ES7, kz);
  SCORES(true);
  if (w < 4) {
    PV(0, GLG, 3 * NLG);
#pragma unroll
    for (int g = GLG; g < 8; ++g) {
      v4u bf = bfrag(pd[g], mask);
      AW1(3 * (7 - g), bf);
#pragma unroll
      for (int j = 0; j < 3; ++j) oacc[j] = mfma(bf, vx[3 * (g - GLG) + j], oacc[j]);
      SCHED_FENCE();
    }
    PV_OUT();
    const unsigned vo3 = col < 10 ? (unsigned)(rq * 5120 + col * 16) : OOB;
    const v4u srdWo = srd(wo + (size_t)(192 * h + 48 * w) * 640 + 80 * s, 47 * 1280 + 160);
    v4u gv[12];
    LOAD_WO(0, WEA);
    const unsigned tg = tz;
    if (ZW < 4 && w == ZW) zjob(tg);
    {
      const int n0 = 80 * sr + 4 * h + 2 * (lane >> 5);
      unsigned xin = 0u;
      if (w == 0) {
        xin = __builtin_amdgcn_raw_buffer_load_b32(rsX, (unsigned)n0 * 2u, 0, 0);
        asm volatile("" : "+v"(xin));
      }
      if (WEA < 12) {
        WAITF(2, 8u, 1);
        if (GWE) gate(tg, GWE);
        CBAR();
        LOAD_WO(WEA, 12);
      }
      STAMP(1);
      const int pw = col & 7, j8 = 2 * rq + (col >> 3);
      unsigned mk4[4];
#pragma unroll
      for (int i = 0; i < 4; ++i) {
        mk4[i] = mask[i];
        asm volatile("" : "+v"(mk4[i]));
      }
      typedef __attribute__((address_space(3))) void* lds_t;
      unsigned l3r = (unsigned)(size_t)(lds_t)(red3 + lane * 2), l3w = l3r + 512u * w, l3f = (unsigned)(size_t)(lds_t)(flg + 5);
      unsigned yoff = yz_off + WS_Y;
      asm volatile("" : "+v"(l3r), "+v"(l3w), "+v"(l3f), "+v"(yoff));
      unsigned go = pw == s ? OOB : WS_P1 + (unsigned)((h * 8 + pw) * P1S + (24 * w + j8) * 16);
      unsigned gm = pw == s ? OOB : WS_P1 + (unsigned)((h * 8 + pw) * P1S + 1536);
      unsigned long long pd = ~(0x0101010101010101ull << s);
      unsigned cnt = SPIN_CAP, t1, t2, t3;
      v4u gp[4], gh[4];
      unsigned sc;
      {
        int ts = GPL;
#pragma unroll 1
        for (unsigned spins = 0; spins < (SPIN_CAP << 6); ++spins) {
          const int t = (int)((unsigned)wall_clock64() - tz);
          if (t >= ts) break;
          if (__hip_atomic_load(&flg[7], __ATOMIC_RELAXED, __HIP_MEMORY_SCOPE_WORKGROUP) >= 3u) ts = min(ts, t + PST);
          __builtin_amdgcn_s_sleep(1);
        }
      }
      CBAR();
      STAMP(15);
      P1_ISSUE(gp);
      unsigned fv[8] = {0u, 0u, 0u, 0u, 0u, 0u, 0u, 0u};
      auto fill = [&]() __attribute__((always_inline)) {
#pragma unroll
        for (int i = 0; i < 3; ++i) {
          const v2u t = *(const v2u*)(own + 2 * (24 * w + j8 + 8 * i));
          fv[2 * i] = pw == s ? t.x : fv[2 * i];
          fv[2 * i + 1] = pw == s ? t.y : fv[2 * i + 1];
        }
        const float m = mls[lc], l = mls[8 + lc];
        const float Mo = max8(m);
        const float Lo = sum8(__builtin_amdgcn_exp2f((m - Mo) * 1.4426950408889634f) * l);
        fv[6] = pw == s ? asu(Mo) : fv[6];
        fv[7] = pw == s ? asu(Lo) : fv[7];
      };
      const bool have = __builtin_amdgcn_readfirstlane(__hip_atomic_load(&flg[7], __ATOMIC_RELAXED, __HIP_MEMORY_SCOPE_WORKGROUP)) >= 3u;
      CBAR();
      if (have) fill();
      P1_LOOP(gp, gh);
      READY_WO();
      if (!have) {
        WAITF(7, 3u, 1);
        fill();
      }
      STAMP(5);
      const float Mh = max8(asf(fv[6]));
      const float eh = __builtin_amdgcn_exp2f((asf(fv[6]) - Mh) * 1.4426950408889634f);
      const float f = eh * __builtin_amdgcn_rcpf(sum8(eh * asf(fv[7])));
      f32x4 ya = {0.f, 0.f, 0.f, 0.f}, yb = {0.f, 0.f, 0.f, 0.f};
      float nn[6];
#pragma unroll
      for (int i = 0; i < 6; ++i) {
        nn[i] = asf(fv[i]) * f;
        asm volatile("" : "+v"(nn[i]));
      }
      SCHED_FENCE();
#pragma unroll
      for (int i = 0; i < 6; ++i) nn[i] += dpp<0xB1>(nn[i]);
      SCHED_FENCE();
#pragma unroll
      for (int i = 0; i < 6; ++i) nn[i] += dpp<0x4E>(nn[i]);
      SCHED_FENCE();
#pragma unroll
      for (int i = 0; i < 6; ++i) nn[i] += dpp<0x141>(nn[i]);
      SCHED_FENCE();
#pragma unroll
      for (int i = 0; i < 6; ++i) {
        const v4u a = bfrag(pk2(nn[i], nn[i]), mk4);
        ya = mfma(a, gv[i], ya);
        yb = mfma(a, gv[6 + i], yb);
      }
      if (rq >= 2) ya = yb;
      {
        const f32x2 r = {fold32(ya[0], ya[2]), fold32(ya[1], ya[3])};
        STAMP(6);
        f32x4 p0, p1;
        unsigned old;
        asm volatile("ds_write_b64 %[aw], %[r]\n"
                     "s_mov_b64 exec, 1\n"
                     "ds_add_rtn_u32 %[old], %[af], %[one]\n"
                     "s_mov_b64 exec, -1\n"
                     "ds_read2st64_b64 %[p0], %[ar] offset1:1\n"
                     "ds_read2st64_b64 %[p1], %[ar] offset0:2 offset1:3\n"
                     "s_waitcnt lgkmcnt(0)\n"
                     : [p0] "=&v"(p0), [p1] "=&v"(p1), [old] "=&v"(old)
                     : [aw] "v"(l3w), [ar] "v"(l3r), [af] "v"(l3f), [one] "v"(1u), [r] "v"(r) : "memory");
        const f32x2 t = (p0.xy + p0.zw) + (p1.xy + p1.zw);
        const v4u g = {asu(t.x), tag, asu(t.y), tag};
        if (__builtin_amdgcn_readfirstlane(old) == 3u) {
          __builtin_amdgcn_raw_buffer_store_b128(g, rsWs, yoff, 0, AUX_SC1);
          COUNTF(6);
          STAMP(7);
        }
      }
      if (w == 0 && h < 20) {
        const unsigned gbase = (unsigned)((sr * 32 + (lane & 31)) * 640 + (2 * h + (lane >> 5)) * 16);
        WAITF(6, 1u, 1);
        if (GP2) gate(tg, GP2);
        STAMP(13);
        unsigned y0 = 0u, y1 = 0u, go = gbase, cnt = SPIN_CAP, sc, t0;
        unsigned long long pend = ~0ull;
        v4u gq[5] = {gp[0], gp[1], gp[2], gp[3], gp[0]};
        P2_LOOP(gq);
        STAMP(8);
        v4u gzz = gq[4];
#pragma unroll 1
        for (unsigned spins = 0; spins < SPIN_CAP; ++spins) {
          if (__all((gzz.y == tag) & (gzz.w == tag))) break;
          CBAR();
          gzz = __builtin_amdgcn_raw_buffer_load_b128(rsWs, gbase, WS_Z, AUX_SC1);
          asm volatile("" : "+v"(gzz));
        }
        const float z0 = rbf(sum32(asf(gzz.x))), z1 = rbf(sum32(asf(gzz.z)));
        const float a0 = sum32(asf(y0)), a1 = sum32(asf(y1));
        if ((lane & 31) == 0) {
          const float r0 = rbf(rbf(bflo(xin) + rbf(a0)) + z0);
          const float r1 = rbf(rbf(bfhi(xin) + rbf(a1)) + z1);
          *(unsigned*)(out + n0) = pk2(r0, r1);
        }
        if (wg == 0 && lane == 0) __builtin_amdgcn_raw_buffer_store_b32(epoch + 1u, rsWs, 0, 0, AUX_SC1);
        gp[0] = gq[0]; gp[1] = gq[1]; gp[2] = gq[2]; gp[3] = gq[3];
      }
      P1_DRAIN(gp, gh);
    }
  } else {
#pragma unroll
    for (int g = 0; g < GLG; ++g)
      if (g >= es) ALDV(vv[g], rsV, g);
    STAMP(9);
    const int u = w - 4;
    const int pair = 32 * u + 4 * col + 2 * (rq & 1) + (rq >> 1);
    unsigned po = col < 8 ? WS_P1 + (unsigned)(wg * P1S + pair * 16) : OOB;
    if (w == 4 && lane == 8) po = WS_P1 + (unsigned)(wg * P1S + 1536);
    f32x4 e0, e1;
    float M, L;
    unsigned pl[NL];
    if (w < 7) {
      gate(tz, GL);
      __builtin_amdgcn_s_setprio(PPR);
#pragma unroll
      for (int t = 0; t < NL; ++t) ALDX(vx[t], vkv, rsL, so[t]);
      COUNTF(2);
      STAMP(1);
      WAITF(8, 8u, 1);
      e0 = *(const f32x4*)(mls);
      e1 = *(const f32x4*)(mls + 4);
      M = fmaxf(fmaxf(fmaxf(e0[0], e0[1]), fmaxf(e0[2], e0[3])), fmaxf(fmaxf(e1[0], e1[1]), fmaxf(e1[2], e1[3])));
#pragma unroll
      for (int t = 0; t < NL; t += 4) {
        const f32x4 v = *(const f32x4*)(lsc + lg * NL + t);
#pragma unroll
        for (int i = 0; i < 4; ++i) {
          const float p = __builtin_amdgcn_exp2f((v[i] - M) * 1.4426950408889634f);
          pl[t + i] = pk2(p, p);
        }
      }
#pragma unroll
      for (int i = 0; i < 4; ++i) {
        e0[i] = __builtin_amdgcn_exp2f((e0[i] - M) * 1.4426950408889634f);
        e1[i] = __builtin_amdgcn_exp2f((e1[i] - M) * 1.4426950408889634f);
      }
#pragma unroll
      for (int t = 0; t < NL; ++t) asm volatile("" : "+v"(pl[t]));
      L = sum8(__builtin_amdgcn_exp2f((mls[lc] - M) * 1.4426950408889634f) * mls[8 + lc]);
      asm volatile("" : "+v"(e0), "+v"(e1), "+v"(M), "+v"(L), "+v"(po));
      SCHED_FENCE();
    } else {
      COUNTF(2);
      asm volatile("s_waitcnt vmcnt(0)" : "=v"(e0), "=v"(e1), "=v"(M), "=v"(L) :: "memory");
#pragma unroll
      for (int t = 0; t < NL; ++t) asm volatile("" : "=v"(pl[t]));
    }
    PV(0, GLG, NL);
    PV_OUT();
    if (w < 7) {
      WAITF(3, 8u, 1);
      f32x4 ca = {0.f, 0.f, 0.f, 0.f}, cb = {0.f, 0.f, 0.f, 0.f};
  #pragma unroll
      for (int i = 0; i < 4; ++i) {
        ca += e0[i] * *(const f32x4*)(red + i * RS + dpos + 64 * u);
        cb += e1[i] * *(const f32x4*)(red + (4 + i) * RS + dpos + 64 * u);
      }
      STAMP(13);
      SCHED_FENCE();
#pragma unroll
      for (int t = 0; t < NL; t += 2) {
        v4u b0 = bfrag(pl[t], mask), b1 = bfrag(pl[t + 1], mask);
        AW2(NL - 2 - t, b0, b1);
        ca = mfma(b0, vx[t], ca);
        cb = mfma(b1, vx[t + 1], cb);
        SCHED_FENCE();
      }
      ca += cb;
  #pragma unroll
      for (int i = 0; i < 4; ++i)
        ca[i] = asf(__builtin_amdgcn_update_dpp(asu(ca[i]), asu(ca[i]), 0x128, 0xC, 0xF, false));
      f32x2 r = {fold32(ca[0], ca[2]), fold32(ca[1], ca[3])};
      if (col >= 8) { r.x = M; r.y = L; }
      const v4u ga = {asu(r.x), tag, asu(r.y), tag};
      __builtin_amdgcn_raw_buffer_store_b128(ga, rsWs, po, 0, AUX_SC1);
      if (col < 8) *(f32x2*)(own + 2 * pair) = r;
      COUNTF(7);
      STAMP(12);
    } else if (ZW == 7) {
      zjob(tz);
    }
  }
}

}

extern "C" void kernel_launch(void* const* d_in, const int* in_sizes, int n_in, void* d_out, int out_size, void* d_ws,
                              size_t ws_size, hipStream_t stream) {
  (void)in_sizes; (void)n_in; (void)out_size;
  if (ws_size < WS_BYTES) return;
  fused_decode_kernel<<<dim3(256), dim3(512), 0, stream>>>(
      (const unsigned short*)d_in[0], (const unsigned short*)d_in[1], (const unsigned short*)d_in[2],
      (const unsigned short*)d_in[3], (const unsigned short*)d_in[4], (const unsigned short*)d_in[6],
      (const unsigned short*)d_in[7], (unsigned short*)d_out, (unsigned char*)d_ws);
}
```
